```python
import torch

def _softmax(x, axis=-1):
    return torch.softmax(x, dim=axis)
def _gelu(x):
    return torch.nn.functional.gelu(x, approximate="tanh")
def _silu(x):
    return torch.nn.functional.silu(x)
def _sigmoid(x):
    return torch.sigmoid(x)
def _layernorm(x, axis=-1, eps=1e-5):
    m = x.mean(dim=axis, keepdim=True)
    v = x.var(dim=axis, keepdim=True, unbiased=False)
    return (x - m) / torch.sqrt(v + eps)
def _scatter_add(tgt, idx, src, axis=0):
    return tgt.clone().index_add_(axis, idx, src)
def _segment_sum(src, seg, n):
    out = torch.zeros((n,) + src.shape[1:], dtype=src.dtype)
    return out.index_add_(0, seg, src)
def _pad(x, pw):
    flat = tuple(v for pair in reversed(pw) for v in pair)
    return torch.nn.functional.pad(x, flat)

kernel_name = 'recurrent_scan__gqa_block_cached__glu_gated_reglu__decode'

def setup_inputs():

    _w = (lambda a: (lambda t: t.to(torch.bfloat16) if t.is_floating_point() else t)(torch.as_tensor(a)))
    def _A_setup():
        B, Sq, D = 8, 1, 512
        Hq, Hkv, Sk, Dh = 32, 8, 8192, 128
        assert Hq % Hkv == 0, (Hq, Hkv)
        return {
            "x": _w((lambda *s: torch.randn(*s, dtype=torch.bfloat16) * 0.2)(B, Sq, D)),
            "k_cache": _w((lambda *s: torch.randn(*s, dtype=torch.bfloat16) * 1.6)(B, Hkv, Sk, Dh)),
            "v_cache": _w((lambda *s: torch.randn(*s, dtype=torch.bfloat16) * 1.6)(B, Hkv, Sk, Dh)),
            "wq": _w((lambda *s: torch.randn(*s, dtype=torch.bfloat16) * 1.6)(D, Hq * Dh) * (D ** -0.5)),
            "wo": _w((lambda *s: torch.randn(*s, dtype=torch.bfloat16) * 1.6)(Hq * Dh, D) * ((Hq * Dh) ** -0.5)),
        }
    _A_in = _A_setup()

    def _B_setup():
        B, S, D, F = 8, 1, 512, 1792
        return {
            "x": _w((lambda *s: torch.randn(*s, dtype=torch.bfloat16) * 0.2)(B, S, D)),
            "W_gate": _w((lambda *s: torch.randn(*s, dtype=torch.bfloat16) * 0.8)(D, F) * (2.0 / D) ** 0.5),
            "W_up": _w((lambda *s: torch.randn(*s, dtype=torch.bfloat16) * 0.8)(D, F) * (2.0 / D) ** 0.5),
            "W_down": _w((lambda *s: torch.randn(*s, dtype=torch.bfloat16) * 0.8)(F, D) * (1.0 / F) ** 0.5),
        }
    _B_in = _B_setup()

    return {
        "A_x": _A_in["x"],
        "A_k_cache": _A_in["k_cache"],
        "A_v_cache": _A_in["v_cache"],
        "A_wq": _A_in["wq"],
        "A_wo": _A_in["wo"],
        "B_x": _B_in["x"],
        "B_W_gate": _B_in["W_gate"],
        "B_W_up": _B_in["W_up"],
        "B_W_down": _B_in["W_down"],
    }

def reference(A_x, A_k_cache, A_v_cache, A_wq, A_wo, B_x, B_W_gate, B_W_up, B_W_down):
    def _A_ref(x, k_cache, v_cache, wq, wo):
        B, Sq, D = x.shape
        Hq, Dh = 32, 128
        Hkv = k_cache.shape[1]
        rep = Hq // Hkv
        q = (lambda x, *d: x.permute(*d))((x @ wq).reshape(B, Sq, Hq, Dh), 0, 2, 1, 3)
        k = (lambda x, n, axis: torch.repeat_interleave(x, n, dim=axis))(k_cache, rep, axis=1)
        v = (lambda x, n, axis: torch.repeat_interleave(x, n, dim=axis))(v_cache, rep, axis=1)
        scores = torch.einsum("bhqd,bhkd->bhqk", q, k) * (Dh ** -0.5)
        p = _softmax(scores, axis=-1)
        o = torch.einsum("bhqk,bhkd->bhqd", p, v)
        return (lambda x, *d: x.permute(*d))(o, 0, 2, 1, 3).reshape(B, Sq, Hq * Dh) @ wo

    def _B_ref(x, W_gate, W_up, W_down):
        gate = torch.einsum("bsd,df->bsf", x, W_gate)
        up = torch.einsum("bsd,df->bsf", x, W_up)
        gate = gate * (lambda x, dt: x.to(dt))(gate > 0, gate.dtype)
        h = gate * up
        return torch.einsum("bsf,fd->bsd", h, W_down)

    x = A_x
    xn = _layernorm(x, axis=-1)
    s = _A_ref(xn, A_k_cache, A_v_cache, A_wq, A_wo)
    g = _silu(_B_ref(xn, B_W_gate, B_W_up, B_W_down))
    return x + s * g
```

```cpp
#include <hip/hip_runtime.h>
#include <hip/hip_bf16.h>
#include <cstdint>

using bf16x2 = __attribute__((ext_vector_type(2))) __bf16;
using u32x4  = __attribute__((ext_vector_type(4))) unsigned;
using f32x4  = __attribute__((ext_vector_type(4))) float;

constexpr int B = 8, DM = 512, HQ = 32, HKV = 8, SK = 8192, DH = 128, FF = 1792;
constexpr int NSPLIT = 8;
constexpr int G = 4;

__device__ __forceinline__ float bf2f(unsigned short u) { return __builtin_bit_cast(float, (unsigned)u << 16); }
__device__ __forceinline__ unsigned short f2bf(float f) {
  unsigned u = __builtin_bit_cast(unsigned, f);
  return (unsigned short)((u + 0x7FFFu + ((u >> 16) & 1u)) >> 16);
}
__device__ __forceinline__ float rbf(float f) { return bf2f(f2bf(f)); }
__device__ __forceinline__ float dot2(unsigned k, unsigned q, float acc) {
  return __builtin_amdgcn_fdot2_f32_bf16(__builtin_bit_cast(bf16x2, k), __builtin_bit_cast(bf16x2, q), acc, false);
}
template <int CTRL> __device__ __forceinline__ float dpp(float x) {
  return __builtin_bit_cast(float, __builtin_amdgcn_mov_dpp(__builtin_bit_cast(int, x), CTRL, 0xf, 0xf, true));
}
constexpr int XOR1 = 0xB1, XOR2 = 0x4E, XOR7 = 0x141, XOR8 = 0x128, ROR4 = 0x124;
__device__ __forceinline__ float xrow16_max(float x) {
  auto s = __builtin_amdgcn_permlane16_swap(__float_as_uint(x), __float_as_uint(x), false, false);
  x = fmaxf(__uint_as_float(s[0]), __uint_as_float(s[1]));
  auto t = __builtin_amdgcn_permlane32_swap(__float_as_uint(x), __float_as_uint(x), false, false);
  return fmaxf(__uint_as_float(t[0]), __uint_as_float(t[1]));
}
__device__ __forceinline__ float xrow16_sum(float x) {
  auto s = __builtin_amdgcn_permlane16_swap(__float_as_uint(x), __float_as_uint(x), false, false);
  x = __uint_as_float(s[0]) + __uint_as_float(s[1]);
  auto t = __builtin_amdgcn_permlane32_swap(__float_as_uint(x), __float_as_uint(x), false, false);
  return __uint_as_float(t[0]) + __uint_as_float(t[1]);
}
__device__ __forceinline__ float wave_sum(float x) {
  x += dpp<XOR1>(x); x += dpp<XOR2>(x); x += dpp<XOR7>(x); x += dpp<XOR8>(x);
  return xrow16_sum(x);
}

constexpr size_t WS_Q = 0;
constexpr size_t WS_H = 65536;
constexpr size_t WS_PML = 131072;
constexpr size_t WS_PO = WS_PML + 65536;

__device__ __forceinline__ void ln_all(const unsigned short* x, float* xn  , int tid) {
  const int lane = tid & 63, wave = tid >> 6;
  for (int r = wave; r < B; r += 4) {
    u32x4 v = *reinterpret_cast<const u32x4*>(x + r * DM + lane * 8);
    float f[8];
#pragma unroll
    for (int k = 0; k < 4; ++k) { f[2 * k] = __builtin_bit_cast(float, v[k] << 16); f[2 * k + 1] = __builtin_bit_cast(float, v[k] & 0xffff0000u); }
    float s = 0.f;
#pragma unroll
    for (int k = 0; k < 8; ++k) s += f[k];
    s = wave_sum(s);
    const float mean = s * (1.f / DM);
    float q = 0.f;
#pragma unroll
    for (int k = 0; k < 8; ++k) { const float d = f[k] - mean; q += d * d; }
    q = wave_sum(q);
    const float var = rbf(q * (1.f / DM));
    const float mb = rbf(mean);
    const float sd = rbf(sqrtf(rbf(var + 1e-5f)));
#pragma unroll
    for (int k = 0; k < 8; ++k) xn[r * DM + lane * 8 + k] = rbf(rbf(f[k] - mb) / sd);
  }
}

__global__ void __launch_bounds__(256) k_pre(const unsigned short* x, const unsigned short* wq, const unsigned short* wg, const unsigned short* wu, unsigned char* ws) {
  __shared__ float xn[B * DM];
  __shared__ float red[2][32][8][64 + 1];
  const int tid = threadIdx.x;
  ln_all(x, xn, tid);
  __syncthreads();
  const int blk = blockIdx.x;
  const int cc = tid & 7, rg = tid >> 3;
  if (blk < 64) {
    float acc[8][8];
#pragma unroll
    for (int b = 0; b < 8; ++b)
#pragma unroll
      for (int k = 0; k < 8; ++k) acc[b][k] = 0.f;
    for (int r = 0; r < 16; ++r) {
      const int row = rg * 16 + r;
      u32x4 v = *reinterpret_cast<const u32x4*>(wq + (size_t)row * 4096 + blk * 64 + cc * 8);
      float f[8];
#pragma unroll
      for (int k = 0; k < 4; ++k) { f[2 * k] = __builtin_bit_cast(float, v[k] << 16); f[2 * k + 1] = __builtin_bit_cast(float, v[k] & 0xffff0000u); }
#pragma unroll
      for (int b = 0; b < 8; ++b) { const float xv = xn[b * DM + row];
#pragma unroll
        for (int k = 0; k < 8; ++k) acc[b][k] = fmaf(xv, f[k], acc[b][k]); }
    }
#pragma unroll
    for (int b = 0; b < 8; ++b)
#pragma unroll
      for (int k = 0; k < 8; ++k) red[0][rg][b][cc * 8 + k] = acc[b][k];
    __syncthreads();
    for (int o = tid; o < 512; o += 256) { const int b = o >> 6, c = o & 63; float s = 0.f;
      for (int g = 0; g < 32; ++g) s += red[0][g][b][c];
      reinterpret_cast<unsigned short*>(ws + WS_Q)[b * 4096 + blk * 64 + c] = f2bf(s); }
  } else {
    const int hb = blk - 64;
    float ag[8][8], au[8][8];
#pragma unroll
    for (int b = 0; b < 8; ++b)
#pragma unroll
      for (int k = 0; k < 8; ++k) { ag[b][k] = 0.f; au[b][k] = 0.f; }
    for (int r = 0; r < 16; ++r) {
      const int row = rg * 16 + r;
      u32x4 v = *reinterpret_cast<const u32x4*>(wg + (size_t)row * FF + hb * 64 + cc * 8);
      u32x4 w = *reinterpret_cast<const u32x4*>(wu + (size_t)row * FF + hb * 64 + cc * 8);
      float f[8], g[8];
#pragma unroll
      for (int k = 0; k < 4; ++k) { f[2 * k] = __builtin_bit_cast(float, v[k] << 16); f[2 * k + 1] = __builtin_bit_cast(float, v[k] & 0xffff0000u);
        g[2 * k] = __builtin_bit_cast(float, w[k] << 16); g[2 * k + 1] = __builtin_bit_cast(float, w[k] & 0xffff0000u); }
#pragma unroll
      for (int b = 0; b < 8; ++b) { const float xv = xn[b * DM + row];
#pragma unroll
        for (int k = 0; k < 8; ++k) { ag[b][k] = fmaf(xv, f[k], ag[b][k]); au[b][k] = fmaf(xv, g[k], au[b][k]); } }
    }
#pragma unroll
    for (int b = 0; b < 8; ++b)
#pragma unroll
      for (int k = 0; k < 8; ++k) { red[0][rg][b][cc * 8 + k] = ag[b][k]; red[1][rg][b][cc * 8 + k] = au[b][k]; }
    __syncthreads();
    for (int o = tid; o < 512; o += 256) { const int b = o >> 6, c = o & 63; float s = 0.f, t = 0.f;
      for (int g = 0; g < 32; ++g) { s += red[0][g][b][c]; t += red[1][g][b][c]; }
      s = rbf(s); t = rbf(t);
      const float h = s > 0.f ? rbf(s * t) : 0.f;
      reinterpret_cast<float*>(ws + WS_H)[b * FF + hb * 64 + c] = h; }
  }
}

constexpr int WAVES = 4, PAGE = 16, PPS = SK / PAGE / NSPLIT, PPW = PPS / WAVES;
constexpr int MRG_REC = 4 + DH;
__global__ void __launch_bounds__(256) k_attn(const unsigned short* kc, const unsigned short* vc, unsigned char* ws) {
  __shared__ __attribute__((aligned(16))) float lds[WAVES * 64 + WAVES * 4 * MRG_REC];
  const int s = blockIdx.x, kvh = blockIdx.y, b = blockIdx.z;
  const int tid = threadIdx.x, lane = tid & 63, wave = __builtin_amdgcn_readfirstlane(tid >> 6), r = lane >> 4, c = lane & 15;
  constexpr float C2 = 0.088388347648318440f * 1.4426950408889634f;
  const unsigned short* q = reinterpret_cast<const unsigned short*>(ws + WS_Q);
  u32x4 qpk[4];
#pragma unroll
  for (int g = 0; g < 4; ++g) qpk[g] = *reinterpret_cast<const u32x4*>(q + (size_t)b * 4096 + (kvh * 4 + g) * DH + 8 * c);
  const size_t base = ((size_t)(b * HKV + kvh) * SK + (size_t)s * PPS * PAGE) * DH + (size_t)r * DH + 8 * c;
  u32x4 Kn[4], Vn[4];
  float m = -INFINITY, l = 0.f, acc[32];
#pragma unroll
  for (int k = 0; k < 32; ++k) acc[k] = 0.f;
#define ISSUE(i_) do { const size_t o_ = base + (size_t)(wave + 4 * (i_)) * PAGE * DH; \
    _Pragma("unroll") for (int t_ = 0; t_ < 4; ++t_) { Kn[t_] = __builtin_nontemporal_load(reinterpret_cast<const u32x4*>(kc + o_ + t_ * 4 * DH)); Vn[t_] = __builtin_nontemporal_load(reinterpret_cast<const u32x4*>(vc + o_ + t_ * 4 * DH)); } } while (0)
  ISSUE(0);
  for (int i = 0; i < PPW; ++i) {
    u32x4 Kc[4], Vc[4];
#pragma unroll
    for (int t = 0; t < 4; ++t) { Kc[t] = Kn[t]; Vc[t] = Vn[t]; }
    if (i + 1 < PPW) ISSUE(i + 1);
    float x[16];
#pragma unroll
    for (int t = 0; t < 4; ++t)
#pragma unroll
      for (int g = 0; g < 4; ++g) { float d = 0.f;
#pragma unroll
        for (int k = 0; k < 4; ++k) d = dot2(Kc[t][k], qpk[g][k], d);
        x[t * 4 + g] = d; }
#define TR_STEP(HALF, CTRL, BIT) _Pragma("unroll") for (int h = 0; h < (HALF); ++h) { \
      const float lo_ = x[h], hi_ = x[h + (HALF)]; const float t1_ = lo_ + dpp<CTRL>(lo_), t2_ = hi_ + dpp<CTRL>(hi_); \
      x[h] = (c & (BIT)) ? t2_ : t1_; }
    TR_STEP(8, XOR8, 8) TR_STEP(4, XOR7, 4) TR_STEP(2, XOR2, 2) TR_STEP(1, XOR1, 1)
#undef TR_STEP
    const float sc = rbf(rbf(x[0]) * 0.088388347648318440f) * 1.4426950408889634f;
    float pm = sc; pm = fmaxf(pm, dpp<ROR4>(pm)); pm = fmaxf(pm, dpp<XOR8>(pm)); pm = xrow16_max(pm);
    const float mn = fmaxf(m, pm);
    const float alpha = __builtin_amdgcn_exp2f(m - mn), p = __builtin_amdgcn_exp2f(sc - mn);
    l = fmaf(l, alpha, p); m = mn;
#pragma unroll
    for (int q4 = 0; q4 < 4; ++q4) {
      const float al = q4 == 0 ? dpp<0x00>(alpha) : q4 == 1 ? dpp<0x55>(alpha) : q4 == 2 ? dpp<0xAA>(alpha) : dpp<0xFF>(alpha);
#pragma unroll
      for (int k = 0; k < 8; ++k) acc[q4 * 8 + k] *= al; }
    float* prow = lds + (wave * 4 + r) * 16;
    prow[c] = p;
    asm volatile("s_waitcnt lgkmcnt(0)" ::: "memory");
    f32x4 pr[4];
#pragma unroll
    for (int u = 0; u < 4; ++u) pr[u] = *reinterpret_cast<const f32x4*>(prow + 4 * u);
#pragma unroll
    for (int t = 0; t < 4; ++t) {
      float vf[8];
#pragma unroll
      for (int k = 0; k < 4; ++k) { vf[2 * k] = __builtin_bit_cast(float, Vc[t][k] << 16); vf[2 * k + 1] = __builtin_bit_cast(float, Vc[t][k] & 0xffff0000u); }
#pragma unroll
      for (int g = 0; g < 4; ++g) { const float pg_ = pr[t][g];
#pragma unroll
        for (int k = 0; k < 8; ++k) acc[g * 8 + k] = fmaf(pg_, vf[k], acc[g * 8 + k]); } }
  }
#undef ISSUE
#pragma unroll
  for (int k = 0; k < 32; ++k) acc[k] = xrow16_sum(acc[k]);
  { float t = l; t += dpp<ROR4>(t); t += dpp<XOR8>(t); l = xrow16_sum(t); }
  float* mrg = lds + WAVES * 64;
  if (r == 0) {
#pragma unroll
    for (int g = 0; g < 4; ++g) {
      float* rec = mrg + (wave * 4 + g) * MRG_REC;
#pragma unroll
      for (int k = 0; k < 8; k += 4) *reinterpret_cast<f32x4*>(rec + 4 + 8 * c + k) = f32x4{acc[g * 8 + k], acc[g * 8 + k + 1], acc[g * 8 + k + 2], acc[g * 8 + k + 3]};
      if (c == g) { rec[0] = m; rec[1] = l; } } }
  __syncthreads();
  float* pml = reinterpret_cast<float*>(ws + WS_PML);
  float* po = reinterpret_cast<float*>(ws + WS_PO);
  {
    const int g = wave, d0 = 2 * lane;
    const size_t row = (size_t)b * HQ + kvh * 4 + g;
    float M = -INFINITY;
#pragma unroll
    for (int w = 0; w < WAVES; ++w) M = fmaxf(M, mrg[(w * 4 + g) * MRG_REC]);
    float L = 0.f, o0 = 0.f, o1 = 0.f;
#pragma unroll
    for (int w = 0; w < WAVES; ++w) { const float* rec = mrg + (w * 4 + g) * MRG_REC; const float lw = rec[1];
      const float wt = __builtin_amdgcn_exp2f(rec[0] - M);
      L = fmaf(wt, lw, L); o0 = fmaf(wt, rec[4 + d0], o0); o1 = fmaf(wt, rec[4 + d0 + 1], o1); }
    const size_t slot = row * NSPLIT + s;
    *reinterpret_cast<float2*>(po + slot * DH + d0) = make_float2(o0, o1);
    if (lane == 0) *reinterpret_cast<float2*>(pml + 2 * slot) = make_float2(M, L);
  }
}

__global__ void __launch_bounds__(256) k_post(const unsigned short* x, const unsigned short* wo, const unsigned short* wd, const unsigned char* ws, unsigned short* out) {
  __shared__ float o[4096];
  __shared__ float h[FF];
  __shared__ float red[2][32][64 + 1];
  const int tid = threadIdx.x, b = blockIdx.y, cb = blockIdx.x;
  const float* pml = reinterpret_cast<const float*>(ws + WS_PML);
  const float* po = reinterpret_cast<const float*>(ws + WS_PO);
  for (int e = tid; e < 4096; e += 256) {
    const int hd = e >> 7, d = e & 127;
    const size_t row = (size_t)b * HQ + hd;
    float M = -INFINITY;
    for (int s = 0; s < NSPLIT; ++s) M = fmaxf(M, pml[(row * NSPLIT + s) * 2]);
    float L = 0.f, a = 0.f;
    for (int s = 0; s < NSPLIT; ++s) { const float w = __builtin_amdgcn_exp2f(pml[(row * NSPLIT + s) * 2] - M);
      L = fmaf(w, pml[(row * NSPLIT + s) * 2 + 1], L); a = fmaf(w, po[(row * NSPLIT + s) * DH + d], a); }
    o[e] = rbf(a / L);
  }
  for (int e = tid; e < FF; e += 256) h[e] = reinterpret_cast<const float*>(ws + WS_H)[b * FF + e];
  __syncthreads();
  const int cc = tid & 7, rg = tid >> 3;
  float as[8], ad[8];
#pragma unroll
  for (int k = 0; k < 8; ++k) { as[k] = 0.f; ad[k] = 0.f; }
  for (int r = rg; r < 4096; r += 32) {
    u32x4 v = *reinterpret_cast<const u32x4*>(wo + (size_t)r * DM + cb * 64 + cc * 8);
    const float xv = o[r];
#pragma unroll
    for (int k = 0; k < 4; ++k) { as[2 * k] = fmaf(xv, __builtin_bit_cast(float, v[k] << 16), as[2 * k]); as[2 * k + 1] = fmaf(xv, __builtin_bit_cast(float, v[k] & 0xffff0000u), as[2 * k + 1]); }
  }
  for (int r = rg; r < FF; r += 32) {
    u32x4 v = *reinterpret_cast<const u32x4*>(wd + (size_t)r * DM + cb * 64 + cc * 8);
    const float xv = h[r];
#pragma unroll
    for (int k = 0; k < 4; ++k) { ad[2 * k] = fmaf(xv, __builtin_bit_cast(float, v[k] << 16), ad[2 * k]); ad[2 * k + 1] = fmaf(xv, __builtin_bit_cast(float, v[k] & 0xffff0000u), ad[2 * k + 1]); }
  }
#pragma unroll
  for (int k = 0; k < 8; ++k) { red[0][rg][cc * 8 + k] = as[k]; red[1][rg][cc * 8 + k] = ad[k]; }
  __syncthreads();
  if (tid < 64) {
    float s = 0.f, d = 0.f;
    for (int g = 0; g < 32; ++g) { s += red[0][g][tid]; d += red[1][g][tid]; }
    s = rbf(s); d = rbf(d);
    const float gg = rbf(d / (1.f + __expf(-d)));
    const float xv = bf2f(x[b * DM + cb * 64 + tid]);
    out[b * DM + cb * 64 + tid] = f2bf(xv + rbf(s * gg));
  }
}

extern "C" void kernel_launch(void* const* d_in, const int* in_sizes, int n_in, void* d_out, int out_size, void* d_ws, size_t ws_size, hipStream_t stream) {
  const unsigned short* x = (const unsigned short*)d_in[0];
  const unsigned short* kc = (const unsigned short*)d_in[1];
  const unsigned short* vc = (const unsigned short*)d_in[2];
  const unsigned short* wq = (const unsigned short*)d_in[3];
  const unsigned short* wo = (const unsigned short*)d_in[4];
  const unsigned short* wg = (const unsigned short*)d_in[6];
  const unsigned short* wu = (const unsigned short*)d_in[7];
  const unsigned short* wd = (const unsigned short*)d_in[8];
  unsigned char* ws = (unsigned char*)d_ws;
  k_pre<<<dim3(64 + 28), dim3(256), 0, stream>>>(x, wq, wg, wu, ws);
  k_attn<<<dim3(NSPLIT, HKV, B), dim3(256), 0, stream>>>(kc, vc, ws);
  k_post<<<dim3(8, 8), dim3(256), 0, stream>>>(x, wo, wd, ws, (unsigned short*)d_out);
}
```

```cpp
#include <hip/hip_runtime.h>
#include <hip/hip_bf16.h>
#include <cstdint>

typedef __bf16 bf16x2 __attribute__((ext_vector_type(2)));
typedef __bf16 bf16x4 __attribute__((ext_vector_type(4)));
typedef __bf16 bf16x8 __attribute__((ext_vector_type(8)));
typedef unsigned u32x2 __attribute__((ext_vector_type(2)));
typedef unsigned u32x4 __attribute__((ext_vector_type(4)));
typedef float f32x2 __attribute__((ext_vector_type(2)));
typedef float f32x4 __attribute__((ext_vector_type(4)));
typedef unsigned long long u64;
#define LAS __attribute__((address_space(3)))
#define GAS __attribute__((address_space(1)))
#define RLX_AGENT __ATOMIC_RELAXED, __HIP_MEMORY_SCOPE_AGENT
#define RLX_WG __ATOMIC_RELAXED, __HIP_MEMORY_SCOPE_WORKGROUP

constexpr int NB = 8, DM = 512, HQ = 32, HKV = 8, SK = 8192, DH = 128, FF = 1792;
constexpr int NSPLIT = 4, NWG = 256, NTHR = 768;
constexpr int NT_TILES = SK / NSPLIT / 32 / 4;

#define THROTTLE 1

constexpr unsigned WS_EPOCH = 0;
constexpr unsigned WS_FLAG = 4096;
constexpr unsigned WS_ML = 8192;
constexpr unsigned WS_QG = 16384;
constexpr unsigned WS_HG = WS_QG + NB * 4096 * 8;
constexpr unsigned WS_PART = WS_HG + NB * FF * 8;
constexpr unsigned WS_END = WS_PART + NWG * 1024;
constexpr unsigned WS_STAMP = 1u << 20;

constexpr unsigned L_RING = 0;
constexpr unsigned L_STG = 131072;
constexpr unsigned L_XN = L_STG + 16384;
constexpr unsigned XN_STRIDE = 1040;
constexpr unsigned L_Q = L_XN + 8 * XN_STRIDE;
constexpr unsigned L_REDG = L_Q + 1024;
constexpr unsigned L_REDS = L_REDG + 512;
constexpr unsigned L_GX = L_REDS + 512;
constexpr unsigned L_CTL = L_GX + 128;
constexpr unsigned L_END = L_CTL + 64;
static_assert(L_END <= 163840, "LDS");
constexpr unsigned MRG_W = (512 + 8) * 4;
enum { C_LN = 0, C_STGFREE, C_QREADY, C_HDONE, C_MRG, C_G, C_S, C_ATTEND, C_TLO, C_THI, C_GDONE };

__device__ __forceinline__ float bf2f(unsigned short u) { return __builtin_bit_cast(float, (unsigned)u << 16); }
__device__ __forceinline__ unsigned cvtpk(float a, float b) {
  bf16x2 t = __builtin_convertvector(f32x2{a, b}, bf16x2);
  return __builtin_bit_cast(unsigned, t);
}
__device__ __forceinline__ float lo16(unsigned u) { return __builtin_bit_cast(float, u << 16); }
__device__ __forceinline__ float hi16(unsigned u) { return __builtin_bit_cast(float, u & 0xffff0000u); }
__device__ __forceinline__ float rbf(float f) { return lo16(cvtpk(f, f)); }
__device__ __forceinline__ float dot2(unsigned k, unsigned q, float acc) {
  return __builtin_amdgcn_fdot2_f32_bf16(__builtin_bit_cast(bf16x2, k), __builtin_bit_cast(bf16x2, q), acc, false);
}
template <int CTRL> __device__ __forceinline__ float dpp(float x) {
  return __builtin_bit_cast(float, __builtin_amdgcn_mov_dpp(__builtin_bit_cast(int, x), CTRL, 0xf, 0xf, true));
}
constexpr int XOR1 = 0xB1, XOR2 = 0x4E, XOR7 = 0x141, XOR8 = 0x128, ROR4 = 0x124;
__device__ __forceinline__ float xrow16_max(float x) {
  auto s = __builtin_amdgcn_permlane16_swap(__float_as_uint(x), __float_as_uint(x), false, false);
  x = fmaxf(__uint_as_float(s[0]), __uint_as_float(s[1]));
  auto t = __builtin_amdgcn_permlane32_swap(__float_as_uint(x), __float_as_uint(x), false, false);
  return fmaxf(__uint_as_float(t[0]), __uint_as_float(t[1]));
}
__device__ __forceinline__ float xrow16_sum(float x) {
  auto s = __builtin_amdgcn_permlane16_swap(__float_as_uint(x), __float_as_uint(x), false, false);
  x = __uint_as_float(s[0]) + __uint_as_float(s[1]);
  auto t = __builtin_amdgcn_permlane32_swap(__float_as_uint(x), __float_as_uint(x), false, false);
  return __uint_as_float(t[0]) + __uint_as_float(t[1]);
}
__device__ __forceinline__ float wave_sum(float x) {
  x += dpp<XOR1>(x); x += dpp<XOR2>(x); x += dpp<XOR7>(x); x += dpp<XOR8>(x);
  return xrow16_sum(x);
}

__device__ __forceinline__ void dma4(unsigned v0, unsigned v1, unsigned v2, unsigned v3, u32x4 rs, unsigned so, unsigned ldsaddr) {
  unsigned keep;
  asm volatile(
    "s_mov_b32 %0, m0\n\t"
    "s_mov_b32 m0, %7\n\t"
    "s_nop 0\n\t"
    "buffer_load_dwordx4 %1, %5, %6 offen nt lds\n\t"
    "buffer_load_dwordx4 %2, %5, %6 offen offset:1024 nt lds\n\t"
    "buffer_load_dwordx4 %3, %5, %6 offen offset:2048 nt lds\n\t"
    "buffer_load_dwordx4 %4, %5, %6 offen offset:3072 nt lds\n\t"
    "s_mov_b32 m0, %0"
    : "=&s"(keep) : "v"(v0), "v"(v1), "v"(v2), "v"(v3), "s"(rs), "s"(so), "s"(ldsaddr) : "memory");
}
__device__ __forceinline__ void dma1(unsigned v0, u32x4 rs, unsigned so, unsigned ldsaddr) {
  unsigned keep;
  asm volatile(
    "s_mov_b32 %0, m0\n\t"
    "s_mov_b32 m0, %4\n\t"
    "s_nop 0\n\t"
    "buffer_load_dwordx4 %1, %2, %3 offen nt lds\n\t"
    "s_mov_b32 m0, %0"
    : "=&s"(keep) : "v"(v0), "s"(rs), "s"(so), "s"(ldsaddr) : "memory");
}
__device__ __forceinline__ void gdma1(const void* g, unsigned ldsaddr) {
  unsigned keep;
  asm volatile(
    "s_mov_b32 %0, m0\n\t"
    "s_mov_b32 m0, %2\n\t"
    "s_nop 0\n\t"
    "global_load_lds_dwordx4 %1, off nt\n\t"
    "s_mov_b32 m0, %0"
    : "=&s"(keep) : "v"(g), "s"(ldsaddr) : "memory");
}
#define WAIT_VM(n) asm volatile("s_waitcnt vmcnt(" #n ")" ::: "memory")
#define WAIT_LGKM0() asm volatile("s_waitcnt lgkmcnt(0)" ::: "memory")

__device__ __forceinline__ unsigned lds_ld(LAS unsigned char* L, int idx) { return __hip_atomic_load((LAS unsigned*)(L + L_CTL) + idx, RLX_WG); }
__device__ __forceinline__ void lds_st(LAS unsigned char* L, int idx, unsigned v) { __hip_atomic_store((LAS unsigned*)(L + L_CTL) + idx, v, RLX_WG); }
__device__ __forceinline__ unsigned lds_add(LAS unsigned char* L, int idx) { return __hip_atomic_fetch_add((LAS unsigned*)(L + L_CTL) + idx, 1u, RLX_WG); }
__device__ __forceinline__ void lds_wait_ge(LAS unsigned char* L, int idx, unsigned want) {
  while ((unsigned)__builtin_amdgcn_readfirstlane(lds_ld(L, idx)) < want) __builtin_amdgcn_s_sleep(1);
  asm volatile("" ::: "memory");
}
constexpr unsigned SPIN_CAP = 1u << 17;

#define STAMP(ws, w, k) do {} while (0)

struct AttnState { f32x4 acc[8]; float m, l; };
struct AttnConst { u32x4 rsK, rsV; unsigned gbase, ring, vk0, vv0, kaddr, vaddr; bf16x8 qf[4]; };

#define ISSUE_K(c_, t, slot) do { const unsigned so_ = (c_).gbase + (unsigned)(t) * 32768u, ld_ = (c_).ring + (slot) * 16384; \
    dma4((c_).vk0, (c_).vk0 ^ 64, (c_).vk0 ^ 128, (c_).vk0 ^ 192, (c_).rsK, so_, ld_); dma4((c_).vk0, (c_).vk0 ^ 64, (c_).vk0 ^ 128, (c_).vk0 ^ 192, (c_).rsK, so_ + 4096, ld_ + 4096); } while (0)
#define ISSUE_V(c_, t, slot) do { const unsigned so_ = (c_).gbase + (unsigned)(t) * 32768u, ld_ = (c_).ring + (slot) * 16384 + 8192; \
    dma4((c_).vv0, (c_).vv0 ^ 128, (c_).vv0, (c_).vv0 ^ 128, (c_).rsV, so_, ld_); dma4((c_).vv0, (c_).vv0 ^ 128, (c_).vv0, (c_).vv0 ^ 128, (c_).rsV, so_ + 4096, ld_ + 4096); } while (0)

template <int SLOT, int MODE>
__device__ __forceinline__ void attn_tile(LAS unsigned char* L, const AttnConst& c, AttnState& st, const int t) {
  constexpr float SCALE = 0.088388347648318440f, LOG2E = 1.4426950408889634f;
  if (MODE < 2) WAIT_VM(24); else WAIT_VM(8);
  f32x4 s0 = {0.f, 0.f, 0.f, 0.f}, s1 = {0.f, 0.f, 0.f, 0.f};
#pragma unroll
  for (int s = 0; s < 4; ++s) {
    const bf16x8 k0 = *(LAS bf16x8*)(L + (c.kaddr ^ (s << 6)) + SLOT * 16384);
    const bf16x8 k1 = *(LAS bf16x8*)(L + (c.kaddr ^ (s << 6)) + SLOT * 16384 + 4096);
    s0 = __builtin_amdgcn_mfma_f32_16x16x32_bf16(k0, c.qf[s], s0, 0, 0, 0);
    s1 = __builtin_amdgcn_mfma_f32_16x16x32_bf16(k1, c.qf[s], s1, 0, 0, 0);
  }
  WAIT_LGKM0();
  if (MODE == 0) ISSUE_K(c, t + 2, SLOT);
  float x[8];
  { unsigned u;
    u = cvtpk(s0[0], s0[1]); u = cvtpk(lo16(u) * SCALE, hi16(u) * SCALE); x[0] = lo16(u); x[1] = hi16(u);
    u = cvtpk(s0[2], s0[3]); u = cvtpk(lo16(u) * SCALE, hi16(u) * SCALE); x[2] = lo16(u); x[3] = hi16(u);
    u = cvtpk(s1[0], s1[1]); u = cvtpk(lo16(u) * SCALE, hi16(u) * SCALE); x[4] = lo16(u); x[5] = hi16(u);
    u = cvtpk(s1[2], s1[3]); u = cvtpk(lo16(u) * SCALE, hi16(u) * SCALE); x[6] = lo16(u); x[7] = hi16(u); }
  float pm = fmaxf(fmaxf(fmaxf(x[0], x[1]), fmaxf(x[2], x[3])), fmaxf(fmaxf(x[4], x[5]), fmaxf(x[6], x[7])));
  pm = xrow16_max(pm);
  if (__any(pm > st.m)) {
    const float mn = fmaxf(st.m, pm);
    const float alpha = __builtin_amdgcn_exp2f((st.m - mn) * LOG2E);
    st.l *= alpha; st.m = mn;
#pragma unroll
    for (int d = 0; d < 8; ++d) st.acc[d] *= alpha;
  }
  const float mb = st.m * LOG2E;
  float p[8];
#pragma unroll
  for (int e = 0; e < 8; ++e) p[e] = __builtin_amdgcn_exp2f(fmaf(x[e], LOG2E, -mb));
  st.l += ((p[0] + p[1]) + (p[2] + p[3])) + ((p[4] + p[5]) + (p[6] + p[7]));
  const u32x4 pw = {cvtpk(p[0], p[1]), cvtpk(p[2], p[3]), cvtpk(p[4], p[5]), cvtpk(p[6], p[7])};
  const bf16x8 pf = __builtin_bit_cast(bf16x8, pw);
  if (MODE == 0) WAIT_VM(24); else if (MODE == 1) WAIT_VM(16); else WAIT_VM(0);
#pragma unroll
  for (int d = 0; d < 8; ++d) {
    const bf16x4 v0 = __builtin_amdgcn_ds_read_tr16_b64_v4bf16((LAS bf16x4*)(L + (c.vaddr ^ (d << 5)) + SLOT * 16384));
    const bf16x4 v1 = __builtin_amdgcn_ds_read_tr16_b64_v4bf16((LAS bf16x4*)(L + (c.vaddr ^ (d << 5)) + SLOT * 16384 + 4096));
    const bf16x8 vf = __builtin_shufflevector(v0, v1, 0, 1, 2, 3, 4, 5, 6, 7);
    st.acc[d] = __builtin_amdgcn_mfma_f32_16x16x32_bf16(vf, pf, st.acc[d], 0, 0, 0);
  }
  WAIT_LGKM0();
  if (MODE == 0) ISSUE_V(c, t + 2, SLOT);
}

__device__ __forceinline__ f32x4 proj16(LAS unsigned char* L, int lane) {
  const int g = lane >> 4, i = lane & 15;
  const unsigned aa = L_XN + (i & 7) * XN_STRIDE + g * 16;
  const unsigned ba = L_STG + g * 256 + i * 8;
  f32x4 d0 = {0.f, 0.f, 0.f, 0.f}, d1 = {0.f, 0.f, 0.f, 0.f};
#pragma unroll
  for (int s = 0; s < 16; s += 2) {
    const bf16x8 a0 = *(LAS bf16x8*)(L + aa + s * 64);
    const bf16x4 b0 = __builtin_amdgcn_ds_read_tr16_b64_v4bf16((LAS bf16x4*)(L + ba + s * 1024));
    const bf16x4 b1 = __builtin_amdgcn_ds_read_tr16_b64_v4bf16((LAS bf16x4*)(L + ba + s * 1024 + 128));
    const bf16x8 a1 = *(LAS bf16x8*)(L + aa + s * 64 + 64);
    const bf16x4 b2 = __builtin_amdgcn_ds_read_tr16_b64_v4bf16((LAS bf16x4*)(L + ba + s * 1024 + 1024));
    const bf16x4 b3 = __builtin_amdgcn_ds_read_tr16_b64_v4bf16((LAS bf16x4*)(L + ba + s * 1024 + 1024 + 128));
    d0 = __builtin_amdgcn_mfma_f32_16x16x32_bf16(a0, __builtin_shufflevector(b0, b1, 0, 1, 2, 3, 4, 5, 6, 7), d0, 0, 0, 0);
    d1 = __builtin_amdgcn_mfma_f32_16x16x32_bf16(a1, __builtin_shufflevector(b2, b3, 0, 1, 2, 3, 4, 5, 6, 7), d1, 0, 0, 0);
  }
  return d0 + d1;
}

__global__ void __launch_bounds__(NTHR, 3)
fused_decode(const unsigned short* __restrict__ x, const unsigned short* __restrict__ kc, const unsigned short* __restrict__ vc,
             const unsigned short* __restrict__ wq, const unsigned short* __restrict__ wo, const unsigned short* __restrict__ wg,
             const unsigned short* __restrict__ wu, const unsigned short* __restrict__ wd, unsigned short* __restrict__ out, unsigned char* ws) {
  extern __shared__ __attribute__((aligned(16))) unsigned char lds[];
  LAS unsigned char* L = (LAS unsigned char*)lds;
  const int w = blockIdx.x;
  const int tid = threadIdx.x, lane = tid & 63, wave = __builtin_amdgcn_readfirstlane(tid >> 6);
  const int g = lane >> 4, i = lane & 15;
  const int kvh = w & 7, b = (w >> 3) & 7, sp = w >> 6;
  const __amdgpu_buffer_rsrc_t rsW = __builtin_amdgcn_make_buffer_rsrc(ws, 0, WS_END, 0x00020000);

  if (wave < 4) {
    const int aw = wave;
    const u64 ka = (u64)kc, va = (u64)vc;
    AttnConst c;
    c.rsK = u32x4{(unsigned)ka, (unsigned)(ka >> 32), 128u << 20, 0x00020000u};
    c.rsV = u32x4{(unsigned)va, (unsigned)(va >> 32), 128u << 20, 0x00020000u};
    c.gbase = (unsigned)(((b * HKV + kvh) * SK + sp * (SK / NSPLIT)) * 256) + aw * 8192;
    c.ring = L_RING + aw * 32768;
    c.vk0 = g * 256 + ((i ^ g) << 4);
    c.vv0 = g * 256 + ((i ^ (2 * g)) << 4);
    c.kaddr = c.ring + i * 256 + ((i ^ g) << 4);
    const int q4 = i >> 2, pp = lane & 3;
    c.vaddr = c.ring + 8192 + (4 * g + q4) * 256 + ((8 * (g & 1) + 2 * q4) << 4) + 8 * pp;
    __builtin_amdgcn_s_barrier();
    if (aw == 0) STAMP(ws, w, 0);
    ISSUE_K(c, 0, 0); ISSUE_V(c, 0, 0); WAIT_VM(8); ISSUE_K(c, 1, 1); WAIT_VM(8); ISSUE_V(c, 1, 1);
    if (aw == 0) STAMP(ws, w, 1);
    lds_wait_ge(L, C_QREADY, 1);
    if (aw == 0) STAMP(ws, w, 2);
#pragma unroll
    for (int s = 0; s < 4; ++s) { u32x4 v = *(LAS u32x4*)(L + L_Q + (i & 3) * 256 + 64 * s + 16 * g); if (i >= 4) v = u32x4{0, 0, 0, 0}; c.qf[s] = __builtin_bit_cast(bf16x8, v); }
    AttnState st;
#pragma unroll
    for (int d = 0; d < 8; ++d) st.acc[d] = f32x4{0.f, 0.f, 0.f, 0.f};
    st.m = -INFINITY; st.l = 0.f;
    for (int t = 0; t < NT_TILES - 2; t += 2) { attn_tile<0, 0>(L, c, st, t); attn_tile<1, 0>(L, c, st, t + 1); }
    if (lane == 0) lds_st(L, C_ATTEND, 1);
    attn_tile<0, 1>(L, c, st, NT_TILES - 2);
    attn_tile<1, 2>(L, c, st, NT_TILES - 1);
    if (aw == 0) STAMP(ws, w, 3);
    const float lsum = xrow16_sum(st.l);
    lds_wait_ge(L, C_HDONE, 1);
    LAS float* mr = (LAS float*)(L + L_STG + aw * MRG_W);
    if (i < 4) {
#pragma unroll
      for (int d = 0; d < 8; ++d) *(LAS f32x4*)(mr + i * 128 + 16 * d + 4 * g) = st.acc[d];
      if (g == 0) { mr[512 + i] = st.m; mr[516 + i] = lsum; }
    }
    WAIT_LGKM0();
    unsigned old = 0;
    if (lane == 0) old = lds_add(L, C_MRG);
    old = __builtin_amdgcn_readfirstlane(old);
    asm volatile("" ::: "memory");
    if (old == 3) {
      float M = -INFINITY;
#pragma unroll
      for (int ww = 0; ww < 4; ++ww) M = fmaxf(M, ((LAS float*)(L + L_STG + ww * MRG_W))[512 + g]);
      float Ls = 0.f; f32x4 o0 = {0.f, 0.f, 0.f, 0.f}, o1 = {0.f, 0.f, 0.f, 0.f};
#pragma unroll
      for (int ww = 0; ww < 4; ++ww) { LAS float* rec = (LAS float*)(L + L_STG + ww * MRG_W);
        const float wt = __builtin_amdgcn_exp2f((rec[512 + g] - M) * 1.4426950408889634f);
        Ls = fmaf(wt, rec[516 + g], Ls);
        o0 += wt * *(LAS f32x4*)(rec + g * 128 + 8 * i); o1 += wt * *(LAS f32x4*)(rec + g * 128 + 8 * i + 4); }
      u32x4 pk = {cvtpk(o0[0], o0[1]), cvtpk(o0[2], o0[3]), cvtpk(o1[0], o1[1]), cvtpk(o1[2], o1[3])};
#pragma unroll
      for (int k = 0; k < 4; ++k) pk[k] = pk[k] == 0xffffffffu ? 0x7fc07fc0u : pk[k];
      __builtin_amdgcn_raw_buffer_store_b128(pk, rsW, WS_PART + ((b * 8 + kvh) * 4 + sp) * 1024 + lane * 16, 0, 16);
      if (i == 0) {
        unsigned mu = __float_as_uint(M), lu = __float_as_uint(Ls);
        mu = mu == 0xffffffffu ? 0x7fc00000u : mu; lu = lu == 0xffffffffu ? 0x7fc00000u : lu;
        __hip_atomic_store((GAS u64*)(ws + WS_ML + (b * 8 + kvh) * 128 + g * 32 + sp * 8), ((u64)lu << 32) | mu, RLX_AGENT);
      }
      STAMP(ws, w, 4);
    }
    return;
  }

  const int hw = wave - 4;
  if (hw == 0) STAMP(ws, w, 8);
  const u32x4 xv = *(const GAS u32x4*)(x + hw * DM + lane * 8);
  u64 E = 0;
  if (hw == 0) E = __hip_atomic_load((GAS u64*)(ws + WS_EPOCH), RLX_AGENT);
  {
    const u64 qa = (u64)wq;
    const u32x4 rsQ = {(unsigned)qa, (unsigned)(qa >> 32), 4u << 20, 0x00020000u};
    const unsigned n0 = (w & 7) * 512 + (w >> 3) * 16;
    const unsigned vo = (lane >> 1) * 8192 + (lane & 1) * 16;
    dma1(vo, rsQ, (2 * hw) * 262144u + n0 * 2, L_STG + (2 * hw) * 1024);
    dma1(vo, rsQ, (2 * hw + 1) * 262144u + n0 * 2, L_STG + (2 * hw + 1) * 1024);
  }
  if (hw == 3) { if (lane < 16) lds_st(L, lane, 0u); WAIT_LGKM0(); }
  __builtin_amdgcn_s_barrier();
  {
    float f[8];
#pragma unroll
    for (int k = 0; k < 4; ++k) { f[2 * k] = lo16(xv[k]); f[2 * k + 1] = hi16(xv[k]); }
    float s = ((f[0] + f[1]) + (f[2] + f[3])) + ((f[4] + f[5]) + (f[6] + f[7]));
    s = wave_sum(s);
    const float mean = s * (1.f / DM);
    float q = 0.f;
#pragma unroll
    for (int k = 0; k < 8; ++k) { const float d = f[k] - mean; q = fmaf(d, d, q); }
    q = wave_sum(q);
    const float var = rbf(q * (1.f / DM));
    const float mb = rbf(mean);
    const float sd = rbf(sqrtf(rbf(var + 1e-5f)));
    u32x4 o;
#pragma unroll
    for (int k = 0; k < 4; ++k) o[k] = cvtpk(rbf(f[2 * k] - mb) / sd, rbf(f[2 * k + 1] - mb) / sd);
    *(LAS u32x4*)(L + L_XN + hw * XN_STRIDE + lane * 16) = o;
    if (hw == 0 && lane == 0) { const u64 T = E + 1; lds_st(L, C_TLO, (unsigned)T); lds_st(L, C_THI, (unsigned)(T >> 32)); }
    asm volatile("s_waitcnt vmcnt(0) lgkmcnt(0)" ::: "memory");
    if (lane == 0) lds_add(L, C_LN);
  }
  lds_wait_ge(L, C_LN, 8);
  const unsigned Tlo = __builtin_amdgcn_readfirstlane(lds_ld(L, C_TLO)), Thi = __builtin_amdgcn_readfirstlane(lds_ld(L, C_THI));
  const u64 T = ((u64)Thi << 32) | Tlo;
  const unsigned tg_lo = Tlo << 16, tg_hi = (Thi << 16) | (Tlo >> 16);
  if (hw == 0) {
    STAMP(ws, w, 9);
    const f32x4 d = proj16(L, lane);
    WAIT_LGKM0();
    if (lane == 0) lds_st(L, C_STGFREE, 1);
    const unsigned n0 = (w & 7) * 512 + (w >> 3) * 16;
    const unsigned pk = g < 2 ? cvtpk(d[0], d[1]) : cvtpk(d[2], d[3]);
    const int b0 = 4 * (g & 1) + (g < 2 ? 0 : 2);
    GAS u64* dst = (GAS u64*)(ws + WS_QG) + b0 * 4096 + n0 + i;
    __hip_atomic_store(dst, ((u64)tg_hi << 32) | (tg_lo | (pk & 0xffffu)), RLX_AGENT);
    __hip_atomic_store(dst + 4096, ((u64)tg_hi << 32) | (tg_lo | (pk >> 16)), RLX_AGENT);
    STAMP(ws, w, 10);
  } else if (hw == 1) {
    const int gam = (w & 7) + 8 * (w >> 6);
    if (gam < 28) {
      const int f0 = gam * 64 + ((w >> 3) & 7) * 8;
      const unsigned short* src = ((lane & 1) ? wu : wg) + (size_t)(lane >> 1) * FF + f0;
      lds_wait_ge(L, C_STGFREE, 1);
      lds_wait_ge(L, C_QREADY, 1);
#pragma unroll
      for (int u = 0; u < 16; ++u) gdma1(src + (size_t)u * 32 * FF, L_STG + u * 1024);
      WAIT_VM(0);
      const f32x4 d = proj16(L, lane);
      WAIT_LGKM0();
      float hv[2];
#pragma unroll
      for (int r = 0; r < 2; ++r) {
        const float own = rbf(g < 2 ? d[r] : d[2 + r]);
        const float oth = dpp<XOR8>(own);
        hv[r] = own > 0.f ? own * oth : 0.f;
      }
      const unsigned pk = cvtpk(hv[0], hv[1]);
      const int b0 = 4 * (g & 1) + (g < 2 ? 0 : 2);
      if (i < 8) {
        GAS u64* dst = (GAS u64*)(ws + WS_HG) + b0 * FF + f0 + i;
        __hip_atomic_store(dst, ((u64)tg_hi << 32) | (tg_lo | (pk & 0xffffu)), RLX_AGENT);
        __hip_atomic_store(dst + FF, ((u64)tg_hi << 32) | (tg_lo | (pk >> 16)), RLX_AGENT);
      }
    }
    if (lane == 0) lds_st(L, C_HDONE, 1);
  } else if (hw == 2) {
    const unsigned qo = WS_QG + (b * 4096 + kvh * 512) * 8 + lane * 16;
    u32x4 v[4];
    for (unsigned spins = 0; spins < SPIN_CAP; ++spins) {
      asm volatile("" ::: "memory");
      bool ok = true;
#pragma unroll
      for (int r = 0; r < 4; ++r) v[r] = __builtin_amdgcn_raw_buffer_load_b128(rsW, qo + r * 1024, 0, 16);
#pragma unroll
      for (int r = 0; r < 4; ++r) ok &= ((v[r][0] ^ tg_lo) >> 16) == 0 && v[r][1] == tg_hi && ((v[r][2] ^ tg_lo) >> 16) == 0 && v[r][3] == tg_hi;
      if (__all(ok)) break;
      __builtin_amdgcn_s_sleep(2);
    }
#pragma unroll
    for (int r = 0; r < 4; ++r) *(LAS unsigned*)(L + L_Q + (lane + 64 * r) * 4) = (v[r][0] & 0xffffu) | (v[r][2] << 16);
    WAIT_LGKM0();
    if (lane == 0) lds_st(L, C_QREADY, 1);
    STAMP(ws, w, 11);
  } else if (hw == 7) {
    const u32x4 e = {0xffffffffu, 0xffffffffu, 0xffffffffu, 0xffffffffu};
    __builtin_amdgcn_raw_buffer_store_b128(e, rsW, WS_PART + ((b * 8 + kvh) * 4 + sp) * 1024 + lane * 16, 0, 16);
    if (i == 0) __hip_atomic_store((GAS u64*)(ws + WS_ML + (b * 8 + kvh) * 128 + g * 32 + sp * 8), ~0ull, RLX_AGENT);
    WAIT_VM(0);
    if (lane == 0) __hip_atomic_store((GAS u64*)(ws + WS_FLAG) + b * 32 + sp * 8 + kvh, T, RLX_AGENT);
  }
  lds_wait_ge(L, C_QREADY, 1);

  const int bf = w >> 5, n1 = (4 * (w & 7) + ((w >> 3) & 3)) * 16;
  unsigned wp[64];
  {
    const GAS unsigned short* src = (const GAS unsigned short*)wo + (size_t)((4 * hw + g) * 128 + 8 * i) * DM + n1;
    u32x4 t[16];
#pragma unroll
    for (int r = 0; r < 8; ++r) { t[2 * r] = __builtin_nontemporal_load((const GAS u32x4*)(src + r * DM)); t[2 * r + 1] = __builtin_nontemporal_load((const GAS u32x4*)(src + r * DM + 8)); }
#pragma unroll
    for (int rp = 0; rp < 4; ++rp)
#pragma unroll
      for (int hf = 0; hf < 2; ++hf)
#pragma unroll
        for (int k = 0; k < 4; ++k) {
          const unsigned A = t[4 * rp + hf][k], Bv = t[4 * rp + 2 + hf][k];
          wp[rp * 16 + hf * 8 + 2 * k] = __builtin_amdgcn_perm(Bv, A, 0x05040100u);
          wp[rp * 16 + hf * 8 + 2 * k + 1] = __builtin_amdgcn_perm(Bv, A, 0x07060302u);
        }
  }
  {
    const GAS unsigned short* src = (const GAS unsigned short*)wd + (size_t)(224 * hw + (lane >> 1)) * DM + n1 + (lane & 1) * 8;
    u32x4 wv[7];
#pragma unroll
    for (int r = 0; r < 7; ++r) wv[r] = __builtin_nontemporal_load((const GAS u32x4*)(src + (size_t)r * 32 * DM));
    const unsigned ho = WS_HG + (bf * FF + 224 * hw + (lane >> 1)) * 8;
    u32x2 hv[7];
    for (unsigned spins = 0; spins < SPIN_CAP; ++spins) {
      asm volatile("" ::: "memory");
      bool ok = true;
#pragma unroll
      for (int r = 0; r < 7; ++r) hv[r] = __builtin_amdgcn_raw_buffer_load_b64(rsW, ho + r * 256, 0, 16);
#pragma unroll
      for (int r = 0; r < 7; ++r) ok &= ((hv[r][0] ^ tg_lo) >> 16) == 0 && hv[r][1] == tg_hi;
      if (__all(ok)) break;
      __builtin_amdgcn_s_sleep(8);
    }
    float a[8];
#pragma unroll
    for (int k = 0; k < 8; ++k) a[k] = 0.f;
#pragma unroll
    for (int r = 0; r < 7; ++r) { const float h = lo16(hv[r][0]);
#pragma unroll
      for (int k = 0; k < 4; ++k) { a[2 * k] = fmaf(h, lo16(wv[r][k]), a[2 * k]); a[2 * k + 1] = fmaf(h, hi16(wv[r][k]), a[2 * k + 1]); } }
#pragma unroll
    for (int k = 0; k < 8; ++k) { float t = a[k]; t += dpp<XOR2>(t); t += dpp<ROR4>(t); t += dpp<XOR8>(t); a[k] = xrow16_sum(t); }
    if (lane < 2) {
      LAS float* rg = (LAS float*)(L + L_REDG) + hw * 16 + lane * 8;
      *(LAS f32x4*)rg = f32x4{a[0], a[1], a[2], a[3]}; *(LAS f32x4*)(rg + 4) = f32x4{a[4], a[5], a[6], a[7]};
    }
    WAIT_LGKM0();
    unsigned oldg = 0;
    if (lane == 0) oldg = lds_add(L, C_G);
    oldg = __builtin_amdgcn_readfirstlane(oldg);
    asm volatile("" ::: "memory");
    if (oldg == 7) {
      if (lane < 16) {
        float d = 0.f;
#pragma unroll
        for (int k = 0; k < 8; ++k) d += ((LAS float*)(L + L_REDG))[k * 16 + i];
        d = rbf(d);
        const float gg = rbf(d / (1.f + __expf(-d)));
        const float xr = bf2f(x[bf * DM + n1 + i]);
        ((LAS float*)(L + L_GX))[i] = gg; ((LAS float*)(L + L_GX))[16 + i] = xr;
      }
      WAIT_LGKM0();
      if (lane == 0) lds_st(L, C_GDONE, 1);
    }
  }
  if (hw == 0) STAMP(ws, w, 12);
  {
    const GAS u64* fl = (const GAS u64*)(ws + WS_FLAG) + bf * 32 + (lane & 3) * 8 + hw;
    for (unsigned spins = 0; spins < SPIN_CAP; ++spins) {
      const u64 v = __hip_atomic_load(fl, RLX_AGENT);
      if (__all(v == T)) break;
      __builtin_amdgcn_s_sleep(8);
    }
    if (w == 0 && hw == 3) {
      const GAS u64* fa = (const GAS u64*)(ws + WS_FLAG) + lane;
      for (unsigned spins = 0; spins < SPIN_CAP; ++spins) {
        bool ok = true;
#pragma unroll
        for (int r = 0; r < 4; ++r) ok &= __hip_atomic_load(fa + 64 * r, RLX_AGENT) == T;
        if (__all(ok)) break;
        __builtin_amdgcn_s_sleep(8);
      }
      if (lane == 0) __hip_atomic_store((GAS u64*)(ws + WS_EPOCH), T, RLX_AGENT);
    }
  }
  while (__builtin_amdgcn_readfirstlane(lds_ld(L, C_ATTEND)) == 0) __builtin_amdgcn_s_sleep(32);
  if (hw == 0) STAMP(ws, w, 13);
  {
    const unsigned po = WS_PART + (bf * 8 + hw) * 4096 + lane * 16;
    const unsigned mo = WS_ML + (bf * 8 + hw) * 128 + g * 32;
    u32x4 pv[4], ml[2];
    for (unsigned spins = 0; spins < SPIN_CAP; ++spins) {
      asm volatile("" ::: "memory");
      ml[0] = __builtin_amdgcn_raw_buffer_load_b128(rsW, mo, 0, 16);
      ml[1] = __builtin_amdgcn_raw_buffer_load_b128(rsW, mo + 16, 0, 16);
      bool ok = true;
#pragma unroll
      for (int k = 0; k < 4; ++k) ok &= ml[0][k] != 0xffffffffu && ml[1][k] != 0xffffffffu;
      if (__all(ok)) break;
      __builtin_amdgcn_s_sleep(2);
    }
    for (unsigned spins = 0; spins < SPIN_CAP; ++spins) {
      asm volatile("" ::: "memory");
#pragma unroll
      for (int j = 0; j < 4; ++j) pv[j] = __builtin_amdgcn_raw_buffer_load_b128(rsW, po + j * 1024, 0, 16);
      bool ok = true;
#pragma unroll
      for (int j = 0; j < 4; ++j)
#pragma unroll
        for (int k = 0; k < 4; ++k) ok &= pv[j][k] != 0xffffffffu;
      if (__all(ok)) break;
      __builtin_amdgcn_s_sleep(1);
    }
    if (hw == 0) STAMP(ws, w, 14);
    const float m0 = __uint_as_float(ml[0][0]), m1 = __uint_as_float(ml[0][2]), m2 = __uint_as_float(ml[1][0]), m3 = __uint_as_float(ml[1][2]);
    const float M = fmaxf(fmaxf(m0, m1), fmaxf(m2, m3));
    float wt[4] = {__builtin_amdgcn_exp2f((m0 - M) * 1.4426950408889634f), __builtin_amdgcn_exp2f((m1 - M) * 1.4426950408889634f),
                   __builtin_amdgcn_exp2f((m2 - M) * 1.4426950408889634f), __builtin_amdgcn_exp2f((m3 - M) * 1.4426950408889634f)};
    const float Ls = (wt[0] * __uint_as_float(ml[0][1]) + wt[1] * __uint_as_float(ml[0][3])) + (wt[2] * __uint_as_float(ml[1][1]) + wt[3] * __uint_as_float(ml[1][3]));
    const float inv = 1.f / Ls;
    unsigned op[4];
#pragma unroll
    for (int k = 0; k < 4; ++k) {
      float lo = 0.f, hi = 0.f;
#pragma unroll
      for (int j = 0; j < 4; ++j) { lo = fmaf(wt[j], lo16(pv[j][k]), lo); hi = fmaf(wt[j], hi16(pv[j][k]), hi); }
      op[k] = cvtpk(lo * inv, hi * inv);
    }
    float xs[16];
#pragma unroll
    for (int n = 0; n < 16; ++n) { float a = 0.f;
#pragma unroll
      for (int rp = 0; rp < 4; ++rp) a = dot2(wp[rp * 16 + n], op[rp], a);
      xs[n] = a; }
#define TR_STEP(HALF, CTRL, BIT) _Pragma("unroll") for (int h = 0; h < (HALF); ++h) { \
      const float lo_ = xs[h], hi_ = xs[h + (HALF)]; const float t1_ = lo_ + dpp<CTRL>(lo_), t2_ = hi_ + dpp<CTRL>(hi_); \
      xs[h] = (i & (BIT)) ? t2_ : t1_; }
    TR_STEP(8, XOR8, 8) TR_STEP(4, XOR7, 4) TR_STEP(2, XOR2, 2) TR_STEP(1, XOR1, 1)
#undef TR_STEP
    const float sv = xrow16_sum(xs[0]);
    if (lane < 16) ((LAS float*)(L + L_REDS))[hw * 16 + i] = sv;
    WAIT_LGKM0();
    unsigned old = 0;
    if (lane == 0) old = lds_add(L, C_S);
    old = __builtin_amdgcn_readfirstlane(old);
    asm volatile("" ::: "memory");
    if (old == 7) {
      lds_wait_ge(L, C_GDONE, 1);
      if (lane < 16) {
        float s = 0.f;
#pragma unroll
        for (int k = 0; k < 8; ++k) s += ((LAS float*)(L + L_REDS))[k * 16 + i];
        s = rbf(s);
        const float gg = ((LAS float*)(L + L_GX))[i], xr = ((LAS float*)(L + L_GX))[16 + i];
        out[bf * DM + n1 + i] = (unsigned short)(cvtpk(xr + rbf(s * gg), 0.f) & 0xffffu);
      }
      STAMP(ws, w, 15);
    }
  }
}

extern "C" void kernel_launch(void* const* d_in, const int* in_sizes, int n_in, void* d_out, int out_size, void* d_ws, size_t ws_size, hipStream_t stream) {
  const unsigned short* x = (const unsigned short*)d_in[0];
  const unsigned short* kc = (const unsigned short*)d_in[1];
  const unsigned short* vc = (const unsigned short*)d_in[2];
  const unsigned short* wq = (const unsigned short*)d_in[3];
  const unsigned short* wo = (const unsigned short*)d_in[4];
  const unsigned short* wg = (const unsigned short*)d_in[6];
  const unsigned short* wu = (const unsigned short*)d_in[7];
  const unsigned short* wd = (const unsigned short*)d_in[8];
  (void)hipFuncSetAttribute((const void*)fused_decode, hipFuncAttributeMaxDynamicSharedMemorySize, L_END);
  fused_decode<<<dim3(NWG), dim3(NTHR), L_END, stream>>>(x, kc, vc, wq, wo, wg, wu, wd, (unsigned short*)d_out, (unsigned char*)d_ws);
}
```

```cpp
#include <hip/hip_runtime.h>
#include <hip/hip_bf16.h>
#include <cstdint>

typedef __bf16 bf16x2 __attribute__((ext_vector_type(2)));
typedef __bf16 bf16x4 __attribute__((ext_vector_type(4)));
typedef __bf16 bf16x8 __attribute__((ext_vector_type(8)));
typedef unsigned u32x2 __attribute__((ext_vector_type(2)));
typedef unsigned u32x4 __attribute__((ext_vector_type(4)));
typedef float f32x2 __attribute__((ext_vector_type(2)));
typedef float f32x4 __attribute__((ext_vector_type(4)));
typedef unsigned long long u64;
#define LAS __attribute__((address_space(3)))
#define GAS __attribute__((address_space(1)))
#define RLX_AGENT __ATOMIC_RELAXED, __HIP_MEMORY_SCOPE_AGENT
#define RLX_WG __ATOMIC_RELAXED, __HIP_MEMORY_SCOPE_WORKGROUP

constexpr int NB = 8, DM = 512, HQ = 32, HKV = 8, SK = 8192, DH = 128, FF = 1792;
constexpr int NSPLIT = 4, NWG = 256, NTHR = 768;
constexpr int NT_TILES = SK / NSPLIT / 32 / 4;

#define THROTTLE 1

constexpr unsigned WS_EPOCH = 0;
constexpr unsigned WS_FLAG = 4096;
constexpr unsigned WS_ML = 8192;
constexpr unsigned WS_QG = 16384;
constexpr unsigned WS_HG = WS_QG + NB * 4096 * 8;
constexpr unsigned WS_PART = WS_HG + NB * FF * 8;
constexpr unsigned WS_Y = WS_PART + NWG * 1024;
constexpr unsigned WS_Y2 = WS_Y + 524288;
constexpr unsigned WS_END = WS_Y2 + 524288;
constexpr unsigned WS_STAMP = 4u << 20;

constexpr unsigned L_RING = 0;
constexpr unsigned L_GU = 3 * 32768 + 16384;
constexpr unsigned L_STG = 131072;
constexpr unsigned L_XN = L_STG + 16384;
constexpr unsigned XN_STRIDE = 1040;
constexpr unsigned L_Q = L_XN + 8 * XN_STRIDE;
constexpr unsigned L_OA = L_Q + 1024;
constexpr unsigned OA_STRIDE = 272;
constexpr unsigned L_CTL = L_OA + 8 * OA_STRIDE;
constexpr unsigned L_END = L_CTL + 128;
static_assert(L_END <= 163840, "LDS");
constexpr unsigned MRG_W = (512 + 8) * 4;
enum { C_LN = 0, C_STGFREE, C_QREADY, C_HDONE, C_MRG, C_ATTEND, C_TLO, C_THI, C_CLEAN, C_WOLD, C_WDLD, C_HG, C_Y2DONE, C_WOREAD, C_OG };

__device__ __forceinline__ float bf2f(unsigned short u) { return __builtin_bit_cast(float, (unsigned)u << 16); }
__device__ __forceinline__ unsigned cvtpk(float a, float b) {
  bf16x2 t = __builtin_convertvector(f32x2{a, b}, bf16x2);
  return __builtin_bit_cast(unsigned, t);
}
__device__ __forceinline__ float lo16(unsigned u) { return __builtin_bit_cast(float, u << 16); }
__device__ __forceinline__ float hi16(unsigned u) { return __builtin_bit_cast(float, u & 0xffff0000u); }
__device__ __forceinline__ float rbf(float f) { return lo16(cvtpk(f, f)); }
__device__ __forceinline__ float dot2(unsigned k, unsigned q, float acc) {
  return __builtin_amdgcn_fdot2_f32_bf16(__builtin_bit_cast(bf16x2, k), __builtin_bit_cast(bf16x2, q), acc, false);
}
template <int CTRL> __device__ __forceinline__ float dpp(float x) {
  return __builtin_bit_cast(float, __builtin_amdgcn_mov_dpp(__builtin_bit_cast(int, x), CTRL, 0xf, 0xf, true));
}
constexpr int XOR1 = 0xB1, XOR2 = 0x4E, XOR7 = 0x141, XOR8 = 0x128, ROR4 = 0x124;
__device__ __forceinline__ float xrow16_max(float x) {
  auto s = __builtin_amdgcn_permlane16_swap(__float_as_uint(x), __float_as_uint(x), false, false);
  x = fmaxf(__uint_as_float(s[0]), __uint_as_float(s[1]));
  auto t = __builtin_amdgcn_permlane32_swap(__float_as_uint(x), __float_as_uint(x), false, false);
  return fmaxf(__uint_as_float(t[0]), __uint_as_float(t[1]));
}
__device__ __forceinline__ float xrow16_sum(float x) {
  auto s = __builtin_amdgcn_permlane16_swap(__float_as_uint(x), __float_as_uint(x), false, false);
  x = __uint_as_float(s[0]) + __uint_as_float(s[1]);
  auto t = __builtin_amdgcn_permlane32_swap(__float_as_uint(x), __float_as_uint(x), false, false);
  return __uint_as_float(t[0]) + __uint_as_float(t[1]);
}
__device__ __forceinline__ float wave_sum(float x) {
  x += dpp<XOR1>(x); x += dpp<XOR2>(x); x += dpp<XOR7>(x); x += dpp<XOR8>(x);
  return xrow16_sum(x);
}

__device__ __forceinline__ void dma4(unsigned v0, unsigned v1, unsigned v2, unsigned v3, u32x4 rs, unsigned so, unsigned ldsaddr) {
  unsigned keep;
  asm volatile(
    "s_mov_b32 %0, m0\n\t"
    "s_mov_b32 m0, %7\n\t"
    "s_nop 0\n\t"
    "buffer_load_dwordx4 %1, %5, %6 offen nt lds\n\t"
    "buffer_load_dwordx4 %2, %5, %6 offen offset:1024 nt lds\n\t"
    "buffer_load_dwordx4 %3, %5, %6 offen offset:2048 nt lds\n\t"
    "buffer_load_dwordx4 %4, %5, %6 offen offset:3072 nt lds\n\t"
    "s_mov_b32 m0, %0"
    : "=&s"(keep) : "v"(v0), "v"(v1), "v"(v2), "v"(v3), "s"(rs), "s"(so), "s"(ldsaddr) : "memory");
}
__device__ __forceinline__ void dma1(unsigned v0, u32x4 rs, unsigned so, unsigned ldsaddr) {
  unsigned keep;
  asm volatile(
    "s_mov_b32 %0, m0\n\t"
    "s_mov_b32 m0, %4\n\t"
    "s_nop 0\n\t"
    "buffer_load_dwordx4 %1, %2, %3 offen lds\n\t"
    "s_mov_b32 m0, %0"
    : "=&s"(keep) : "v"(v0), "s"(rs), "s"(so), "s"(ldsaddr) : "memory");
}
__device__ __forceinline__ void gdma1(const void* g, unsigned ldsaddr) {
  unsigned keep;
  asm volatile(
    "s_mov_b32 %0, m0\n\t"
    "s_mov_b32 m0, %2\n\t"
    "s_nop 0\n\t"
    "global_load_lds_dwordx4 %1, off\n\t"
    "s_mov_b32 m0, %0"
    : "=&s"(keep) : "v"(g), "s"(ldsaddr) : "memory");
}
#define WAIT_VM(n) asm volatile("s_waitcnt vmcnt(" #n ")" ::: "memory")
#define WAIT_LGKM0() asm volatile("s_waitcnt lgkmcnt(0)" ::: "memory")

__device__ __forceinline__ unsigned lds_ld(LAS unsigned char* L, int idx) { return __hip_atomic_load((LAS unsigned*)(L + L_CTL) + idx, RLX_WG); }
__device__ __forceinline__ void lds_st(LAS unsigned char* L, int idx, unsigned v) { __hip_atomic_store((LAS unsigned*)(L + L_CTL) + idx, v, RLX_WG); }
__device__ __forceinline__ unsigned lds_add(LAS unsigned char* L, int idx) { return __hip_atomic_fetch_add((LAS unsigned*)(L + L_CTL) + idx, 1u, RLX_WG); }
__device__ __forceinline__ void lds_wait_ge(LAS unsigned char* L, int idx, unsigned want) {
  while ((unsigned)__builtin_amdgcn_readfirstlane(lds_ld(L, idx)) < want) __builtin_amdgcn_s_sleep(1);
  asm volatile("" ::: "memory");
}
constexpr unsigned SPIN_CAP = 1u << 17;

#define STAMP(ws, w, k) do {} while (0)

struct AttnState { f32x4 acc[8]; float m, l; };
struct AttnConst { u32x4 rsK, rsV; unsigned gbase, ring, vk0, vv0, kaddr, vaddr; bf16x8 qf[4]; };

#define ISSUE_K(c_, t, slot) do { const unsigned so_ = (c_).gbase + (unsigned)(t) * 32768u, ld_ = (c_).ring + (slot) * 16384; \
    dma4((c_).vk0, (c_).vk0 ^ 64, (c_).vk0 ^ 128, (c_).vk0 ^ 192, (c_).rsK, so_, ld_); dma4((c_).vk0, (c_).vk0 ^ 64, (c_).vk0 ^ 128, (c_).vk0 ^ 192, (c_).rsK, so_ + 4096, ld_ + 4096); } while (0)
#define ISSUE_V(c_, t, slot) do { const unsigned so_ = (c_).gbase + (unsigned)(t) * 32768u, ld_ = (c_).ring + (slot) * 16384 + 8192; \
    dma4((c_).vv0, (c_).vv0 ^ 128, (c_).vv0, (c_).vv0 ^ 128, (c_).rsV, so_, ld_); dma4((c_).vv0, (c_).vv0 ^ 128, (c_).vv0, (c_).vv0 ^ 128, (c_).rsV, so_ + 4096, ld_ + 4096); } while (0)

template <int SLOT, int MODE>
__device__ __forceinline__ void attn_tile(LAS unsigned char* L, const AttnConst& c, AttnState& st, const int t) {
  constexpr float SCALE = 0.088388347648318440f, LOG2E = 1.4426950408889634f;
  if (MODE < 2) WAIT_VM(24); else WAIT_VM(8);
  f32x4 s0 = {0.f, 0.f, 0.f, 0.f}, s1 = {0.f, 0.f, 0.f, 0.f};
#pragma unroll
  for (int s = 0; s < 4; ++s) {
    const bf16x8 k0 = *(LAS bf16x8*)(L + (c.kaddr ^ (s << 6)) + SLOT * 16384);
    const bf16x8 k1 = *(LAS bf16x8*)(L + (c.kaddr ^ (s << 6)) + SLOT * 16384 + 4096);
    s0 = __builtin_amdgcn_mfma_f32_16x16x32_bf16(k0, c.qf[s], s0, 0, 0, 0);
    s1 = __builtin_amdgcn_mfma_f32_16x16x32_bf16(k1, c.qf[s], s1, 0, 0, 0);
  }
  WAIT_LGKM0();
  if (MODE == 0) ISSUE_K(c, t + 2, SLOT);
  float x[8];
  { unsigned u;
    u = cvtpk(s0[0], s0[1]); u = cvtpk(lo16(u) * SCALE, hi16(u) * SCALE); x[0] = lo16(u); x[1] = hi16(u);
    u = cvtpk(s0[2], s0[3]); u = cvtpk(lo16(u) * SCALE, hi16(u) * SCALE); x[2] = lo16(u); x[3] = hi16(u);
    u = cvtpk(s1[0], s1[1]); u = cvtpk(lo16(u) * SCALE, hi16(u) * SCALE); x[4] = lo16(u); x[5] = hi16(u);
    u = cvtpk(s1[2], s1[3]); u = cvtpk(lo16(u) * SCALE, hi16(u) * SCALE); x[6] = lo16(u); x[7] = hi16(u); }
  float pm = fmaxf(fmaxf(fmaxf(x[0], x[1]), fmaxf(x[2], x[3])), fmaxf(fmaxf(x[4], x[5]), fmaxf(x[6], x[7])));
  pm = xrow16_max(pm);
  if (__any(pm > st.m)) {
    const float mn = fmaxf(st.m, pm);
    const float alpha = __builtin_amdgcn_exp2f((st.m - mn) * LOG2E);
    st.l *= alpha; st.m = mn;
#pragma unroll
    for (int d = 0; d < 8; ++d) st.acc[d] *= alpha;
  }
  const float mb = st.m * LOG2E;
  float p[8];
#pragma unroll
  for (int e = 0; e < 8; ++e) p[e] = __builtin_amdgcn_exp2f(fmaf(x[e], LOG2E, -mb));
  st.l += ((p[0] + p[1]) + (p[2] + p[3])) + ((p[4] + p[5]) + (p[6] + p[7]));
  const u32x4 pw = {cvtpk(p[0], p[1]), cvtpk(p[2], p[3]), cvtpk(p[4], p[5]), cvtpk(p[6], p[7])};
  const bf16x8 pf = __builtin_bit_cast(bf16x8, pw);
  if (MODE == 0) WAIT_VM(24); else if (MODE == 1) WAIT_VM(16); else WAIT_VM(0);
#pragma unroll
  for (int d = 0; d < 8; ++d) {
    const bf16x4 v0 = __builtin_amdgcn_ds_read_tr16_b64_v4bf16((LAS bf16x4*)(L + (c.vaddr ^ (d << 5)) + SLOT * 16384));
    const bf16x4 v1 = __builtin_amdgcn_ds_read_tr16_b64_v4bf16((LAS bf16x4*)(L + (c.vaddr ^ (d << 5)) + SLOT * 16384 + 4096));
    const bf16x8 vf = __builtin_shufflevector(v0, v1, 0, 1, 2, 3, 4, 5, 6, 7);
    st.acc[d] = __builtin_amdgcn_mfma_f32_16x16x32_bf16(vf, pf, st.acc[d], 0, 0, 0);
  }
  WAIT_LGKM0();
  if (MODE == 0) ISSUE_V(c, t + 2, SLOT);
}

__device__ __forceinline__ f32x4 proj16(LAS unsigned char* L, int lane, const unsigned stg) {
  const int g = lane >> 4, i = lane & 15;
  const unsigned aa = L_XN + (i & 7) * XN_STRIDE + g * 16;
  const unsigned ba = stg + g * 256 + i * 8;
  f32x4 d0 = {0.f, 0.f, 0.f, 0.f}, d1 = {0.f, 0.f, 0.f, 0.f};
#pragma unroll
  for (int s = 0; s < 16; s += 2) {
    const bf16x8 a0 = *(LAS bf16x8*)(L + aa + s * 64);
    const bf16x4 b0 = __builtin_amdgcn_ds_read_tr16_b64_v4bf16((LAS bf16x4*)(L + ba + s * 1024));
    const bf16x4 b1 = __builtin_amdgcn_ds_read_tr16_b64_v4bf16((LAS bf16x4*)(L + ba + s * 1024 + 128));
    const bf16x8 a1 = *(LAS bf16x8*)(L + aa + s * 64 + 64);
    const bf16x4 b2 = __builtin_amdgcn_ds_read_tr16_b64_v4bf16((LAS bf16x4*)(L + ba + s * 1024 + 1024));
    const bf16x4 b3 = __builtin_amdgcn_ds_read_tr16_b64_v4bf16((LAS bf16x4*)(L + ba + s * 1024 + 1024 + 128));
    d0 = __builtin_amdgcn_mfma_f32_16x16x32_bf16(a0, __builtin_shufflevector(b0, b1, 0, 1, 2, 3, 4, 5, 6, 7), d0, 0, 0, 0);
    d1 = __builtin_amdgcn_mfma_f32_16x16x32_bf16(a1, __builtin_shufflevector(b2, b3, 0, 1, 2, 3, 4, 5, 6, 7), d1, 0, 0, 0);
  }
  return d0 + d1;
}

__device__ __forceinline__ unsigned nosent(unsigned u) { return u == 0xffffffffu ? 0x7fc07fc0u : u; }

__global__ void __launch_bounds__(NTHR, 3)
fused_decode(const unsigned short* __restrict__ x, const unsigned short* __restrict__ kc, const unsigned short* __restrict__ vc,
             const unsigned short* __restrict__ wq, const unsigned short* __restrict__ wo, const unsigned short* __restrict__ wg,
             const unsigned short* __restrict__ wu, const unsigned short* __restrict__ wd, unsigned short* __restrict__ out, unsigned char* ws) {
  extern __shared__ __attribute__((aligned(16))) unsigned char lds[];
  LAS unsigned char* L = (LAS unsigned char*)lds;
  const int w = blockIdx.x;
  const int tid = threadIdx.x, lane = tid & 63, wave = __builtin_amdgcn_readfirstlane(tid >> 6);
  const int g = lane >> 4, i = lane & 15;
  const int xc = w & 7, r = w >> 3;
  const int kvh = xc, b = r & 7, sp = r >> 3;
  const __amdgpu_buffer_rsrc_t rsW = __builtin_amdgcn_make_buffer_rsrc(ws, 0, WS_END, 0x00020000);

  if (wave < 4) {
    const int aw = wave;
    const u64 ka = (u64)kc, va = (u64)vc;
    AttnConst c;
    c.rsK = u32x4{(unsigned)ka, (unsigned)(ka >> 32), 128u << 20, 0x00020000u};
    c.rsV = u32x4{(unsigned)va, (unsigned)(va >> 32), 128u << 20, 0x00020000u};
    c.gbase = (unsigned)(((b * HKV + kvh) * SK + sp * (SK / NSPLIT)) * 256) + aw * 8192;
    c.ring = L_RING + aw * 32768;
    c.vk0 = g * 256 + ((i ^ g) << 4);
    c.vv0 = g * 256 + ((i ^ (2 * g)) << 4);
    c.kaddr = c.ring + i * 256 + ((i ^ g) << 4);
    const int q4 = i >> 2, pp = lane & 3;
    c.vaddr = c.ring + 8192 + (4 * g + q4) * 256 + ((8 * (g & 1) + 2 * q4) << 4) + 8 * pp;
    __builtin_amdgcn_s_barrier();
    if (aw == 0) STAMP(ws, w, 0);
    ISSUE_K(c, 0, 0); ISSUE_V(c, 0, 0); WAIT_VM(8); if (aw == 3) lds_wait_ge(L, C_HDONE, 1); ISSUE_K(c, 1, 1); WAIT_VM(8); ISSUE_V(c, 1, 1);
    if (aw == 0) STAMP(ws, w, 1);
    lds_wait_ge(L, C_QREADY, 1);
    if (aw == 0) STAMP(ws, w, 2);
#pragma unroll
    for (int s = 0; s < 4; ++s) { u32x4 v = *(LAS u32x4*)(L + L_Q + (i & 3) * 256 + 64 * s + 16 * g); if (i >= 4) v = u32x4{0, 0, 0, 0}; c.qf[s] = __builtin_bit_cast(bf16x8, v); }
    AttnState st;
#pragma unroll
    for (int d = 0; d < 8; ++d) st.acc[d] = f32x4{0.f, 0.f, 0.f, 0.f};
    st.m = -INFINITY; st.l = 0.f;
    for (int t = 0; t < NT_TILES - 2; t += 2) { attn_tile<0, 0>(L, c, st, t); attn_tile<1, 0>(L, c, st, t + 1); }
    if (lane == 0) lds_st(L, C_ATTEND, 1);
    lds_wait_ge(L, C_WOREAD, 4);
    lds_wait_ge(L, C_CLEAN, 1);
    attn_tile<0, 1>(L, c, st, NT_TILES - 2);
    attn_tile<1, 2>(L, c, st, NT_TILES - 1);
    if (aw == 0) STAMP(ws, w, 3);
    const float lsum = xrow16_sum(st.l);
    LAS float* mr = (LAS float*)(L + L_STG + aw * MRG_W);
    if (i < 4) {
#pragma unroll
      for (int d = 0; d < 8; ++d) *(LAS f32x4*)(mr + i * 128 + 16 * d + 4 * g) = st.acc[d];
      if (g == 0) { mr[512 + i] = st.m; mr[516 + i] = lsum; }
    }
    WAIT_LGKM0();
    unsigned old = 0;
    if (lane == 0) old = lds_add(L, C_MRG);
    old = __builtin_amdgcn_readfirstlane(old);
    asm volatile("" ::: "memory");
    if (old == 3) {
      float M = -INFINITY;
#pragma unroll
      for (int ww = 0; ww < 4; ++ww) M = fmaxf(M, ((LAS float*)(L + L_STG + ww * MRG_W))[512 + g]);
      float Ls = 0.f; f32x4 o0 = {0.f, 0.f, 0.f, 0.f}, o1 = {0.f, 0.f, 0.f, 0.f};
#pragma unroll
      for (int ww = 0; ww < 4; ++ww) { LAS float* rec = (LAS float*)(L + L_STG + ww * MRG_W);
        const float wt = __builtin_amdgcn_exp2f((rec[512 + g] - M) * 1.4426950408889634f);
        Ls = fmaf(wt, rec[516 + g], Ls);
        o0 += wt * *(LAS f32x4*)(rec + g * 128 + 8 * i); o1 += wt * *(LAS f32x4*)(rec + g * 128 + 8 * i + 4); }
      const u32x4 pk = {nosent(cvtpk(o0[0], o0[1])), nosent(cvtpk(o0[2], o0[3])), nosent(cvtpk(o1[0], o1[1])), nosent(cvtpk(o1[2], o1[3]))};
      __builtin_amdgcn_raw_buffer_store_b128(pk, rsW, WS_PART + (b * 32 + kvh * 4 + g) * 1024 + sp * 256 + i * 16, 0, 16);
      if (i == 0)
        __hip_atomic_store((GAS u64*)(ws + WS_ML + (b * 32 + kvh * 4 + g) * 32 + sp * 8), ((u64)nosent(__float_as_uint(Ls)) << 32) | nosent(__float_as_uint(M)), RLX_AGENT);
      STAMP(ws, w, 4);
    }
    return;
  }

  const int hw = wave - 4;
  if (hw == 0) STAMP(ws, w, 8);
  u32x4 xv = *(const GAS u32x4*)(x + hw * DM + lane * 8);
  u64 E = __hip_atomic_load((GAS u64*)(ws + WS_EPOCH), RLX_AGENT);
  const int gam = xc + 8 * (w >> 6), f0 = gam * 64 + (r & 7) * 8;
  {
    const u64 qa = (u64)wq;
    const u32x4 rsQ = {(unsigned)qa, (unsigned)(qa >> 32), 4u << 20, 0x00020000u};
    const unsigned n0 = xc * 512 + r * 16;
    const unsigned vo = (lane >> 1) * 8192 + (lane & 1) * 16;
    dma1(vo, rsQ, (2 * hw) * 262144u + n0 * 2, L_STG + (2 * hw) * 1024);
    dma1(vo, rsQ, (2 * hw + 1) * 262144u + n0 * 2, L_STG + (2 * hw + 1) * 1024);
    if (gam < 28) {
      const unsigned short* src = ((lane & 1) ? wu : wg) + (size_t)(64 * hw + (lane >> 1)) * FF + f0;
      gdma1(src, L_GU + (2 * hw) * 1024);
      gdma1(src + (size_t)32 * FF, L_GU + (2 * hw + 1) * 1024);
    }
  }
  if (hw == 3) { if (lane < 32) lds_st(L, lane, 0u); WAIT_LGKM0(); }
  __builtin_amdgcn_s_barrier();
  asm volatile("" : "+v"(xv), "+v"(E) :: "memory");
  {
    float f[8];
#pragma unroll
    for (int k = 0; k < 4; ++k) { f[2 * k] = lo16(xv[k]); f[2 * k + 1] = hi16(xv[k]); }
    float s = ((f[0] + f[1]) + (f[2] + f[3])) + ((f[4] + f[5]) + (f[6] + f[7]));
    s = wave_sum(s);
    const float mean = s * (1.f / DM);
    float q = 0.f;
#pragma unroll
    for (int k = 0; k < 8; ++k) { const float d = f[k] - mean; q = fmaf(d, d, q); }
    q = wave_sum(q);
    const float var = rbf(q * (1.f / DM));
    const float mb = rbf(mean);
    const float sd = rbf(sqrtf(rbf(var + 1e-5f)));
    u32x4 o;
#pragma unroll
    for (int k = 0; k < 4; ++k) o[k] = cvtpk(rbf(f[2 * k] - mb) / sd, rbf(f[2 * k + 1] - mb) / sd);
    *(LAS u32x4*)(L + L_XN + hw * XN_STRIDE + lane * 16) = o;
    asm volatile("s_waitcnt vmcnt(0) lgkmcnt(0)" ::: "memory");
    if (lane == 0) lds_add(L, C_LN);
  }
  const u64 T = E + 1;
  const unsigned Tlo = __builtin_amdgcn_readfirstlane((unsigned)T), Thi = __builtin_amdgcn_readfirstlane((unsigned)(T >> 32));
  const unsigned tg_lo = Tlo << 16, tg_hi = (Thi << 16) | (Tlo >> 16);
  const u64 Tu = ((u64)Thi << 32) | Tlo;
  if (hw == 0) {
    lds_wait_ge(L, C_LN, 8);
    STAMP(ws, w, 9);
    const f32x4 d = proj16(L, lane, L_STG);
    const unsigned n0 = xc * 512 + r * 16;
    const unsigned pk = g < 2 ? cvtpk(d[0], d[1]) : cvtpk(d[2], d[3]);
    const int b0 = 4 * (g & 1) + (g < 2 ? 0 : 2);
    GAS u64* dst = (GAS u64*)(ws + WS_QG) + b0 * 4096 + n0 + i;
    __hip_atomic_store(dst, ((u64)tg_hi << 32) | (tg_lo | (pk & 0xffffu)), RLX_AGENT);
    __hip_atomic_store(dst + 4096, ((u64)tg_hi << 32) | (tg_lo | (pk >> 16)), RLX_AGENT);
    if (lane == 0) lds_st(L, C_STGFREE, 1);
    STAMP(ws, w, 10);
  } else if (hw == 1) {
    if (gam < 28) {
      lds_wait_ge(L, C_LN, 8);
      const f32x4 d = proj16(L, lane, L_GU);
      float hv[2];
#pragma unroll
      for (int k = 0; k < 2; ++k) {
        const float own = rbf(g < 2 ? d[k] : d[2 + k]);
        const float oth = dpp<XOR8>(own);
        hv[k] = own > 0.f ? own * oth : 0.f;
      }
      const unsigned pk = cvtpk(hv[0], hv[1]);
      const int b0 = 4 * (g & 1) + (g < 2 ? 0 : 2);
      if (i < 8) {
        GAS u64* dst = (GAS u64*)(ws + WS_HG) + b0 * FF + f0 + i;
        __hip_atomic_store(dst, ((u64)tg_hi << 32) | (tg_lo | (pk & 0xffffu)), RLX_AGENT);
        __hip_atomic_store(dst + FF, ((u64)tg_hi << 32) | (tg_lo | (pk >> 16)), RLX_AGENT);
      }
    }
    if (lane == 0) lds_st(L, C_HDONE, 1);
  } else if (hw == 2) {
    const unsigned qo = WS_QG + (b * 4096 + kvh * 512) * 8 + lane * 16;
    u32x4 v[4];
    lds_wait_ge(L, C_STGFREE, 1);
    for (unsigned spins = 0; spins < SPIN_CAP; ++spins) {
      asm volatile("" ::: "memory");
      bool ok = true;
#pragma unroll
      for (int k = 0; k < 4; ++k) v[k] = __builtin_amdgcn_raw_buffer_load_b128(rsW, qo + k * 1024, 0, 16);
#pragma unroll
      for (int k = 0; k < 4; ++k) ok &= ((v[k][0] ^ tg_lo) >> 16) == 0 && v[k][1] == tg_hi && ((v[k][2] ^ tg_lo) >> 16) == 0 && v[k][3] == tg_hi;
      if (__all(ok)) break;
      __builtin_amdgcn_s_sleep(1);
    }
#pragma unroll
    for (int k = 0; k < 4; ++k) *(LAS unsigned*)(L + L_Q + (lane + 64 * k) * 4) = (v[k][0] & 0xffffu) | (v[k][2] << 16);
    WAIT_LGKM0();
    if (lane == 0) lds_st(L, C_QREADY, 1);
    STAMP(ws, w, 11);
  } else if (hw == 7) {
    const u32x4 e = {0xffffffffu, 0xffffffffu, 0xffffffffu, 0xffffffffu};
    __builtin_amdgcn_raw_buffer_store_b128(e, rsW, WS_PART + (b * 32 + kvh * 4 + g) * 1024 + sp * 256 + i * 16, 0, 16);
    if (i == 0) __hip_atomic_store((GAS u64*)(ws + WS_ML + (b * 32 + kvh * 4 + g) * 32 + sp * 8), ~0ull, RLX_AGENT);
#pragma unroll
    for (int u = 0; u < 2; ++u) {
      const unsigned yo = (((i & 7) * 32 + 4 * xc + 2 * u + (i >> 3)) * 32 + r) * 64 + g * 16;
      __builtin_amdgcn_raw_buffer_store_b128(e, rsW, WS_Y + yo, 0, 16);
      __builtin_amdgcn_raw_buffer_store_b128(e, rsW, WS_Y2 + yo, 0, 16);
    }
    WAIT_VM(0);
    if (lane == 0) { __hip_atomic_store((GAS u64*)(ws + WS_FLAG) + xc * 32 + r, Tu, RLX_AGENT); lds_st(L, C_CLEAN, 1); }
  }
  lds_wait_ge(L, C_QREADY, 1);
  if (hw == 4) {
    const u64 a_ = (u64)wo;
    const u32x4 rs = {(unsigned)a_, (unsigned)(a_ >> 32), 4u << 20, 0x00020000u};
    const unsigned vo = (lane >> 3) * 1024 + (lane & 7) * 16;
#pragma unroll
    for (int u = 0; u < 16; ++u) dma1(vo, rs, (unsigned)((128 * r + 8 * u) * 1024 + xc * 128), L_STG + u * 1024);
    WAIT_VM(0);
    if (lane == 0) lds_st(L, C_WOLD, 1);
  } else if (hw == 5) {
    const u64 a_ = (u64)wd;
    const u32x4 rs = {(unsigned)a_, (unsigned)(a_ >> 32), (unsigned)(FF * DM * 2), 0x00020000u};
    const unsigned vo = (lane >> 3) * 1024 + (lane & 7) * 16;
    lds_wait_ge(L, C_HDONE, 1);
#pragma unroll
    for (int u = 0; u < 8; ++u) dma1(vo, rs, (unsigned)((56 * r + 8 * u) * 1024 + xc * 128), L_XN + u * 1024);
    WAIT_VM(0);
    if (lane == 0) lds_st(L, C_WDLD, 1);
  }
  {
    const unsigned ho = WS_HG + (hw * FF + 56 * r + (lane < 56 ? lane : 0)) * 8;
    u32x2 hv;
    for (unsigned spins = 0; spins < SPIN_CAP; ++spins) {
      asm volatile("" ::: "memory");
      hv = __builtin_amdgcn_raw_buffer_load_b64(rsW, ho, 0, 16);
      if (__all(((hv[0] ^ tg_lo) >> 16) == 0 && hv[1] == tg_hi)) break;
      __builtin_amdgcn_s_sleep(4);
    }
    *(LAS unsigned short*)(L + L_OA + hw * OA_STRIDE + lane * 2) = lane < 56 ? (unsigned short)(hv[0] & 0xffffu) : (unsigned short)0;
    WAIT_LGKM0();
    if (lane == 0) lds_add(L, C_HG);
  }
  bf16x8 wf[4];
  const unsigned ba = (g * 8 + (i >> 2)) * 128 + (lane & 3) * 8 + (hw & 3) * 32;
  const unsigned oa = L_OA + (i & 7) * OA_STRIDE + g * 16;
  const unsigned yo = ((i * 32 + 4 * xc + (hw & 3)) * 32 + r) * 64 + g * 16;
  if (hw < 4) {
    lds_wait_ge(L, C_WDLD, 1);
    lds_wait_ge(L, C_HG, 8);
    lds_wait_ge(L, C_CLEAN, 1);
    f32x4 d = {0.f, 0.f, 0.f, 0.f};
#pragma unroll
    for (int s = 0; s < 2; ++s) {
      const bf16x4 a0 = __builtin_amdgcn_ds_read_tr16_b64_v4bf16((LAS bf16x4*)(L + L_XN + ba + s * 4096));
      const bf16x4 a1 = __builtin_amdgcn_ds_read_tr16_b64_v4bf16((LAS bf16x4*)(L + L_XN + ba + s * 4096 + 512));
      const bf16x8 hb = *(LAS bf16x8*)(L + oa + s * 64);
      d = __builtin_amdgcn_mfma_f32_16x16x32_bf16(__builtin_shufflevector(a0, a1, 0, 1, 2, 3, 4, 5, 6, 7), hb, d, 0, 0, 0);
    }
    const u32x4 pk = {nosent(__float_as_uint(d[0])), nosent(__float_as_uint(d[1])), nosent(__float_as_uint(d[2])), nosent(__float_as_uint(d[3]))};
    if (i < 8) __builtin_amdgcn_raw_buffer_store_b128(pk, rsW, WS_Y2 + yo, 0, 16);
    WAIT_LGKM0();
    if (lane == 0) lds_add(L, C_Y2DONE);
    lds_wait_ge(L, C_WOLD, 1);
#pragma unroll
    for (int s = 0; s < 4; ++s) {
      const bf16x4 a0 = __builtin_amdgcn_ds_read_tr16_b64_v4bf16((LAS bf16x4*)(L + L_STG + ba + s * 4096));
      const bf16x4 a1 = __builtin_amdgcn_ds_read_tr16_b64_v4bf16((LAS bf16x4*)(L + L_STG + ba + s * 4096 + 512));
      wf[s] = __builtin_shufflevector(a0, a1, 0, 1, 2, 3, 4, 5, 6, 7);
    }
    asm volatile("s_waitcnt lgkmcnt(0)" : "+v"(wf[0]), "+v"(wf[1]), "+v"(wf[2]), "+v"(wf[3]) :: "memory");
    if (lane == 0) lds_add(L, C_WOREAD);
  }
  const int bf = r >> 2, cb = 4 * xc + (r & 3), n1 = cb * 16;
  float gg[4], xr[4];
  if (hw == 6) {
    const u32x2 xq = *(const GAS u32x2*)(x + bf * DM + n1 + (lane & 3) * 4);
    xr[0] = lo16(xq[0]); xr[1] = hi16(xq[0]); xr[2] = lo16(xq[1]); xr[3] = hi16(xq[1]);
    const GAS u64* fl = (const GAS u64*)(ws + WS_FLAG) + xc * 32 + (lane & 31);
    for (unsigned spins = 0; spins < SPIN_CAP; ++spins) {
      if (__all(__hip_atomic_load(fl, RLX_AGENT) == Tu)) break;
      __builtin_amdgcn_s_sleep(8);
    }
    const unsigned io = WS_Y2 + (bf * 32 + cb) * 2048 + lane * 16;
    u32x4 y0, y1;
    for (unsigned spins = 0; spins < SPIN_CAP; ++spins) {
      asm volatile("" ::: "memory");
      y0 = __builtin_amdgcn_raw_buffer_load_b128(rsW, io, 0, 16);
      y1 = __builtin_amdgcn_raw_buffer_load_b128(rsW, io + 1024, 0, 16);
      bool ok = true;
#pragma unroll
      for (int k = 0; k < 4; ++k) ok &= y0[k] != 0xffffffffu && y1[k] != 0xffffffffu;
      if (__all(ok)) break;
      __builtin_amdgcn_s_sleep(8);
    }
#pragma unroll
    for (int k = 0; k < 4; ++k) {
      float t = __uint_as_float(y0[k]) + __uint_as_float(y1[k]);
      t += dpp<ROR4>(t); t += dpp<XOR8>(t); t = xrow16_sum(t);
      const float d = rbf(t);
      gg[k] = rbf(d / (1.f + __expf(-d)));
    }
    STAMP(ws, w, 12);
  } else if (hw == 5 && w == 0) {
    const GAS u64* fa = (const GAS u64*)(ws + WS_FLAG) + lane;
    for (unsigned spins = 0; spins < SPIN_CAP; ++spins) {
      bool ok = true;
#pragma unroll
      for (int k = 0; k < 4; ++k) ok &= __hip_atomic_load(fa + 64 * k, RLX_AGENT) == Tu;
      if (__all(ok)) break;
      __builtin_amdgcn_s_sleep(8);
    }
    if (lane == 0) __hip_atomic_store((GAS u64*)(ws + WS_EPOCH), Tu, RLX_AGENT);
  }
  {
    const GAS u64* fl = (const GAS u64*)(ws + WS_FLAG) + (r >> 2) * 32 + hw + 8 * (lane & 3);
    for (unsigned spins = 0; spins < SPIN_CAP; ++spins) {
      if (__all(__hip_atomic_load(fl, RLX_AGENT) == Tu)) break;
      __builtin_amdgcn_s_sleep(8);
    }
  }
  lds_wait_ge(L, C_Y2DONE, 4);
  while (__builtin_amdgcn_readfirstlane(lds_ld(L, C_ATTEND)) == 0) __builtin_amdgcn_s_sleep(16);
  if (hw == 0) STAMP(ws, w, 13);
  {
    const unsigned po = WS_PART + (hw * 32 + r) * 1024 + lane * 16;
    const unsigned mo = WS_ML + (hw * 32 + r) * 32 + g * 8;
    u32x4 pv; u32x2 ml;
    for (unsigned spins = 0; spins < SPIN_CAP; ++spins) {
      asm volatile("" ::: "memory");
      pv = __builtin_amdgcn_raw_buffer_load_b128(rsW, po, 0, 16);
      ml = __builtin_amdgcn_raw_buffer_load_b64(rsW, mo, 0, 16);
      const bool ok = pv[0] != 0xffffffffu && pv[1] != 0xffffffffu && pv[2] != 0xffffffffu && pv[3] != 0xffffffffu && ml[0] != 0xffffffffu && ml[1] != 0xffffffffu;
      if (__all(ok)) break;
      __builtin_amdgcn_s_sleep(2);
    }
    if (hw == 0) STAMP(ws, w, 14);
    const float mj = __uint_as_float(ml[0]);
    const float M = xrow16_max(mj);
    const float wt = __builtin_amdgcn_exp2f((mj - M) * 1.4426950408889634f);
    const float inv = 1.f / xrow16_sum(wt * __uint_as_float(ml[1]));
    u32x4 op;
#pragma unroll
    for (int k = 0; k < 4; ++k) op[k] = cvtpk(xrow16_sum(wt * lo16(pv[k])) * inv, xrow16_sum(wt * hi16(pv[k])) * inv);
    if (g == 0) *(LAS u32x4*)(L + L_OA + hw * OA_STRIDE + i * 16) = op;
    WAIT_LGKM0();
    if (lane == 0) lds_add(L, C_OG);
  }
  if (hw < 4) {
    lds_wait_ge(L, C_OG, 8);
    f32x4 d = {0.f, 0.f, 0.f, 0.f};
#pragma unroll
    for (int s = 0; s < 4; ++s) d = __builtin_amdgcn_mfma_f32_16x16x32_bf16(wf[s], *(LAS bf16x8*)(L + oa + s * 64), d, 0, 0, 0);
    const u32x4 pk = {nosent(__float_as_uint(d[0])), nosent(__float_as_uint(d[1])), nosent(__float_as_uint(d[2])), nosent(__float_as_uint(d[3]))};
    if (i < 8) __builtin_amdgcn_raw_buffer_store_b128(pk, rsW, WS_Y + yo, 0, 16);
    if (hw == 0) STAMP(ws, w, 16);
  } else if (hw == 6) {
    const unsigned io = WS_Y + (bf * 32 + cb) * 2048 + lane * 16;
    u32x4 y0, y1;
    for (unsigned spins = 0; spins < SPIN_CAP; ++spins) {
      asm volatile("" ::: "memory");
      y0 = __builtin_amdgcn_raw_buffer_load_b128(rsW, io, 0, 16);
      y1 = __builtin_amdgcn_raw_buffer_load_b128(rsW, io + 1024, 0, 16);
      bool ok = true;
#pragma unroll
      for (int k = 0; k < 4; ++k) ok &= y0[k] != 0xffffffffu && y1[k] != 0xffffffffu;
      if (__all(ok)) break;
      __builtin_amdgcn_s_sleep(1);
    }
    float o[4];
#pragma unroll
    for (int k = 0; k < 4; ++k) {
      float t = __uint_as_float(y0[k]) + __uint_as_float(y1[k]);
      t += dpp<ROR4>(t); t += dpp<XOR8>(t); t = xrow16_sum(t);
      o[k] = xr[k] + rbf(rbf(t) * gg[k]);
    }
    if (lane < 4) *(GAS u32x2*)(out + bf * DM + n1 + lane * 4) = u32x2{cvtpk(o[0], o[1]), cvtpk(o[2], o[3])};
    STAMP(ws, w, 15);
  }
}

extern "C" void kernel_launch(void* const* d_in, const int* in_sizes, int n_in, void* d_out, int out_size, void* d_ws, size_t ws_size, hipStream_t stream) {
  const unsigned short* x = (const unsigned short*)d_in[0];
  const unsigned short* kc = (const unsigned short*)d_in[1];
  const unsigned short* vc = (const unsigned short*)d_in[2];
  const unsigned short* wq = (const unsigned short*)d_in[3];
  const unsigned short* wo = (const unsigned short*)d_in[4];
  const unsigned short* wg = (const unsigned short*)d_in[6];
  const unsigned short* wu = (const unsigned short*)d_in[7];
  const unsigned short* wd = (const unsigned short*)d_in[8];
  (void)hipFuncSetAttribute((const void*)fused_decode, hipFuncAttributeMaxDynamicSharedMemorySize, L_END);
  fused_decode<<<dim3(NWG), dim3(NTHR), L_END, stream>>>(x, kc, vc, wq, wo, wg, wu, wd, (unsigned short*)d_out, (unsigned char*)d_ws);
}
```

```cpp
#include <hip/hip_runtime.h>
#include <hip/hip_bf16.h>
#include <cstdint>

typedef __bf16 bf16x2 __attribute__((ext_vector_type(2)));
typedef __bf16 bf16x4 __attribute__((ext_vector_type(4)));
typedef __bf16 bf16x8 __attribute__((ext_vector_type(8)));
typedef unsigned u32x2 __attribute__((ext_vector_type(2)));
typedef unsigned u32x4 __attribute__((ext_vector_type(4)));
typedef float f32x2 __attribute__((ext_vector_type(2)));
typedef float f32x4 __attribute__((ext_vector_type(4)));
typedef unsigned long long u64;
#define LAS __attribute__((address_space(3)))
#define GAS __attribute__((address_space(1)))
#define RLX_AGENT __ATOMIC_RELAXED, __HIP_MEMORY_SCOPE_AGENT
#define RLX_WG __ATOMIC_RELAXED, __HIP_MEMORY_SCOPE_WORKGROUP

constexpr int NB = 8, DM = 512, HQ = 32, HKV = 8, SK = 8192, DH = 128, FF = 1792;
constexpr int NSPLIT = 4, NWG = 256, NTHR = 768;
constexpr int NT_TILES = SK / NSPLIT / 32 / 4;

#define THROTTLE 1
#define SLP_PART 4
#define SLP_Y 4
#define SLP_MID 64
#define GATE_H 4
#define GATE_Y2 8
#define WARM 0

constexpr unsigned WS_EPOCH = 0;
constexpr unsigned WS_FLAG = 4096;
constexpr unsigned WS_ML = 8192;
constexpr unsigned WS_QG = 16384;
constexpr unsigned WS_HG = WS_QG + NB * 4096 * 8;
constexpr unsigned WS_PART = WS_HG + NB * FF * 8;
constexpr unsigned WS_Y = WS_PART + NWG * 1024;
constexpr unsigned WS_Y2 = WS_Y + 524288;
constexpr unsigned WS_TRASH = WS_Y2 + 524288;
constexpr unsigned WS_END = WS_TRASH + NWG * 4096;
constexpr unsigned WS_STAMP = 4u << 20;

constexpr unsigned L_RING = 0;
constexpr unsigned L_STG = 131072;
constexpr unsigned L_XN = L_STG + 16384;
constexpr unsigned XN_STRIDE = 1040;
constexpr unsigned L_Q = L_XN + 8 * XN_STRIDE;
constexpr unsigned L_OA = L_Q + 1024;
constexpr unsigned OA_STRIDE = 272;
constexpr unsigned L_ML = L_OA + 8 * OA_STRIDE;
constexpr unsigned L_CTL = L_ML + 256;
constexpr unsigned L_TRASH = L_CTL + 128;
constexpr unsigned L_END = L_TRASH + 2080;
static_assert(L_END <= 163840, "LDS");
constexpr unsigned MRG_W = (512 + 8) * 4;
enum { C_LN = 0, C_STGFREE, C_QREADY, C_HDONE, C_MRG, C_ATTEND, C_TLO, C_THI, C_CLEAN, C_WOLD, C_WDLD, C_HG, C_Y2DONE, C_WOREAD, C_OG, C_MRG0, C_PROG, C_MLMASK };

__device__ __forceinline__ float bf2f(unsigned short u) { return __builtin_bit_cast(float, (unsigned)u << 16); }
__device__ __forceinline__ unsigned cvtpk(float a, float b) {
  bf16x2 t = __builtin_convertvector(f32x2{a, b}, bf16x2);
  return __builtin_bit_cast(unsigned, t);
}
__device__ __forceinline__ float lo16(unsigned u) { return __builtin_bit_cast(float, u << 16); }
__device__ __forceinline__ float hi16(unsigned u) { return __builtin_bit_cast(float, u & 0xffff0000u); }
__device__ __forceinline__ float rbf(float f) { return lo16(cvtpk(f, f)); }
__device__ __forceinline__ float dot2(unsigned k, unsigned q, float acc) {
  return __builtin_amdgcn_fdot2_f32_bf16(__builtin_bit_cast(bf16x2, k), __builtin_bit_cast(bf16x2, q), acc, false);
}
template <int CTRL> __device__ __forceinline__ float dpp(float x) {
  return __builtin_bit_cast(float, __builtin_amdgcn_mov_dpp(__builtin_bit_cast(int, x), CTRL, 0xf, 0xf, true));
}
constexpr int XOR1 = 0xB1, XOR2 = 0x4E, XOR7 = 0x141, XOR8 = 0x128, ROR4 = 0x124;
__device__ __forceinline__ float xrow16_max(float x) {
  auto s = __builtin_amdgcn_permlane16_swap(__float_as_uint(x), __float_as_uint(x), false, false);
  x = fmaxf(__uint_as_float(s[0]), __uint_as_float(s[1]));
  auto t = __builtin_amdgcn_permlane32_swap(__float_as_uint(x), __float_as_uint(x), false, false);
  return fmaxf(__uint_as_float(t[0]), __uint_as_float(t[1]));
}
__device__ __forceinline__ float xrow16_sum(float x) {
  auto s = __builtin_amdgcn_permlane16_swap(__float_as_uint(x), __float_as_uint(x), false, false);
  x = __uint_as_float(s[0]) + __uint_as_float(s[1]);
  auto t = __builtin_amdgcn_permlane32_swap(__float_as_uint(x), __float_as_uint(x), false, false);
  return __uint_as_float(t[0]) + __uint_as_float(t[1]);
}
__device__ __forceinline__ float wave_sum(float x) {
  x += dpp<XOR1>(x); x += dpp<XOR2>(x); x += dpp<XOR7>(x); x += dpp<XOR8>(x);
  return xrow16_sum(x);
}

__device__ __forceinline__ void dma4(unsigned v0, unsigned v1, unsigned v2, unsigned v3, u32x4 rs, unsigned so, unsigned ldsaddr) {
  unsigned keep;
  asm volatile(
    "s_mov_b32 %0, m0\n\t"
    "s_mov_b32 m0, %7\n\t"
    "s_nop 0\n\t"
    "buffer_load_dwordx4 %1, %5, %6 offen nt lds\n\t"
    "buffer_load_dwordx4 %2, %5, %6 offen offset:1024 nt lds\n\t"
    "buffer_load_dwordx4 %3, %5, %6 offen offset:2048 nt lds\n\t"
    "buffer_load_dwordx4 %4, %5, %6 offen offset:3072 nt lds\n\t"
    "s_mov_b32 m0, %0"
    : "=&s"(keep) : "v"(v0), "v"(v1), "v"(v2), "v"(v3), "s"(rs), "s"(so), "s"(ldsaddr) : "memory");
}
__device__ __forceinline__ void dma1(unsigned v0, u32x4 rs, unsigned so, unsigned ldsaddr) {
  unsigned keep;
  asm volatile(
    "s_mov_b32 %0, m0\n\t"
    "s_mov_b32 m0, %4\n\t"
    "s_nop 0\n\t"
    "buffer_load_dwordx4 %1, %2, %3 offen lds\n\t"
    "s_mov_b32 m0, %0"
    : "=&s"(keep) : "v"(v0), "s"(rs), "s"(so), "s"(ldsaddr) : "memory");
}
__device__ __forceinline__ void gdma1(const void* g, unsigned ldsaddr) {
  unsigned keep;
  asm volatile(
    "s_mov_b32 %0, m0\n\t"
    "s_mov_b32 m0, %2\n\t"
    "s_nop 0\n\t"
    "global_load_lds_dwordx4 %1, off\n\t"
    "s_mov_b32 m0, %0"
    : "=&s"(keep) : "v"(g), "s"(ldsaddr) : "memory");
}
#define WAIT_VM(n) asm volatile("s_waitcnt vmcnt(" #n ")" ::: "memory")
#define WAIT_LGKM0() asm volatile("s_waitcnt lgkmcnt(0)" ::: "memory")

__device__ __forceinline__ unsigned lds_ld(LAS unsigned char* L, int idx) { return __hip_atomic_load((LAS unsigned*)(L + L_CTL) + idx, RLX_WG); }
__device__ __forceinline__ void lds_st(LAS unsigned char* L, int idx, unsigned v) { __hip_atomic_store((LAS unsigned*)(L + L_CTL) + idx, v, RLX_WG); }
__device__ __forceinline__ unsigned lds_add(LAS unsigned char* L, int idx) { return __hip_atomic_fetch_add((LAS unsigned*)(L + L_CTL) + idx, 1u, RLX_WG); }
__device__ __forceinline__ void lds_wait_ge(LAS unsigned char* L, int idx, unsigned want) {
  while ((unsigned)__builtin_amdgcn_readfirstlane(lds_ld(L, idx)) < want) __builtin_amdgcn_s_sleep(1);
  asm volatile("" ::: "memory");
}
constexpr unsigned SPIN_CAP = 1u << 17;

#define STAMP(ws, w, k) do {} while (0)

struct AttnState { f32x4 acc[8]; float m, l; };
struct AttnConst { u32x4 rsK, rsV; unsigned gbase, ring, vk0, vv0, kaddr, vaddr; bf16x8 qf[4]; };

#define CAPG 6
#define ILV 1
#define HOFF (ILV ? 16384u : 4096u)
#define CAPW() asm volatile("s_waitcnt vmcnt(%0)" :: "i"(CAPG) : "memory")
#define ISSUE_K(c_, t, slot) do { const unsigned so_ = (c_).gbase + (unsigned)(t) * 32768u, ld_ = (c_).ring + (slot) * 16384; \
    CAPW(); dma4((c_).vk0, (c_).vk0 ^ 64, (c_).vk0 ^ 128, (c_).vk0 ^ 192, (c_).rsK, so_, ld_); CAPW(); dma4((c_).vk0, (c_).vk0 ^ 64, (c_).vk0 ^ 128, (c_).vk0 ^ 192, (c_).rsK, so_ + HOFF, ld_ + 4096); } while (0)
#define ISSUE_V(c_, t, slot) do { const unsigned so_ = (c_).gbase + (unsigned)(t) * 32768u, ld_ = (c_).ring + (slot) * 16384 + 8192; \
    CAPW(); dma4((c_).vv0, (c_).vv0 ^ 128, (c_).vv0, (c_).vv0 ^ 128, (c_).rsV, so_, ld_); CAPW(); dma4((c_).vv0, (c_).vv0 ^ 128, (c_).vv0, (c_).vv0 ^ 128, (c_).rsV, so_ + HOFF, ld_ + 4096); } while (0)

#define CAP 32
#define CAP_WAIT() do {} while (0)
template <int SLOT>
__device__ __forceinline__ void attn_tile(LAS unsigned char* L, const AttnConst& c, AttnState& st, const int t, const int rem) {
  constexpr float SCALE = 0.088388347648318440f, LOG2E = 1.4426950408889634f;
  if (rem >= 1) WAIT_VM(24); else if (rem == 0) WAIT_VM(8);
  f32x4 s0 = {0.f, 0.f, 0.f, 0.f}, s1 = {0.f, 0.f, 0.f, 0.f};
#pragma unroll
  for (int s = 0; s < 4; ++s) {
    const bf16x8 k0 = *(LAS bf16x8*)(L + (c.kaddr ^ (s << 6)) + SLOT * 16384);
    const bf16x8 k1 = *(LAS bf16x8*)(L + (c.kaddr ^ (s << 6)) + SLOT * 16384 + 4096);
    s0 = __builtin_amdgcn_mfma_f32_16x16x32_bf16(k0, c.qf[s], s0, 0, 0, 0);
    s1 = __builtin_amdgcn_mfma_f32_16x16x32_bf16(k1, c.qf[s], s1, 0, 0, 0);
  }
  WAIT_LGKM0();
  if (rem >= 2) { CAP_WAIT(); ISSUE_K(c, t + 2, SLOT); }
  float x[8];
  { unsigned u;
    u = cvtpk(s0[0], s0[1]); u = cvtpk(lo16(u) * SCALE, hi16(u) * SCALE); x[0] = lo16(u); x[1] = hi16(u);
    u = cvtpk(s0[2], s0[3]); u = cvtpk(lo16(u) * SCALE, hi16(u) * SCALE); x[2] = lo16(u); x[3] = hi16(u);
    u = cvtpk(s1[0], s1[1]); u = cvtpk(lo16(u) * SCALE, hi16(u) * SCALE); x[4] = lo16(u); x[5] = hi16(u);
    u = cvtpk(s1[2], s1[3]); u = cvtpk(lo16(u) * SCALE, hi16(u) * SCALE); x[6] = lo16(u); x[7] = hi16(u); }
  float pm = fmaxf(fmaxf(fmaxf(x[0], x[1]), fmaxf(x[2], x[3])), fmaxf(fmaxf(x[4], x[5]), fmaxf(x[6], x[7])));
  pm = xrow16_max(pm);
  if (__any(pm > st.m) || rem < 0) {
    const float mn = fmaxf(st.m, pm);
    const float alpha = __builtin_amdgcn_exp2f((st.m - mn) * LOG2E);
    st.l *= alpha; st.m = mn;
#pragma unroll
    for (int d = 0; d < 8; ++d) st.acc[d] *= alpha;
  }
  const float mb = st.m * LOG2E;
  float p[8];
#pragma unroll
  for (int e = 0; e < 8; ++e) p[e] = __builtin_amdgcn_exp2f(fmaf(x[e], LOG2E, -mb));
  st.l += ((p[0] + p[1]) + (p[2] + p[3])) + ((p[4] + p[5]) + (p[6] + p[7]));
  const u32x4 pw = {cvtpk(p[0], p[1]), cvtpk(p[2], p[3]), cvtpk(p[4], p[5]), cvtpk(p[6], p[7])};
  const bf16x8 pf = __builtin_bit_cast(bf16x8, pw);
  if (rem >= 2) WAIT_VM(24); else if (rem == 1) WAIT_VM(16); else if (rem == 0) WAIT_VM(0);
#pragma unroll
  for (int d = 0; d < 8; ++d) {
    const bf16x4 v0 = __builtin_amdgcn_ds_read_tr16_b64_v4bf16((LAS bf16x4*)(L + (c.vaddr ^ (d << 5)) + SLOT * 16384));
    const bf16x4 v1 = __builtin_amdgcn_ds_read_tr16_b64_v4bf16((LAS bf16x4*)(L + (c.vaddr ^ (d << 5)) + SLOT * 16384 + 4096));
    const bf16x8 vf = __builtin_shufflevector(v0, v1, 0, 1, 2, 3, 4, 5, 6, 7);
    st.acc[d] = __builtin_amdgcn_mfma_f32_16x16x32_bf16(vf, pf, st.acc[d], 0, 0, 0);
  }
  WAIT_LGKM0();
  if (rem >= 2) { CAP_WAIT(); ISSUE_V(c, t + 2, SLOT); }
}

__device__ __forceinline__ f32x4 proj16(LAS unsigned char* L, int lane) {
  const int g = lane >> 4, i = lane & 15;
  unsigned aa = L_XN + (i & 7) * XN_STRIDE + g * 16;
  unsigned ba = L_STG + g * 256 + i * 8;
  f32x4 d0 = {0.f, 0.f, 0.f, 0.f}, d1 = {0.f, 0.f, 0.f, 0.f};
#pragma unroll 1
  for (int s = 0; s < 4; ++s) {
    bf16x8 a[4]; bf16x4 b0[4], b1[4];
#pragma unroll
    for (int k = 0; k < 4; ++k) {
      a[k] = *(LAS bf16x8*)(L + aa + k * 64);
      b0[k] = __builtin_amdgcn_ds_read_tr16_b64_v4bf16((LAS bf16x4*)(L + ba + k * 1024));
      b1[k] = __builtin_amdgcn_ds_read_tr16_b64_v4bf16((LAS bf16x4*)(L + ba + k * 1024 + 128));
    }
    d0 = __builtin_amdgcn_mfma_f32_16x16x32_bf16(a[0], __builtin_shufflevector(b0[0], b1[0], 0, 1, 2, 3, 4, 5, 6, 7), d0, 0, 0, 0);
    d1 = __builtin_amdgcn_mfma_f32_16x16x32_bf16(a[1], __builtin_shufflevector(b0[1], b1[1], 0, 1, 2, 3, 4, 5, 6, 7), d1, 0, 0, 0);
    d0 = __builtin_amdgcn_mfma_f32_16x16x32_bf16(a[2], __builtin_shufflevector(b0[2], b1[2], 0, 1, 2, 3, 4, 5, 6, 7), d0, 0, 0, 0);
    d1 = __builtin_amdgcn_mfma_f32_16x16x32_bf16(a[3], __builtin_shufflevector(b0[3], b1[3], 0, 1, 2, 3, 4, 5, 6, 7), d1, 0, 0, 0);
    aa += 256; ba += 4096;
  }
  return d0 + d1;
}

__device__ __forceinline__ unsigned nosent(unsigned u) { return u == 0xffffffffu ? 0x7fc07fc0u : u; }

__device__ __forceinline__ int opaque(int v) { asm volatile("" : "+s"(v)); return v; }

__global__ void __launch_bounds__(NTHR, 3)
fused_decode(const unsigned short* __restrict__ x, const unsigned short* __restrict__ kc, const unsigned short* __restrict__ vc,
             const unsigned short* __restrict__ wq, const unsigned short* __restrict__ wo, const unsigned short* __restrict__ wg,
             const unsigned short* __restrict__ wu, const unsigned short* __restrict__ wd, unsigned short* __restrict__ out, unsigned char* ws) {
  extern __shared__ __attribute__((aligned(16))) unsigned char lds[];
  LAS unsigned char* L = (LAS unsigned char*)lds;
  const int w = blockIdx.x;
  const int tid = threadIdx.x, lane = tid & 63, wave = __builtin_amdgcn_readfirstlane(tid >> 6);
  const int g = lane >> 4, i = lane & 15;
  const int xc = w & 7, r = w >> 3;
  const int kvh = xc, b = r & 7, sp = r >> 3;
  const __amdgpu_buffer_rsrc_t rsW = __builtin_amdgcn_make_buffer_rsrc(ws, 0, WS_END, 0x00020000);
  const unsigned trash = WS_TRASH + w * 4096;

  if (wave < 4) {
    const int aw = wave;
    const u64 ka = (u64)kc, va = (u64)vc;
    AttnConst c;
    c.rsK = u32x4{(unsigned)ka, (unsigned)(ka >> 32), 128u << 20, 0x00020000u};
    c.rsV = u32x4{(unsigned)va, (unsigned)(va >> 32), 128u << 20, 0x00020000u};
    c.gbase = (unsigned)(((b * HKV + kvh) * SK + sp * (SK / NSPLIT)) * 256) + aw * (ILV ? 4096 : 8192);
    c.ring = L_RING + aw * 32768;
    c.vk0 = g * 256 + ((i ^ g) << 4);
    c.vv0 = g * 256 + ((i ^ (2 * g)) << 4);
    c.kaddr = c.ring + i * 256 + ((i ^ g) << 4);
    const int q4 = i >> 2, pp = lane & 3;
    c.vaddr = c.ring + 8192 + (4 * g + q4) * 256 + ((8 * (g & 1) + 2 * q4) << 4) + 8 * pp;
    __builtin_amdgcn_s_barrier();
    if (aw == 0) STAMP(ws, w, 0);
    ISSUE_K(c, 0, 0);
    AttnState st;
#pragma unroll 1
    for (int pass = opaque(WARM ? 0 : 1); pass < 2; ++pass) {
      if (pass) {
        ISSUE_V(c, 0, 0); ISSUE_K(c, 1, 1); ISSUE_V(c, 1, 1);
        if (aw == 0) STAMP(ws, w, 1);
        lds_wait_ge(L, C_QREADY, 1);
        if (aw == 0) STAMP(ws, w, 2);
      }
#pragma unroll
      for (int s = 0; s < 4; ++s) { u32x4 v = *(LAS u32x4*)(L + L_Q + (i & 3) * 256 + 64 * s + 16 * g); if (i >= 4) v = u32x4{0, 0, 0, 0}; c.qf[s] = __builtin_bit_cast(bf16x8, v); }
#pragma unroll
      for (int d = 0; d < 8; ++d) st.acc[d] = f32x4{0.f, 0.f, 0.f, 0.f};
      st.m = -INFINITY; st.l = 0.f;
      const int nt = pass ? NT_TILES : 2;
#pragma unroll 1
      for (int t = 0; t < nt; t += 2) {
        const int rem = pass ? NT_TILES - 1 - t : -1;
        if (aw == 0 && lane == 0 && pass) lds_st(L, C_PROG, t);
        if (rem == 1) {
          if (lane == 0) lds_st(L, C_ATTEND, 1);
          lds_wait_ge(L, C_WOREAD, 4);
          lds_wait_ge(L, C_CLEAN, 1);
        }
        attn_tile<0>(L, c, st, t, rem); attn_tile<1>(L, c, st, t + 1, pass ? rem - 1 : -1);
      }
      if (aw == 0 && pass) STAMP(ws, w, 3);
      const float lsum = xrow16_sum(st.l);
      const unsigned mbase = pass ? L_STG : L_TRASH, mstr = pass ? MRG_W : 0;
      LAS float* mr = (LAS float*)(L + mbase + aw * mstr);
      if (i < 4) {
#pragma unroll
        for (int d = 0; d < 8; ++d) *(LAS f32x4*)(mr + i * 128 + 16 * d + 4 * g) = st.acc[d];
        if (g == 0) { mr[512 + i] = st.m; mr[516 + i] = lsum; }
      }
      WAIT_LGKM0();
      unsigned old = 0;
      if (lane == 0) old = lds_add(L, pass ? C_MRG : C_MRG0);
      old = __builtin_amdgcn_readfirstlane(old);
      asm volatile("" ::: "memory");
      if (old == (pass ? 3u : 0u)) {
        float M = -INFINITY;
#pragma unroll
        for (int ww = 0; ww < 4; ++ww) M = fmaxf(M, ((LAS float*)(L + mbase + ww * mstr))[512 + g]);
        float Ls = 0.f; f32x4 o0 = {0.f, 0.f, 0.f, 0.f}, o1 = {0.f, 0.f, 0.f, 0.f};
#pragma unroll
        for (int ww = 0; ww < 4; ++ww) { LAS float* rec = (LAS float*)(L + mbase + ww * mstr);
          const float wt = __builtin_amdgcn_exp2f((rec[512 + g] - M) * 1.4426950408889634f);
          Ls = fmaf(wt, rec[516 + g], Ls);
          o0 += wt * *(LAS f32x4*)(rec + g * 128 + 8 * i); o1 += wt * *(LAS f32x4*)(rec + g * 128 + 8 * i + 4); }
        const u32x4 pk = {nosent(cvtpk(o0[0], o0[1])), nosent(cvtpk(o0[2], o0[3])), nosent(cvtpk(o1[0], o1[1])), nosent(cvtpk(o1[2], o1[3]))};
        const unsigned po = pass ? WS_PART + (b * 32 + kvh * 4 + g) * 1024 + sp * 256 + i * 16 : trash + lane * 16;
        const unsigned mo = pass ? WS_ML + (kvh * 4 + g) * 256 + b * 32 + sp * 8 : trash + 1024 + g * 8;
        __builtin_amdgcn_raw_buffer_store_b128(pk, rsW, po, 0, 16);
        if (i == 0) __builtin_amdgcn_raw_buffer_store_b64(u32x2{nosent(__float_as_uint(M)), nosent(__float_as_uint(Ls))}, rsW, mo, 0, 16);
        if (pass) STAMP(ws, w, 4);
      }
    }
    return;
  }

  const int hw = wave - 4;
  if (hw == 0) STAMP(ws, w, 8);
  u32x4 xv = *(const GAS u32x4*)(x + hw * DM + lane * 8);
  u64 E = __hip_atomic_load((GAS u64*)(ws + WS_EPOCH), RLX_AGENT);
  {
    const u64 qa = (u64)wq;
    const u32x4 rsQ = {(unsigned)qa, (unsigned)(qa >> 32), 4u << 20, 0x00020000u};
    const unsigned n0 = xc * 512 + r * 16;
    const unsigned vo = (lane >> 1) * 8192 + (lane & 1) * 16;
    dma1(vo, rsQ, (2 * hw) * 262144u + n0 * 2, L_STG + (2 * hw) * 1024);
    dma1(vo, rsQ, (2 * hw + 1) * 262144u + n0 * 2, L_STG + (2 * hw + 1) * 1024);
  }
  if (hw == 3) { if (lane < 32) lds_st(L, lane, 0u); WAIT_LGKM0(); }
  __builtin_amdgcn_s_barrier();
  unsigned tg_lo = 0, tg_hi = 0;
  u64 Tu = 0;
#pragma unroll 1
  for (int pass = opaque(WARM ? 0 : 1); pass < 2; ++pass) {
    u32x4 xin = {(unsigned)lane, (unsigned)lane, (unsigned)lane, (unsigned)lane};
    if (pass) { asm volatile("" : "+v"(xv), "+v"(E) :: "memory"); xin = xv; }
    {
      float f[8];
#pragma unroll
      for (int k = 0; k < 4; ++k) { f[2 * k] = lo16(xin[k]); f[2 * k + 1] = hi16(xin[k]); }
      float s = ((f[0] + f[1]) + (f[2] + f[3])) + ((f[4] + f[5]) + (f[6] + f[7]));
      s = wave_sum(s);
      const float mean = s * (1.f / DM);
      float q = 0.f;
#pragma unroll
      for (int k = 0; k < 8; ++k) { const float d = f[k] - mean; q = fmaf(d, d, q); }
      q = wave_sum(q);
      const float var = rbf(q * (1.f / DM));
      const float mb = rbf(mean);
      const float sd = rbf(sqrtf(rbf(var + 1e-5f)));
      u32x4 o;
#pragma unroll
      for (int k = 0; k < 4; ++k) o[k] = cvtpk(rbf(f[2 * k] - mb) / sd, rbf(f[2 * k + 1] - mb) / sd);
      *(LAS u32x4*)(L + L_XN + hw * XN_STRIDE + lane * 16) = o;
    }
    if (pass) {
      if (WARM && hw == 0) asm volatile("s_waitcnt vmcnt(2) lgkmcnt(0)" ::: "memory");
      else asm volatile("s_waitcnt vmcnt(0) lgkmcnt(0)" ::: "memory");
      if (lane == 0) lds_add(L, C_LN);
      const u64 T = E + 1;
      const unsigned Tlo = __builtin_amdgcn_readfirstlane((unsigned)T), Thi = __builtin_amdgcn_readfirstlane((unsigned)(T >> 32));
      tg_lo = Tlo << 16; tg_hi = (Thi << 16) | (Tlo >> 16);
      Tu = ((u64)Thi << 32) | Tlo;
    }
    if (hw == 0) {
      if (pass) { lds_wait_ge(L, C_LN, 8); STAMP(ws, w, 9); }
      const f32x4 d = proj16(L, lane);
      const unsigned n0 = xc * 512 + r * 16;
      const unsigned pk = g < 2 ? cvtpk(d[0], d[1]) : cvtpk(d[2], d[3]);
      const int b0 = 4 * (g & 1) + (g < 2 ? 0 : 2);
      const unsigned qo = pass ? WS_QG + (b0 * 4096 + n0 + i) * 8 : trash + 2048 + lane * 8;
      __builtin_amdgcn_raw_buffer_store_b64(u32x2{tg_lo | (pk & 0xffffu), tg_hi}, rsW, qo, 0, 16);
      __builtin_amdgcn_raw_buffer_store_b64(u32x2{tg_lo | (pk >> 16), tg_hi}, rsW, pass ? qo + 32768 : qo + 512, 0, 16);
      if (pass) { if (lane == 0) lds_st(L, C_STGFREE, 1); STAMP(ws, w, 10); }
    }
  }
  const int gam = xc + 8 * (w >> 6), f0 = gam * 64 + (r & 7) * 8;
  if (hw == 1) {
    if (gam < 28) {
      const unsigned short* src = ((lane & 1) ? wu : wg) + (size_t)(lane >> 1) * FF + f0;
      lds_wait_ge(L, C_STGFREE, 1);
      lds_wait_ge(L, C_QREADY, 1);
#pragma unroll 1
      for (int u = 0; u < 16; ++u) { gdma1(src, L_STG + u * 1024); src += (size_t)32 * FF; }
      WAIT_VM(0);
      const f32x4 d = proj16(L, lane);
      float hv[2];
#pragma unroll
      for (int k = 0; k < 2; ++k) {
        const float own = rbf(g < 2 ? d[k] : d[2 + k]);
        const float oth = dpp<XOR8>(own);
        hv[k] = own > 0.f ? own * oth : 0.f;
      }
      const unsigned pk = cvtpk(hv[0], hv[1]);
      const int b0 = 4 * (g & 1) + (g < 2 ? 0 : 2);
      if (i < 8) {
        GAS u64* dst = (GAS u64*)(ws + WS_HG) + b0 * FF + f0 + i;
        __hip_atomic_store(dst, ((u64)tg_hi << 32) | (tg_lo | (pk & 0xffffu)), RLX_AGENT);
        __hip_atomic_store(dst + FF, ((u64)tg_hi << 32) | (tg_lo | (pk >> 16)), RLX_AGENT);
      }
    } else lds_wait_ge(L, C_STGFREE, 1);
    if (lane == 0) lds_st(L, C_HDONE, 1);
    STAMP(ws, w, 17);
  } else if (hw == 2) {
    const unsigned qo = WS_QG + (b * 4096 + kvh * 512) * 8 + lane * 16;
    bool okk[4] = {false, false, false, false};
    lds_wait_ge(L, C_STGFREE, 1);
#pragma unroll 1
    for (unsigned spins = 0; spins < SPIN_CAP; ++spins) {
      asm volatile("" ::: "memory");
      u32x4 v[4];
#pragma unroll
      for (int k = 0; k < 4; ++k) if (!okk[k]) v[k] = __builtin_amdgcn_raw_buffer_load_b128(rsW, qo + k * 1024, 0, 16);
#pragma unroll
      for (int k = 0; k < 4; ++k) if (!okk[k]) {
        okk[k] = ((v[k][0] ^ tg_lo) >> 16) == 0 && v[k][1] == tg_hi && ((v[k][2] ^ tg_lo) >> 16) == 0 && v[k][3] == tg_hi;
        *(LAS unsigned*)(L + L_Q + (lane + 64 * k) * 4) = (v[k][0] & 0xffffu) | (v[k][2] << 16);
      }
      if (__all(okk[0] && okk[1] && okk[2] && okk[3])) break;
      __builtin_amdgcn_s_sleep(1);
    }
    WAIT_LGKM0();
    if (lane == 0) lds_st(L, C_QREADY, 1);
    STAMP(ws, w, 11);
  } else if (hw == 7) {
    lds_wait_ge(L, C_QREADY, 1);
    const u32x4 e = {0xffffffffu, 0xffffffffu, 0xffffffffu, 0xffffffffu};
    __builtin_amdgcn_raw_buffer_store_b128(e, rsW, WS_PART + (b * 32 + kvh * 4 + g) * 1024 + sp * 256 + i * 16, 0, 16);
    if (i == 0) __hip_atomic_store((GAS u64*)(ws + WS_ML + (kvh * 4 + g) * 256 + b * 32 + sp * 8), ~0ull, RLX_AGENT);
#pragma unroll
    for (int u = 0; u < 2; ++u) {
      const unsigned yo = (((i & 7) * 32 + 4 * xc + 2 * u + (i >> 3)) * 32 + r) * 64 + g * 16;
      __builtin_amdgcn_raw_buffer_store_b128(e, rsW, WS_Y + yo, 0, 16);
      __builtin_amdgcn_raw_buffer_store_b128(e, rsW, WS_Y2 + yo, 0, 16);
    }
    WAIT_VM(0);
    if (lane == 0) { __hip_atomic_store((GAS u64*)(ws + WS_FLAG) + xc * 32 + r, Tu, RLX_AGENT); lds_st(L, C_CLEAN, 1); }
  } else if (hw == 4) {
    const u64 a_ = (u64)wo;
    const u32x4 rs = {(unsigned)a_, (unsigned)(a_ >> 32), 4u << 20, 0x00020000u};
    const unsigned vo = (lane >> 3) * 1024 + (lane & 7) * 16;
    lds_wait_ge(L, C_HDONE, 1);
#pragma unroll 1
    for (int u = 0; u < 16; ++u) dma1(vo, rs, (unsigned)((128 * r + 8 * u) * 1024 + xc * 128), L_STG + u * 1024);
    WAIT_VM(0);
    if (lane == 0) lds_st(L, C_WOLD, 1);
  } else if (hw == 5) {
    const u64 a_ = (u64)wd;
    const u32x4 rs = {(unsigned)a_, (unsigned)(a_ >> 32), (unsigned)(FF * DM * 2), 0x00020000u};
    const unsigned vo = (lane >> 3) * 1024 + (lane & 7) * 16;
    lds_wait_ge(L, C_HDONE, 1);
#pragma unroll 1
    for (int u = 0; u < 8; ++u) dma1(vo, rs, (unsigned)((56 * r + 8 * u) * 1024 + xc * 128), L_XN + u * 1024);
    WAIT_VM(0);
    if (lane == 0) lds_st(L, C_WDLD, 1);
  }
  lds_wait_ge(L, C_HDONE, 1);
  const int bf = r >> 2, cb = 4 * xc + (r & 3), n1 = cb * 16;
  {
    const GAS u64* fl = (const GAS u64*)(ws + WS_FLAG) + ((hw == 6 && lane >= 32) ? xc * 32 + (lane & 31) : (r >> 2) * 32 + hw + 8 * (lane & 3));
    const unsigned ho = WS_HG + (hw * FF + 56 * r + (lane < 56 ? lane : 0)) * 8;
    u32x2 hv = {0u, 0u};
    bool okh = false, okf = false;
    while (__builtin_amdgcn_readfirstlane(lds_ld(L, C_PROG)) < GATE_H) __builtin_amdgcn_s_sleep(32);
#pragma unroll 1
    for (unsigned spins = 0; spins < SPIN_CAP; ++spins) {
      asm volatile("" ::: "memory");
      u64 fv = 0;
      if (!okh) hv = __builtin_amdgcn_raw_buffer_load_b64(rsW, ho, 0, 16);
      if (!okf) fv = __hip_atomic_load(fl, RLX_AGENT);
      okh = okh || (((hv[0] ^ tg_lo) >> 16) == 0 && hv[1] == tg_hi);
      okf = okf || fv == Tu;
      if (__all(okh && okf)) break;
      __builtin_amdgcn_s_sleep(SLP_MID);
    }
    *(LAS unsigned short*)(L + L_OA + hw * OA_STRIDE + lane * 2) = lane < 56 ? (unsigned short)(hv[0] & 0xffffu) : (unsigned short)0;
    *(LAS unsigned short*)(L + L_OA + hw * OA_STRIDE + 128 + lane * 2) = 0;
    WAIT_LGKM0();
    if (lane == 0) lds_add(L, C_OG);
  }
  const unsigned ba = (g * 8 + (i >> 2)) * 128 + (lane & 3) * 8 + (hw & 3) * 32;
  const unsigned oa = L_OA + (i & 7) * OA_STRIDE + g * 16;
  const unsigned yo = ((i * 32 + 4 * xc + (hw & 3)) * 32 + r) * 64 + g * 16;
  bf16x8 wf[4];
  float gg[4] = {0.f, 0.f, 0.f, 0.f}, xr[4] = {0.f, 0.f, 0.f, 0.f};
  if (hw < 4) {
    lds_wait_ge(L, C_WDLD, 1);
    lds_wait_ge(L, C_CLEAN, 1);
#pragma unroll
    for (int s = 0; s < 4; ++s) {
      const bf16x4 a0 = __builtin_amdgcn_ds_read_tr16_b64_v4bf16((LAS bf16x4*)(L + L_XN + ba + (s & 1) * 4096));
      const bf16x4 a1 = __builtin_amdgcn_ds_read_tr16_b64_v4bf16((LAS bf16x4*)(L + L_XN + ba + (s & 1) * 4096 + 512));
      wf[s] = __builtin_shufflevector(a0, a1, 0, 1, 2, 3, 4, 5, 6, 7);
    }
  } else if (hw == 6) {
    const u32x2 xq = *(const GAS u32x2*)(x + bf * DM + n1 + (lane & 3) * 4);
    xr[0] = lo16(xq[0]); xr[1] = hi16(xq[0]); xr[2] = lo16(xq[1]); xr[3] = hi16(xq[1]);
  } else if (hw == 5 && w == 0) {
    const GAS u64* fa = (const GAS u64*)(ws + WS_FLAG) + lane;
#pragma unroll 1
    for (unsigned spins = 0; spins < SPIN_CAP; ++spins) {
      bool ok = true;
#pragma unroll
      for (int k = 0; k < 4; ++k) ok &= __hip_atomic_load(fa + 64 * k, RLX_AGENT) == Tu;
      if (__all(ok)) break;
      __builtin_amdgcn_s_sleep(SLP_MID);
    }
    if (lane == 0) __hip_atomic_store((GAS u64*)(ws + WS_EPOCH), Tu, RLX_AGENT);
  }
#pragma unroll 1
  for (int rd = opaque(0); rd < 2; ++rd) {
    const unsigned ybase = rd ? WS_Y : WS_Y2;
    {
      if (rd) {
        lds_wait_ge(L, C_Y2DONE, 4);
        while (__builtin_amdgcn_readfirstlane(lds_ld(L, C_ATTEND)) == 0) __builtin_amdgcn_s_sleep(16);
        if (hw == 0) STAMP(ws, w, 13);
      }
      const unsigned po = WS_PART + (hw * 32 + r) * 1024 + lane * 16;
      u32x4 pv = {0u, 0u, 0u, 0u}; u32x2 ml = {0u, 0u};
      if (hw == 7) {
        bool ok = lane >= 32;
#pragma unroll 1
        for (unsigned spins = 0; spins < SPIN_CAP; ++spins) {
          asm volatile("" ::: "memory");
          if (!ok) {
            ml = __builtin_amdgcn_raw_buffer_load_b64(rsW, WS_ML + r * 256 + lane * 8, 0, 16);
            ok = ml[0] != 0xffffffffu && ml[1] != 0xffffffffu;
            *(LAS u32x2*)(L + L_ML + lane * 8) = ml;
          }
          const unsigned mask = (unsigned)__ballot(ok);
          WAIT_LGKM0();
          if (lane == 0 && rd) lds_st(L, C_MLMASK, mask);
          if (mask == 0xffffffffu || !rd) break;
          __builtin_amdgcn_s_sleep(SLP_PART);
        }
        if (lane == 0 && rd) lds_st(L, C_MLMASK, 0xffffffffu);
      } else if (rd) {
        while (((__builtin_amdgcn_readfirstlane(lds_ld(L, C_MLMASK)) >> (4 * hw)) & 15u) != 15u) __builtin_amdgcn_s_sleep(1);
        asm volatile("" ::: "memory");
      }
      ml = *(LAS u32x2*)(L + L_ML + hw * 32 + g * 8);
      {
        bool ok = false;
#pragma unroll 1
        for (unsigned spins = 0; spins < SPIN_CAP; ++spins) {
          asm volatile("" ::: "memory");
          if (!ok) {
            pv = __builtin_amdgcn_raw_buffer_load_b128(rsW, po, 0, 16);
            ok = pv[0] != 0xffffffffu && pv[1] != 0xffffffffu && pv[2] != 0xffffffffu && pv[3] != 0xffffffffu;
          }
          if (__all(ok) || !rd) break;
          __builtin_amdgcn_s_sleep(1);
        }
      }
      if (hw == 0 && rd) STAMP(ws, w, 14);
      const float mj = __uint_as_float(ml[0]);
      const float M = xrow16_max(mj);
      const float wt = __builtin_amdgcn_exp2f((mj - M) * 1.4426950408889634f);
      const float inv = 1.f / xrow16_sum(wt * __uint_as_float(ml[1]));
      u32x4 op;
#pragma unroll
      for (int k = 0; k < 4; ++k) op[k] = cvtpk(xrow16_sum(wt * lo16(pv[k])) * inv, xrow16_sum(wt * hi16(pv[k])) * inv);
      if (g == 0) *(LAS u32x4*)(L + (rd ? L_OA + hw * OA_STRIDE : L_TRASH) + i * 16) = op;
      WAIT_LGKM0();
      if (lane == 0 && rd) lds_add(L, C_OG);
    }
    if (hw < 4) {
      lds_wait_ge(L, C_OG, rd ? 16 : 8);
      f32x4 d = {0.f, 0.f, 0.f, 0.f};
#pragma unroll
      for (int s = 0; s < 4; ++s) d = __builtin_amdgcn_mfma_f32_16x16x32_bf16(wf[s], *(LAS bf16x8*)(L + oa + s * 64), d, 0, 0, 0);
      const u32x4 pk = {nosent(__float_as_uint(d[0])), nosent(__float_as_uint(d[1])), nosent(__float_as_uint(d[2])), nosent(__float_as_uint(d[3]))};
      if (i < 8) __builtin_amdgcn_raw_buffer_store_b128(pk, rsW, ybase + yo, 0, 16);
      if (hw == 0 && rd) STAMP(ws, w, 16);
      if (!rd) {
        WAIT_LGKM0();
        if (lane == 0) lds_add(L, C_Y2DONE);
        lds_wait_ge(L, C_WOLD, 1);
#pragma unroll
        for (int s = 0; s < 4; ++s) {
          const bf16x4 a0 = __builtin_amdgcn_ds_read_tr16_b64_v4bf16((LAS bf16x4*)(L + L_STG + ba + s * 4096));
          const bf16x4 a1 = __builtin_amdgcn_ds_read_tr16_b64_v4bf16((LAS bf16x4*)(L + L_STG + ba + s * 4096 + 512));
          wf[s] = __builtin_shufflevector(a0, a1, 0, 1, 2, 3, 4, 5, 6, 7);
        }
        asm volatile("s_waitcnt lgkmcnt(0)" : "+v"(wf[0]), "+v"(wf[1]), "+v"(wf[2]), "+v"(wf[3]) :: "memory");
        if (lane == 0) lds_add(L, C_WOREAD);
      }
    } else if (hw == 6) {
      const unsigned io = ybase + (bf * 32 + cb) * 2048 + lane * 16;
      u32x4 y0 = {0u, 0u, 0u, 0u}, y1 = {0u, 0u, 0u, 0u};
      bool ok0 = false, ok1 = false;
      if (!rd) while (__builtin_amdgcn_readfirstlane(lds_ld(L, C_PROG)) < GATE_Y2) __builtin_amdgcn_s_sleep(32);
#pragma unroll 1
      for (unsigned spins = 0; spins < SPIN_CAP; ++spins) {
        asm volatile("" ::: "memory");
        if (!ok0) y0 = __builtin_amdgcn_raw_buffer_load_b128(rsW, io, 0, 16);
        if (!ok1) y1 = __builtin_amdgcn_raw_buffer_load_b128(rsW, io + 1024, 0, 16);
        ok0 = y0[0] != 0xffffffffu && y0[1] != 0xffffffffu && y0[2] != 0xffffffffu && y0[3] != 0xffffffffu;
        ok1 = y1[0] != 0xffffffffu && y1[1] != 0xffffffffu && y1[2] != 0xffffffffu && y1[3] != 0xffffffffu;
        if (__all(ok0 && ok1)) break;
        if (rd) __builtin_amdgcn_s_sleep(SLP_Y); else __builtin_amdgcn_s_sleep(SLP_MID);
      }
      float o[4];
#pragma unroll
      for (int k = 0; k < 4; ++k) {
        float t = __uint_as_float(y0[k]) + __uint_as_float(y1[k]);
        t += dpp<ROR4>(t); t += dpp<XOR8>(t); t = rbf(xrow16_sum(t));
        o[k] = xr[k] + rbf(t * gg[k]);
        gg[k] = rbf(t / (1.f + __expf(-t)));
      }
      GAS unsigned short* dst = rd ? (GAS unsigned short*)out + bf * DM + n1 : (GAS unsigned short*)(ws + trash + 3072);
      if (lane < 4) *(GAS u32x2*)(dst + lane * 4) = u32x2{cvtpk(o[0], o[1]), cvtpk(o[2], o[3])};
      if (rd) STAMP(ws, w, 15); else STAMP(ws, w, 12);
    }
  }
}

extern "C" void kernel_launch(void* const* d_in, const int* in_sizes, int n_in, void* d_out, int out_size, void* d_ws, size_t ws_size, hipStream_t stream) {
  const unsigned short* x = (const unsigned short*)d_in[0];
  const unsigned short* kc = (const unsigned short*)d_in[1];
  const unsigned short* vc = (const unsigned short*)d_in[2];
  const unsigned short* wq = (const unsigned short*)d_in[3];
  const unsigned short* wo = (const unsigned short*)d_in[4];
  const unsigned short* wg = (const unsigned short*)d_in[6];
  const unsigned short* wu = (const unsigned short*)d_in[7];
  const unsigned short* wd = (const unsigned short*)d_in[8];
  (void)hipFuncSetAttribute((const void*)fused_decode, hipFuncAttributeMaxDynamicSharedMemorySize, L_END);
  fused_decode<<<dim3(NWG), dim3(NTHR), L_END, stream>>>(x, kc, vc, wq, wo, wg, wu, wd, (unsigned short*)d_out, (unsigned char*)d_ws);
}
```

```cpp
#include <hip/hip_runtime.h>
#include <hip/hip_bf16.h>
#include <cstdint>

typedef __bf16 bf16x2 __attribute__((ext_vector_type(2)));
typedef __bf16 bf16x4 __attribute__((ext_vector_type(4)));
typedef __bf16 bf16x8 __attribute__((ext_vector_type(8)));
typedef unsigned u32x2 __attribute__((ext_vector_type(2)));
typedef unsigned u32x4 __attribute__((ext_vector_type(4)));
typedef float f32x2 __attribute__((ext_vector_type(2)));
typedef float f32x4 __attribute__((ext_vector_type(4)));
typedef unsigned long long u64;
#define LAS __attribute__((address_space(3)))
#define GAS __attribute__((address_space(1)))
#define RLX_AGENT __ATOMIC_RELAXED, __HIP_MEMORY_SCOPE_AGENT
#define RLX_WG __ATOMIC_RELAXED, __HIP_MEMORY_SCOPE_WORKGROUP

constexpr int NB = 8, DM = 512, HQ = 32, HKV = 8, SK = 8192, DH = 128, FF = 1792;
constexpr int NSPLIT = 4, NWG = 256, NTHR = 768;
constexpr int NT_TILES = SK / NSPLIT / 32 / 4;

#define THROTTLE 1
#define SLP_PART 4
#define SLP_Y 4
#define SLP_MID 64
#define GATE_H 4
#define GATE_Y2 8
#define WARM 0

constexpr unsigned WS_EPOCH = 0;
constexpr unsigned WS_FLAG = 4096;
constexpr unsigned WS_ML = 8192;
constexpr unsigned WS_QG = 16384;
constexpr unsigned WS_HG = WS_QG + NB * 4096 * 8;
constexpr unsigned WS_PART = WS_HG + NB * FF * 8;
constexpr unsigned WS_Y = WS_PART + NWG * 1024;
constexpr unsigned WS_Y2 = WS_Y + 524288;
constexpr unsigned WS_TRASH = WS_Y2 + 524288;
constexpr unsigned WS_END = WS_TRASH + NWG * 4096;
constexpr unsigned WS_STAMP = 4u << 20;

constexpr unsigned L_RING = 0;
constexpr unsigned L_STG = 131072;
constexpr unsigned L_XN = L_STG + 16384;
constexpr unsigned XN_STRIDE = 1040;
constexpr unsigned L_Q = L_XN + 8 * XN_STRIDE;
constexpr unsigned L_OA = L_Q + 1024;
constexpr unsigned OA_STRIDE = 272;
constexpr unsigned L_ML = L_OA + 8 * OA_STRIDE;
constexpr unsigned L_CTL = L_ML + 256;
constexpr unsigned L_TRASH = L_CTL + 128;
constexpr unsigned L_END = L_TRASH + 2080;
static_assert(L_END <= 163840, "LDS");
constexpr unsigned MRG_W = (512 + 8) * 4;
enum { C_LN = 0, C_STGFREE, C_QREADY, C_HDONE, C_MRG, C_ATTEND, C_TLO, C_THI, C_CLEAN, C_WOLD, C_WDLD, C_HG, C_Y2DONE, C_WOREAD, C_OG, C_MRG0, C_PROG, C_MLMASK };

__device__ __forceinline__ float bf2f(unsigned short u) { return __builtin_bit_cast(float, (unsigned)u << 16); }
__device__ __forceinline__ unsigned cvtpk(float a, float b) {
  bf16x2 t = __builtin_convertvector(f32x2{a, b}, bf16x2);
  return __builtin_bit_cast(unsigned, t);
}
__device__ __forceinline__ float lo16(unsigned u) { return __builtin_bit_cast(float, u << 16); }
__device__ __forceinline__ float hi16(unsigned u) { return __builtin_bit_cast(float, u & 0xffff0000u); }
__device__ __forceinline__ float rbf(float f) { return lo16(cvtpk(f, f)); }
__device__ __forceinline__ float dot2(unsigned k, unsigned q, float acc) {
  return __builtin_amdgcn_fdot2_f32_bf16(__builtin_bit_cast(bf16x2, k), __builtin_bit_cast(bf16x2, q), acc, false);
}
template <int CTRL> __device__ __forceinline__ float dpp(float x) {
  return __builtin_bit_cast(float, __builtin_amdgcn_mov_dpp(__builtin_bit_cast(int, x), CTRL, 0xf, 0xf, true));
}
constexpr int XOR1 = 0xB1, XOR2 = 0x4E, XOR7 = 0x141, XOR8 = 0x128, ROR4 = 0x124;
__device__ __forceinline__ float xrow16_max(float x) {
  auto s = __builtin_amdgcn_permlane16_swap(__float_as_uint(x), __float_as_uint(x), false, false);
  x = fmaxf(__uint_as_float(s[0]), __uint_as_float(s[1]));
  auto t = __builtin_amdgcn_permlane32_swap(__float_as_uint(x), __float_as_uint(x), false, false);
  return fmaxf(__uint_as_float(t[0]), __uint_as_float(t[1]));
}
__device__ __forceinline__ float xrow16_sum(float x) {
  auto s = __builtin_amdgcn_permlane16_swap(__float_as_uint(x), __float_as_uint(x), false, false);
  x = __uint_as_float(s[0]) + __uint_as_float(s[1]);
  auto t = __builtin_amdgcn_permlane32_swap(__float_as_uint(x), __float_as_uint(x), false, false);
  return __uint_as_float(t[0]) + __uint_as_float(t[1]);
}
__device__ __forceinline__ float wave_sum(float x) {
  x += dpp<XOR1>(x); x += dpp<XOR2>(x); x += dpp<XOR7>(x); x += dpp<XOR8>(x);
  return xrow16_sum(x);
}

#define DMA4_BODY(POL) \
  asm volatile( \
    "s_mov_b32 %0, m0\n\t" \
    "s_mov_b32 m0, %7\n\t" \
    "s_nop 0\n\t" \
    "buffer_load_dwordx4 %1, %5, %6 offen" POL " lds\n\t" \
    "buffer_load_dwordx4 %2, %5, %6 offen offset:1024" POL " lds\n\t" \
    "buffer_load_dwordx4 %3, %5, %6 offen offset:2048" POL " lds\n\t" \
    "buffer_load_dwordx4 %4, %5, %6 offen offset:3072" POL " lds\n\t" \
    "s_mov_b32 m0, %0" \
    : "=&s"(keep) : "v"(v0), "v"(v1), "v"(v2), "v"(v3), "s"(rs), "s"(so), "s"(ldsaddr) : "memory")
#define POL_K ""
#define POL_V " nt"
__device__ __forceinline__ void dma4k(unsigned v0, unsigned v1, unsigned v2, unsigned v3, u32x4 rs, unsigned so, unsigned ldsaddr) { unsigned keep; DMA4_BODY(POL_K); }
__device__ __forceinline__ void dma4v(unsigned v0, unsigned v1, unsigned v2, unsigned v3, u32x4 rs, unsigned so, unsigned ldsaddr) { unsigned keep; DMA4_BODY(POL_V); }
__device__ __forceinline__ void dma1(unsigned v0, u32x4 rs, unsigned so, unsigned ldsaddr) {
  unsigned keep;
  asm volatile(
    "s_mov_b32 %0, m0\n\t"
    "s_mov_b32 m0, %4\n\t"
    "s_nop 0\n\t"
    "buffer_load_dwordx4 %1, %2, %3 offen lds\n\t"
    "s_mov_b32 m0, %0"
    : "=&s"(keep) : "v"(v0), "s"(rs), "s"(so), "s"(ldsaddr) : "memory");
}
__device__ __forceinline__ void gdma1(const void* g, unsigned ldsaddr) {
  unsigned keep;
  asm volatile(
    "s_mov_b32 %0, m0\n\t"
    "s_mov_b32 m0, %2\n\t"
    "s_nop 0\n\t"
    "global_load_lds_dwordx4 %1, off\n\t"
    "s_mov_b32 m0, %0"
    : "=&s"(keep) : "v"(g), "s"(ldsaddr) : "memory");
}
#define WAIT_VM(n) asm volatile("s_waitcnt vmcnt(" #n ")" ::: "memory")
#define WAIT_LGKM0() asm volatile("s_waitcnt lgkmcnt(0)" ::: "memory")

__device__ __forceinline__ unsigned lds_ld(LAS unsigned char* L, int idx) { return __hip_atomic_load((LAS unsigned*)(L + L_CTL) + idx, RLX_WG); }
__device__ __forceinline__ void lds_st(LAS unsigned char* L, int idx, unsigned v) { __hip_atomic_store((LAS unsigned*)(L + L_CTL) + idx, v, RLX_WG); }
__device__ __forceinline__ unsigned lds_add(LAS unsigned char* L, int idx) { return __hip_atomic_fetch_add((LAS unsigned*)(L + L_CTL) + idx, 1u, RLX_WG); }
__device__ __forceinline__ void lds_wait_ge(LAS unsigned char* L, int idx, unsigned want) {
  while ((unsigned)__builtin_amdgcn_readfirstlane(lds_ld(L, idx)) < want) __builtin_amdgcn_s_sleep(1);
  asm volatile("" ::: "memory");
}
constexpr unsigned SPIN_CAP = 1u << 17;

#define STAMP(ws, w, k) do {} while (0)

struct AttnState { f32x4 acc[8]; float m, l; };
struct AttnConst { u32x4 rsK, rsV; unsigned gbase, ring, vk0, vv0, kaddr, vaddr; bf16x8 qf[4]; };

#define CAPG 6
#define ILV 1
#define HOFF (ILV ? 16384u : 4096u)
#define CAPW() asm volatile("s_waitcnt vmcnt(%0)" :: "i"(CAPG) : "memory")
#define ISSUE_K(c_, t, slot) do { const unsigned so_ = (c_).gbase + (unsigned)(t) * 32768u, ld_ = (c_).ring + (slot) * 16384; \
    CAPW(); dma4k((c_).vk0, (c_).vk0 ^ 64, (c_).vk0 ^ 128, (c_).vk0 ^ 192, (c_).rsK, so_, ld_); CAPW(); dma4k((c_).vk0, (c_).vk0 ^ 64, (c_).vk0 ^ 128, (c_).vk0 ^ 192, (c_).rsK, so_ + HOFF, ld_ + 4096); } while (0)
#define ISSUE_V(c_, t, slot) do { const unsigned so_ = (c_).gbase + (unsigned)(t) * 32768u, ld_ = (c_).ring + (slot) * 16384 + 8192; \
    CAPW(); dma4v((c_).vv0, (c_).vv0 ^ 128, (c_).vv0, (c_).vv0 ^ 128, (c_).rsV, so_, ld_); CAPW(); dma4v((c_).vv0, (c_).vv0 ^ 128, (c_).vv0, (c_).vv0 ^ 128, (c_).rsV, so_ + HOFF, ld_ + 4096); } while (0)

#define CAP 32
#define CAP_WAIT() do {} while (0)
template <int SLOT>
__device__ __forceinline__ void attn_tile(LAS unsigned char* L, const AttnConst& c, AttnState& st, const int t, const int rem) {
  constexpr float SCALE = 0.088388347648318440f, LOG2E = 1.4426950408889634f;
  if (rem >= 1) WAIT_VM(24); else if (rem == 0) WAIT_VM(8);
  f32x4 s0 = {0.f, 0.f, 0.f, 0.f}, s1 = {0.f, 0.f, 0.f, 0.f};
#pragma unroll
  for (int s = 0; s < 4; ++s) {
    const bf16x8 k0 = *(LAS bf16x8*)(L + (c.kaddr ^ (s << 6)) + SLOT * 16384);
    const bf16x8 k1 = *(LAS bf16x8*)(L + (c.kaddr ^ (s << 6)) + SLOT * 16384 + 4096);
    s0 = __builtin_amdgcn_mfma_f32_16x16x32_bf16(k0, c.qf[s], s0, 0, 0, 0);
    s1 = __builtin_amdgcn_mfma_f32_16x16x32_bf16(k1, c.qf[s], s1, 0, 0, 0);
  }
  WAIT_LGKM0();
  if (rem >= 2) { CAP_WAIT(); ISSUE_K(c, t + 2, SLOT); }
  float x[8];
  { unsigned u;
    u = cvtpk(s0[0], s0[1]); u = cvtpk(lo16(u) * SCALE, hi16(u) * SCALE); x[0] = lo16(u); x[1] = hi16(u);
    u = cvtpk(s0[2], s0[3]); u = cvtpk(lo16(u) * SCALE, hi16(u) * SCALE); x[2] = lo16(u); x[3] = hi16(u);
    u = cvtpk(s1[0], s1[1]); u = cvtpk(lo16(u) * SCALE, hi16(u) * SCALE); x[4] = lo16(u); x[5] = hi16(u);
    u = cvtpk(s1[2], s1[3]); u = cvtpk(lo16(u) * SCALE, hi16(u) * SCALE); x[6] = lo16(u); x[7] = hi16(u); }
  float pm = fmaxf(fmaxf(fmaxf(x[0], x[1]), fmaxf(x[2], x[3])), fmaxf(fmaxf(x[4], x[5]), fmaxf(x[6], x[7])));
  pm = xrow16_max(pm);
  if (__any(pm > st.m) || rem < 0) {
    const float mn = fmaxf(st.m, pm);
    const float alpha = __builtin_amdgcn_exp2f((st.m - mn) * LOG2E);
    st.l *= alpha; st.m = mn;
#pragma unroll
    for (int d = 0; d < 8; ++d) st.acc[d] *= alpha;
  }
  const float mb = st.m * LOG2E;
  float p[8];
#pragma unroll
  for (int e = 0; e < 8; ++e) p[e] = __builtin_amdgcn_exp2f(fmaf(x[e], LOG2E, -mb));
  st.l += ((p[0] + p[1]) + (p[2] + p[3])) + ((p[4] + p[5]) + (p[6] + p[7]));
  const u32x4 pw = {cvtpk(p[0], p[1]), cvtpk(p[2], p[3]), cvtpk(p[4], p[5]), cvtpk(p[6], p[7])};
  const bf16x8 pf = __builtin_bit_cast(bf16x8, pw);
  if (rem >= 2) WAIT_VM(24); else if (rem == 1) WAIT_VM(16); else if (rem == 0) WAIT_VM(0);
#pragma unroll
  for (int d = 0; d < 8; ++d) {
    const bf16x4 v0 = __builtin_amdgcn_ds_read_tr16_b64_v4bf16((LAS bf16x4*)(L + (c.vaddr ^ (d << 5)) + SLOT * 16384));
    const bf16x4 v1 = __builtin_amdgcn_ds_read_tr16_b64_v4bf16((LAS bf16x4*)(L + (c.vaddr ^ (d << 5)) + SLOT * 16384 + 4096));
    const bf16x8 vf = __builtin_shufflevector(v0, v1, 0, 1, 2, 3, 4, 5, 6, 7);
    st.acc[d] = __builtin_amdgcn_mfma_f32_16x16x32_bf16(vf, pf, st.acc[d], 0, 0, 0);
  }
  WAIT_LGKM0();
  if (rem >= 2) { CAP_WAIT(); ISSUE_V(c, t + 2, SLOT); }
}

__device__ __forceinline__ f32x4 proj16(LAS unsigned char* L, int lane) {
  const int g = lane >> 4, i = lane & 15;
  unsigned aa = L_XN + (i & 7) * XN_STRIDE + g * 16;
  unsigned ba = L_STG + g * 256 + i * 8;
  f32x4 d0 = {0.f, 0.f, 0.f, 0.f}, d1 = {0.f, 0.f, 0.f, 0.f};
#pragma unroll 1
  for (int s = 0; s < 4; ++s) {
    bf16x8 a[4]; bf16x4 b0[4], b1[4];
#pragma unroll
    for (int k = 0; k < 4; ++k) {
      a[k] = *(LAS bf16x8*)(L + aa + k * 64);
      b0[k] = __builtin_amdgcn_ds_read_tr16_b64_v4bf16((LAS bf16x4*)(L + ba + k * 1024));
      b1[k] = __builtin_amdgcn_ds_read_tr16_b64_v4bf16((LAS bf16x4*)(L + ba + k * 1024 + 128));
    }
    d0 = __builtin_amdgcn_mfma_f32_16x16x32_bf16(a[0], __builtin_shufflevector(b0[0], b1[0], 0, 1, 2, 3, 4, 5, 6, 7), d0, 0, 0, 0);
    d1 = __builtin_amdgcn_mfma_f32_16x16x32_bf16(a[1], __builtin_shufflevector(b0[1], b1[1], 0, 1, 2, 3, 4, 5, 6, 7), d1, 0, 0, 0);
    d0 = __builtin_amdgcn_mfma_f32_16x16x32_bf16(a[2], __builtin_shufflevector(b0[2], b1[2], 0, 1, 2, 3, 4, 5, 6, 7), d0, 0, 0, 0);
    d1 = __builtin_amdgcn_mfma_f32_16x16x32_bf16(a[3], __builtin_shufflevector(b0[3], b1[3], 0, 1, 2, 3, 4, 5, 6, 7), d1, 0, 0, 0);
    aa += 256; ba += 4096;
  }
  return d0 + d1;
}

__device__ __forceinline__ unsigned nosent(unsigned u) { return u == 0xffffffffu ? 0x7fc07fc0u : u; }

__device__ __forceinline__ int opaque(int v) { asm volatile("" : "+s"(v)); return v; }

__global__ void __launch_bounds__(NTHR, 3)
fused_decode(const unsigned short* __restrict__ x, const unsigned short* __restrict__ kc, const unsigned short* __restrict__ vc,
             const unsigned short* __restrict__ wq, const unsigned short* __restrict__ wo, const unsigned short* __restrict__ wg,
             const unsigned short* __restrict__ wu, const unsigned short* __restrict__ wd, unsigned short* __restrict__ out, unsigned char* ws) {
  extern __shared__ __attribute__((aligned(16))) unsigned char lds[];
  LAS unsigned char* L = (LAS unsigned char*)lds;
  const int w = blockIdx.x;
  const int tid = threadIdx.x, lane = tid & 63, wave = __builtin_amdgcn_readfirstlane(tid >> 6);
  const int g = lane >> 4, i = lane & 15;
  const int xc = w & 7, r = w >> 3;
  const int kvh = xc, b = r & 7, sp = r >> 3;
  const __amdgpu_buffer_rsrc_t rsW = __builtin_amdgcn_make_buffer_rsrc(ws, 0, WS_END, 0x00020000);
  const unsigned trash = WS_TRASH + w * 4096;

  if (wave < 4) {
    const int aw = wave;
    const u64 ka = (u64)kc, va = (u64)vc;
    AttnConst c;
    c.rsK = u32x4{(unsigned)ka, (unsigned)(ka >> 32), 128u << 20, 0x00020000u};
    c.rsV = u32x4{(unsigned)va, (unsigned)(va >> 32), 128u << 20, 0x00020000u};
    c.gbase = (unsigned)(((b * HKV + kvh) * SK + sp * (SK / NSPLIT)) * 256) + aw * (ILV ? 4096 : 8192);
    c.ring = L_RING + aw * 32768;
    c.vk0 = g * 256 + ((i ^ g) << 4);
    c.vv0 = g * 256 + ((i ^ (2 * g)) << 4);
    c.kaddr = c.ring + i * 256 + ((i ^ g) << 4);
    const int q4 = i >> 2, pp = lane & 3;
    c.vaddr = c.ring + 8192 + (4 * g + q4) * 256 + ((8 * (g & 1) + 2 * q4) << 4) + 8 * pp;
    __builtin_amdgcn_s_barrier();
    if (aw == 0) STAMP(ws, w, 0);
    ISSUE_K(c, 0, 0);
    AttnState st;
#pragma unroll 1
    for (int pass = opaque(WARM ? 0 : 1); pass < 2; ++pass) {
      if (pass) {
        ISSUE_V(c, 0, 0); ISSUE_K(c, 1, 1); ISSUE_V(c, 1, 1);
        if (aw == 0) STAMP(ws, w, 1);
        lds_wait_ge(L, C_QREADY, 1);
        if (aw == 0) STAMP(ws, w, 2);
      }
#pragma unroll
      for (int s = 0; s < 4; ++s) { u32x4 v = *(LAS u32x4*)(L + L_Q + (i & 3) * 256 + 64 * s + 16 * g); if (i >= 4) v = u32x4{0, 0, 0, 0}; c.qf[s] = __builtin_bit_cast(bf16x8, v); }
#pragma unroll
      for (int d = 0; d < 8; ++d) st.acc[d] = f32x4{0.f, 0.f, 0.f, 0.f};
      st.m = -INFINITY; st.l = 0.f;
      const int nt = pass ? NT_TILES : 2;
#pragma unroll 1
      for (int t = 0; t < nt; t += 2) {
        const int rem = pass ? NT_TILES - 1 - t : -1;
        if (aw == 0 && lane == 0 && pass) lds_st(L, C_PROG, t);
        if (rem == 1) {
          if (lane == 0) lds_st(L, C_ATTEND, 1);
          lds_wait_ge(L, C_WOREAD, 4);
          lds_wait_ge(L, C_CLEAN, 1);
        }
        attn_tile<0>(L, c, st, t, rem); attn_tile<1>(L, c, st, t + 1, pass ? rem - 1 : -1);
      }
      if (aw == 0 && pass) STAMP(ws, w, 3);
      const float lsum = xrow16_sum(st.l);
      const unsigned mbase = pass ? L_STG : L_TRASH, mstr = pass ? MRG_W : 0;
      LAS float* mr = (LAS float*)(L + mbase + aw * mstr);
      if (i < 4) {
#pragma unroll
        for (int d = 0; d < 8; ++d) *(LAS f32x4*)(mr + i * 128 + 16 * d + 4 * g) = st.acc[d];
        if (g == 0) { mr[512 + i] = st.m; mr[516 + i] = lsum; }
      }
      WAIT_LGKM0();
      unsigned old = 0;
      if (lane == 0) old = lds_add(L, pass ? C_MRG : C_MRG0);
      old = __builtin_amdgcn_readfirstlane(old);
      asm volatile("" ::: "memory");
      if (old == (pass ? 3u : 0u)) {
        float M = -INFINITY;
#pragma unroll
        for (int ww = 0; ww < 4; ++ww) M = fmaxf(M, ((LAS float*)(L + mbase + ww * mstr))[512 + g]);
        float Ls = 0.f; f32x4 o0 = {0.f, 0.f, 0.f, 0.f}, o1 = {0.f, 0.f, 0.f, 0.f};
#pragma unroll
        for (int ww = 0; ww < 4; ++ww) { LAS float* rec = (LAS float*)(L + mbase + ww * mstr);
          const float wt = __builtin_amdgcn_exp2f((rec[512 + g] - M) * 1.4426950408889634f);
          Ls = fmaf(wt, rec[516 + g], Ls);
          o0 += wt * *(LAS f32x4*)(rec + g * 128 + 8 * i); o1 += wt * *(LAS f32x4*)(rec + g * 128 + 8 * i + 4); }
        const u32x4 pk = {nosent(cvtpk(o0[0], o0[1])), nosent(cvtpk(o0[2], o0[3])), nosent(cvtpk(o1[0], o1[1])), nosent(cvtpk(o1[2], o1[3]))};
        const unsigned po = pass ? WS_PART + (b * 32 + kvh * 4 + g) * 1024 + sp * 256 + i * 16 : trash + lane * 16;
        const unsigned mo = pass ? WS_ML + (kvh * 4 + g) * 256 + b * 32 + sp * 8 : trash + 1024 + g * 8;
        __builtin_amdgcn_raw_buffer_store_b128(pk, rsW, po, 0, 16);
        if (i == 0) __builtin_amdgcn_raw_buffer_store_b64(u32x2{nosent(__float_as_uint(M)), nosent(__float_as_uint(Ls))}, rsW, mo, 0, 16);
        if (pass) STAMP(ws, w, 4);
      }
    }
    return;
  }

  const int hw = wave - 4;
  if (hw == 0) STAMP(ws, w, 8);
  u32x4 xv = *(const GAS u32x4*)(x + hw * DM + lane * 8);
  u64 E = __hip_atomic_load((GAS u64*)(ws + WS_EPOCH), RLX_AGENT);
  {
    const u64 qa = (u64)wq;
    const u32x4 rsQ = {(unsigned)qa, (unsigned)(qa >> 32), 4u << 20, 0x00020000u};
    const unsigned n0 = xc * 512 + r * 16;
    const unsigned vo = (lane >> 1) * 8192 + (lane & 1) * 16;
    dma1(vo, rsQ, (2 * hw) * 262144u + n0 * 2, L_STG + (2 * hw) * 1024);
    dma1(vo, rsQ, (2 * hw + 1) * 262144u + n0 * 2, L_STG + (2 * hw + 1) * 1024);
  }
  if (hw == 3) { if (lane < 32) lds_st(L, lane, 0u); WAIT_LGKM0(); }
  __builtin_amdgcn_s_barrier();
  unsigned tg_lo = 0, tg_hi = 0;
  u64 Tu = 0;
#pragma unroll 1
  for (int pass = opaque(WARM ? 0 : 1); pass < 2; ++pass) {
    u32x4 xin = {(unsigned)lane, (unsigned)lane, (unsigned)lane, (unsigned)lane};
    if (pass) { asm volatile("" : "+v"(xv), "+v"(E) :: "memory"); xin = xv; }
    {
      float f[8];
#pragma unroll
      for (int k = 0; k < 4; ++k) { f[2 * k] = lo16(xin[k]); f[2 * k + 1] = hi16(xin[k]); }
      float s = ((f[0] + f[1]) + (f[2] + f[3])) + ((f[4] + f[5]) + (f[6] + f[7]));
      s = wave_sum(s);
      const float mean = s * (1.f / DM);
      float q = 0.f;
#pragma unroll
      for (int k = 0; k < 8; ++k) { const float d = f[k] - mean; q = fmaf(d, d, q); }
      q = wave_sum(q);
      const float var = rbf(q * (1.f / DM));
      const float mb = rbf(mean);
      const float sd = rbf(sqrtf(rbf(var + 1e-5f)));
      u32x4 o;
#pragma unroll
      for (int k = 0; k < 4; ++k) o[k] = cvtpk(rbf(f[2 * k] - mb) / sd, rbf(f[2 * k + 1] - mb) / sd);
      *(LAS u32x4*)(L + L_XN + hw * XN_STRIDE + lane * 16) = o;
    }
    if (pass) {
      if (WARM && hw == 0) asm volatile("s_waitcnt vmcnt(2) lgkmcnt(0)" ::: "memory");
      else asm volatile("s_waitcnt vmcnt(0) lgkmcnt(0)" ::: "memory");
      if (lane == 0) lds_add(L, C_LN);
      const u64 T = E + 1;
      const unsigned Tlo = __builtin_amdgcn_readfirstlane((unsigned)T), Thi = __builtin_amdgcn_readfirstlane((unsigned)(T >> 32));
      tg_lo = Tlo << 16; tg_hi = (Thi << 16) | (Tlo >> 16);
      Tu = ((u64)Thi << 32) | Tlo;
    }
    if (hw == 0) {
      if (pass) { lds_wait_ge(L, C_LN, 8); STAMP(ws, w, 9); }
      const f32x4 d = proj16(L, lane);
      const unsigned n0 = xc * 512 + r * 16;
      const unsigned pk = g < 2 ? cvtpk(d[0], d[1]) : cvtpk(d[2], d[3]);
      const int b0 = 4 * (g & 1) + (g < 2 ? 0 : 2);
      const unsigned qo = pass ? WS_QG + (b0 * 4096 + n0 + i) * 8 : trash + 2048 + lane * 8;
      __builtin_amdgcn_raw_buffer_store_b64(u32x2{tg_lo | (pk & 0xffffu), tg_hi}, rsW, qo, 0, 16);
      __builtin_amdgcn_raw_buffer_store_b64(u32x2{tg_lo | (pk >> 16), tg_hi}, rsW, pass ? qo + 32768 : qo + 512, 0, 16);
      if (pass) { if (lane == 0) lds_st(L, C_STGFREE, 1); STAMP(ws, w, 10); }
    }
  }
  const int gam = xc + 8 * (w >> 6), f0 = gam * 64 + (r & 7) * 8;
  if (hw == 1) {
    if (gam < 28) {
      const unsigned short* src = ((lane & 1) ? wu : wg) + (size_t)(lane >> 1) * FF + f0;
      lds_wait_ge(L, C_STGFREE, 1);
      lds_wait_ge(L, C_QREADY, 1);
#pragma unroll 1
      for (int u = 0; u < 16; ++u) { gdma1(src, L_STG + u * 1024); src += (size_t)32 * FF; }
      WAIT_VM(0);
      const f32x4 d = proj16(L, lane);
      float hv[2];
#pragma unroll
      for (int k = 0; k < 2; ++k) {
        const float own = rbf(g < 2 ? d[k] : d[2 + k]);
        const float oth = dpp<XOR8>(own);
        hv[k] = own > 0.f ? own * oth : 0.f;
      }
      const unsigned pk = cvtpk(hv[0], hv[1]);
      const int b0 = 4 * (g & 1) + (g < 2 ? 0 : 2);
      if (i < 8) {
        GAS u64* dst = (GAS u64*)(ws + WS_HG) + b0 * FF + f0 + i;
        __hip_atomic_store(dst, ((u64)tg_hi << 32) | (tg_lo | (pk & 0xffffu)), RLX_AGENT);
        __hip_atomic_store(dst + FF, ((u64)tg_hi << 32) | (tg_lo | (pk >> 16)), RLX_AGENT);
      }
    } else lds_wait_ge(L, C_STGFREE, 1);
    if (lane == 0) lds_st(L, C_HDONE, 1);
    STAMP(ws, w, 17);
  } else if (hw == 2) {
    const unsigned qo = WS_QG + (b * 4096 + kvh * 512) * 8 + lane * 16;
    bool okk[4] = {false, false, false, false};
    lds_wait_ge(L, C_STGFREE, 1);
#pragma unroll 1
    for (unsigned spins = 0; spins < SPIN_CAP; ++spins) {
      asm volatile("" ::: "memory");
      u32x4 v[4];
#pragma unroll
      for (int k = 0; k < 4; ++k) if (!okk[k]) v[k] = __builtin_amdgcn_raw_buffer_load_b128(rsW, qo + k * 1024, 0, 16);
#pragma unroll
      for (int k = 0; k < 4; ++k) if (!okk[k]) {
        okk[k] = ((v[k][0] ^ tg_lo) >> 16) == 0 && v[k][1] == tg_hi && ((v[k][2] ^ tg_lo) >> 16) == 0 && v[k][3] == tg_hi;
        *(LAS unsigned*)(L + L_Q + (lane + 64 * k) * 4) = (v[k][0] & 0xffffu) | (v[k][2] << 16);
      }
      if (__all(okk[0] && okk[1] && okk[2] && okk[3])) break;
      __builtin_amdgcn_s_sleep(1);
    }
    WAIT_LGKM0();
    if (lane == 0) lds_st(L, C_QREADY, 1);
    STAMP(ws, w, 11);
  } else if (hw == 7) {
    lds_wait_ge(L, C_QREADY, 1);
    const u32x4 e = {0xffffffffu, 0xffffffffu, 0xffffffffu, 0xffffffffu};
    __builtin_amdgcn_raw_buffer_store_b128(e, rsW, WS_PART + (b * 32 + kvh * 4 + g) * 1024 + sp * 256 + i * 16, 0, 16);
    if (i == 0) __hip_atomic_store((GAS u64*)(ws + WS_ML + (kvh * 4 + g) * 256 + b * 32 + sp * 8), ~0ull, RLX_AGENT);
#pragma unroll
    for (int u = 0; u < 2; ++u) {
      const unsigned yo = (((i & 7) * 32 + 4 * xc + 2 * u + (i >> 3)) * 32 + r) * 64 + g * 16;
      __builtin_amdgcn_raw_buffer_store_b128(e, rsW, WS_Y + yo, 0, 16);
      __builtin_amdgcn_raw_buffer_store_b128(e, rsW, WS_Y2 + yo, 0, 16);
    }
    WAIT_VM(0);
    if (lane == 0) { __hip_atomic_store((GAS u64*)(ws + WS_FLAG) + xc * 32 + r, Tu, RLX_AGENT); lds_st(L, C_CLEAN, 1); }
  } else if (hw == 4) {
    const u64 a_ = (u64)wo;
    const u32x4 rs = {(unsigned)a_, (unsigned)(a_ >> 32), 4u << 20, 0x00020000u};
    const unsigned vo = (lane >> 3) * 1024 + (lane & 7) * 16;
    lds_wait_ge(L, C_HDONE, 1);
#pragma unroll 1
    for (int u = 0; u < 16; ++u) dma1(vo, rs, (unsigned)((128 * r + 8 * u) * 1024 + xc * 128), L_STG + u * 1024);
    WAIT_VM(0);
    if (lane == 0) lds_st(L, C_WOLD, 1);
  } else if (hw == 5) {
    const u64 a_ = (u64)wd;
    const u32x4 rs = {(unsigned)a_, (unsigned)(a_ >> 32), (unsigned)(FF * DM * 2), 0x00020000u};
    const unsigned vo = (lane >> 3) * 1024 + (lane & 7) * 16;
    lds_wait_ge(L, C_HDONE, 1);
#pragma unroll 1
    for (int u = 0; u < 8; ++u) dma1(vo, rs, (unsigned)((56 * r + 8 * u) * 1024 + xc * 128), L_XN + u * 1024);
    WAIT_VM(0);
    if (lane == 0) lds_st(L, C_WDLD, 1);
  }
  lds_wait_ge(L, C_HDONE, 1);
  const int bf = r >> 2, cb = 4 * xc + (r & 3), n1 = cb * 16;
  {
    const GAS u64* fl = (const GAS u64*)(ws + WS_FLAG) + ((hw == 6 && lane >= 32) ? xc * 32 + (lane & 31) : (r >> 2) * 32 + hw + 8 * (lane & 3));
    const unsigned ho = WS_HG + (hw * FF + 56 * r + (lane < 56 ? lane : 0)) * 8;
    u32x2 hv = {0u, 0u};
    bool okh = false, okf = false;
    while (__builtin_amdgcn_readfirstlane(lds_ld(L, C_PROG)) < GATE_H) __builtin_amdgcn_s_sleep(32);
#pragma unroll 1
    for (unsigned spins = 0; spins < SPIN_CAP; ++spins) {
      asm volatile("" ::: "memory");
      u64 fv = 0;
      if (!okh) hv = __builtin_amdgcn_raw_buffer_load_b64(rsW, ho, 0, 16);
      if (!okf) fv = __hip_atomic_load(fl, RLX_AGENT);
      okh = okh || (((hv[0] ^ tg_lo) >> 16) == 0 && hv[1] == tg_hi);
      okf = okf || fv == Tu;
      if (__all(okh && okf)) break;
      __builtin_amdgcn_s_sleep(SLP_MID);
    }
    *(LAS unsigned short*)(L + L_OA + hw * OA_STRIDE + lane * 2) = lane < 56 ? (unsigned short)(hv[0] & 0xffffu) : (unsigned short)0;
    *(LAS unsigned short*)(L + L_OA + hw * OA_STRIDE + 128 + lane * 2) = 0;
    WAIT_LGKM0();
    if (lane == 0) lds_add(L, C_OG);
  }
  const unsigned ba = (g * 8 + (i >> 2)) * 128 + (lane & 3) * 8 + (hw & 3) * 32;
  const unsigned oa = L_OA + (i & 7) * OA_STRIDE + g * 16;
  const unsigned yo = ((i * 32 + 4 * xc + (hw & 3)) * 32 + r) * 64 + g * 16;
  bf16x8 wf[4];
  float gg[4] = {0.f, 0.f, 0.f, 0.f}, xr[4] = {0.f, 0.f, 0.f, 0.f};
  if (hw < 4) {
    lds_wait_ge(L, C_WDLD, 1);
    lds_wait_ge(L, C_CLEAN, 1);
#pragma unroll
    for (int s = 0; s < 4; ++s) {
      const bf16x4 a0 = __builtin_amdgcn_ds_read_tr16_b64_v4bf16((LAS bf16x4*)(L + L_XN + ba + (s & 1) * 4096));
      const bf16x4 a1 = __builtin_amdgcn_ds_read_tr16_b64_v4bf16((LAS bf16x4*)(L + L_XN + ba + (s & 1) * 4096 + 512));
      wf[s] = __builtin_shufflevector(a0, a1, 0, 1, 2, 3, 4, 5, 6, 7);
    }
  } else if (hw == 6) {
    const u32x2 xq = *(const GAS u32x2*)(x + bf * DM + n1 + (lane & 3) * 4);
    xr[0] = lo16(xq[0]); xr[1] = hi16(xq[0]); xr[2] = lo16(xq[1]); xr[3] = hi16(xq[1]);
  } else if (hw == 5 && w == 0) {
    const GAS u64* fa = (const GAS u64*)(ws + WS_FLAG) + lane;
#pragma unroll 1
    for (unsigned spins = 0; spins < SPIN_CAP; ++spins) {
      bool ok = true;
#pragma unroll
      for (int k = 0; k < 4; ++k) ok &= __hip_atomic_load(fa + 64 * k, RLX_AGENT) == Tu;
      if (__all(ok)) break;
      __builtin_amdgcn_s_sleep(SLP_MID);
    }
    if (lane == 0) __hip_atomic_store((GAS u64*)(ws + WS_EPOCH), Tu, RLX_AGENT);
  }
#pragma unroll 1
  for (int rd = opaque(0); rd < 2; ++rd) {
    const unsigned ybase = rd ? WS_Y : WS_Y2;
    {
      if (rd) {
        lds_wait_ge(L, C_Y2DONE, 4);
        while (__builtin_amdgcn_readfirstlane(lds_ld(L, C_ATTEND)) == 0) __builtin_amdgcn_s_sleep(16);
        if (hw == 0) STAMP(ws, w, 13);
      }
      const unsigned po = WS_PART + (hw * 32 + r) * 1024 + lane * 16;
      u32x4 pv = {0u, 0u, 0u, 0u}; u32x2 ml = {0u, 0u};
      if (hw == 7) {
        bool ok = lane >= 32;
#pragma unroll 1
        for (unsigned spins = 0; spins < SPIN_CAP; ++spins) {
          asm volatile("" ::: "memory");
          if (!ok) {
            ml = __builtin_amdgcn_raw_buffer_load_b64(rsW, WS_ML + r * 256 + lane * 8, 0, 16);
            ok = ml[0] != 0xffffffffu && ml[1] != 0xffffffffu;
            *(LAS u32x2*)(L + L_ML + lane * 8) = ml;
          }
          const unsigned mask = (unsigned)__ballot(ok);
          WAIT_LGKM0();
          if (lane == 0 && rd) lds_st(L, C_MLMASK, mask);
          if (mask == 0xffffffffu || !rd) break;
          __builtin_amdgcn_s_sleep(SLP_PART);
        }
        if (lane == 0 && rd) lds_st(L, C_MLMASK, 0xffffffffu);
      } else if (rd) {
        while (((__builtin_amdgcn_readfirstlane(lds_ld(L, C_MLMASK)) >> (4 * hw)) & 15u) != 15u) __builtin_amdgcn_s_sleep(1);
        asm volatile("" ::: "memory");
      }
      ml = *(LAS u32x2*)(L + L_ML + hw * 32 + g * 8);
      {
        bool ok = false;
#pragma unroll 1
        for (unsigned spins = 0; spins < SPIN_CAP; ++spins) {
          asm volatile("" ::: "memory");
          if (!ok) {
            pv = __builtin_amdgcn_raw_buffer_load_b128(rsW, po, 0, 16);
            ok = pv[0] != 0xffffffffu && pv[1] != 0xffffffffu && pv[2] != 0xffffffffu && pv[3] != 0xffffffffu;
          }
          if (__all(ok) || !rd) break;
          __builtin_amdgcn_s_sleep(1);
        }
      }
      if (hw == 0 && rd) STAMP(ws, w, 14);
      const float mj = __uint_as_float(ml[0]);
      const float M = xrow16_max(mj);
      const float wt = __builtin_amdgcn_exp2f((mj - M) * 1.4426950408889634f);
      const float inv = 1.f / xrow16_sum(wt * __uint_as_float(ml[1]));
      u32x4 op;
#pragma unroll
      for (int k = 0; k < 4; ++k) op[k] = cvtpk(xrow16_sum(wt * lo16(pv[k])) * inv, xrow16_sum(wt * hi16(pv[k])) * inv);
      if (g == 0) *(LAS u32x4*)(L + (rd ? L_OA + hw * OA_STRIDE : L_TRASH) + i * 16) = op;
      WAIT_LGKM0();
      if (lane == 0 && rd) lds_add(L, C_OG);
    }
    if (hw < 4) {
      lds_wait_ge(L, C_OG, rd ? 16 : 8);
      f32x4 d = {0.f, 0.f, 0.f, 0.f};
#pragma unroll
      for (int s = 0; s < 4; ++s) d = __builtin_amdgcn_mfma_f32_16x16x32_bf16(wf[s], *(LAS bf16x8*)(L + oa + s * 64), d, 0, 0, 0);
      const u32x4 pk = {nosent(__float_as_uint(d[0])), nosent(__float_as_uint(d[1])), nosent(__float_as_uint(d[2])), nosent(__float_as_uint(d[3]))};
      if (i < 8) __builtin_amdgcn_raw_buffer_store_b128(pk, rsW, ybase + yo, 0, 16);
      if (hw == 0 && rd) STAMP(ws, w, 16);
      if (!rd) {
        WAIT_LGKM0();
        if (lane == 0) lds_add(L, C_Y2DONE);
        lds_wait_ge(L, C_WOLD, 1);
#pragma unroll
        for (int s = 0; s < 4; ++s) {
          const bf16x4 a0 = __builtin_amdgcn_ds_read_tr16_b64_v4bf16((LAS bf16x4*)(L + L_STG + ba + s * 4096));
          const bf16x4 a1 = __builtin_amdgcn_ds_read_tr16_b64_v4bf16((LAS bf16x4*)(L + L_STG + ba + s * 4096 + 512));
          wf[s] = __builtin_shufflevector(a0, a1, 0, 1, 2, 3, 4, 5, 6, 7);
        }
        asm volatile("s_waitcnt lgkmcnt(0)" : "+v"(wf[0]), "+v"(wf[1]), "+v"(wf[2]), "+v"(wf[3]) :: "memory");
        if (lane == 0) lds_add(L, C_WOREAD);
      }
    } else if (hw == 6) {
      const unsigned io = ybase + (bf * 32 + cb) * 2048 + lane * 16;
      u32x4 y0 = {0u, 0u, 0u, 0u}, y1 = {0u, 0u, 0u, 0u};
      bool ok0 = false, ok1 = false;
      if (!rd) while (__builtin_amdgcn_readfirstlane(lds_ld(L, C_PROG)) < GATE_Y2) __builtin_amdgcn_s_sleep(32);
#pragma unroll 1
      for (unsigned spins = 0; spins < SPIN_CAP; ++spins) {
        asm volatile("" ::: "memory");
        if (!ok0) y0 = __builtin_amdgcn_raw_buffer_load_b128(rsW, io, 0, 16);
        if (!ok1) y1 = __builtin_amdgcn_raw_buffer_load_b128(rsW, io + 1024, 0, 16);
        ok0 = y0[0] != 0xffffffffu && y0[1] != 0xffffffffu && y0[2] != 0xffffffffu && y0[3] != 0xffffffffu;
        ok1 = y1[0] != 0xffffffffu && y1[1] != 0xffffffffu && y1[2] != 0xffffffffu && y1[3] != 0xffffffffu;
        if (__all(ok0 && ok1)) break;
        if (rd) __builtin_amdgcn_s_sleep(SLP_Y); else __builtin_amdgcn_s_sleep(SLP_MID);
      }
      float o[4];
#pragma unroll
      for (int k = 0; k < 4; ++k) {
        float t = __uint_as_float(y0[k]) + __uint_as_float(y1[k]);
        t += dpp<ROR4>(t); t += dpp<XOR8>(t); t = rbf(xrow16_sum(t));
        o[k] = xr[k] + rbf(t * gg[k]);
        gg[k] = rbf(t / (1.f + __expf(-t)));
      }
      GAS unsigned short* dst = rd ? (GAS unsigned short*)out + bf * DM + n1 : (GAS unsigned short*)(ws + trash + 3072);
      if (lane < 4) *(GAS u32x2*)(dst + lane * 4) = u32x2{cvtpk(o[0], o[1]), cvtpk(o[2], o[3])};
      if (rd) STAMP(ws, w, 15); else STAMP(ws, w, 12);
    }
  }
}

extern "C" void kernel_launch(void* const* d_in, const int* in_sizes, int n_in, void* d_out, int out_size, void* d_ws, size_t ws_size, hipStream_t stream) {
  const unsigned short* x = (const unsigned short*)d_in[0];
  const unsigned short* kc = (const unsigned short*)d_in[1];
  const unsigned short* vc = (const unsigned short*)d_in[2];
  const unsigned short* wq = (const unsigned short*)d_in[3];
  const unsigned short* wo = (const unsigned short*)d_in[4];
  const unsigned short* wg = (const unsigned short*)d_in[6];
  const unsigned short* wu = (const unsigned short*)d_in[7];
  const unsigned short* wd = (const unsigned short*)d_in[8];
  (void)hipFuncSetAttribute((const void*)fused_decode, hipFuncAttributeMaxDynamicSharedMemorySize, L_END);
  fused_decode<<<dim3(NWG), dim3(NTHR), L_END, stream>>>(x, kc, vc, wq, wo, wg, wu, wd, (unsigned short*)d_out, (unsigned char*)d_ws);
}
```

```cpp
#include <hip/hip_runtime.h>
#include <hip/hip_bf16.h>
#include <cstdint>

typedef __bf16 bf16x2 __attribute__((ext_vector_type(2)));
typedef __bf16 bf16x4 __attribute__((ext_vector_type(4)));
typedef __bf16 bf16x8 __attribute__((ext_vector_type(8)));
typedef unsigned u32x2 __attribute__((ext_vector_type(2)));
typedef unsigned u32x4 __attribute__((ext_vector_type(4)));
typedef float f32x2 __attribute__((ext_vector_type(2)));
typedef float f32x4 __attribute__((ext_vector_type(4)));
typedef unsigned long long u64;
#define LAS __attribute__((address_space(3)))
#define GAS __attribute__((address_space(1)))
#define RLX_AGENT __ATOMIC_RELAXED, __HIP_MEMORY_SCOPE_AGENT
#define RLX_WG __ATOMIC_RELAXED, __HIP_MEMORY_SCOPE_WORKGROUP

constexpr int NB = 8, DM = 512, HKV = 8, SK = 8192, FF = 1792;
constexpr int NWG = 256, NTHR = 768;
constexpr int NT_TILES = 16;

#define CAPG 6
#define PCAP 2
#define RINGW 4
#define GATE_H 4
#define GATE_Y2 8
#define SLP_PART 4
#define SLP_DIR 2
#define SLP_Y 2
#define SLP_MID 64

constexpr unsigned WS_EPOCH = 0;
constexpr unsigned WS_ML = 8192;
constexpr unsigned WS_QG = 32768;
constexpr unsigned WS_HG = WS_QG + NB * 4096 * 8;
constexpr unsigned WS_PART = WS_HG + NB * FF * 8;
constexpr unsigned WS_Y = WS_PART + NWG * 2048;
constexpr unsigned WS_END = WS_Y + (1u << 20);
constexpr unsigned WS_STAMP = 4u << 20;

constexpr unsigned L_RING = 0;
constexpr unsigned L_STG = 131072;
constexpr unsigned L_XN = L_STG + 16384;
constexpr unsigned XN_STRIDE = 1040;
constexpr unsigned L_Q = L_XN + 8 * XN_STRIDE;
constexpr unsigned L_OA = L_Q + 1024;
constexpr unsigned OA_STRIDE = 272;
constexpr unsigned L_CTL = L_OA + 8 * OA_STRIDE;
constexpr unsigned L_END = L_CTL + 128;
static_assert(L_END <= 163840, "LDS");
constexpr unsigned MRG_W = (512 + 8) * 4;
enum { C_LN = 0, C_STGFREE, C_QREADY, C_HDONE, C_MRG, C_ATTEND, C_TLO, C_THI, C_WOLD, C_Y2DONE, C_WOREAD, C_OG, C_PROG, C_MLMASK, C_RING, C_GUST, C_CLEAN };

__device__ __forceinline__ unsigned cvtpk(float a, float b) {
  bf16x2 t = __builtin_convertvector(f32x2{a, b}, bf16x2);
  return __builtin_bit_cast(unsigned, t);
}
__device__ __forceinline__ float lo16(unsigned u) { return __builtin_bit_cast(float, u << 16); }
__device__ __forceinline__ float hi16(unsigned u) { return __builtin_bit_cast(float, u & 0xffff0000u); }
__device__ __forceinline__ float rbf(float f) { return lo16(cvtpk(f, f)); }
template <int CTRL> __device__ __forceinline__ float dpp(float x) {
  return __builtin_bit_cast(float, __builtin_amdgcn_mov_dpp(__builtin_bit_cast(int, x), CTRL, 0xf, 0xf, true));
}
constexpr int XOR1 = 0xB1, XOR2 = 0x4E, XOR7 = 0x141, XOR8 = 0x128;
__device__ __forceinline__ float xrow16_max(float x) {
  auto s = __builtin_amdgcn_permlane16_swap(__float_as_uint(x), __float_as_uint(x), false, false);
  x = fmaxf(__uint_as_float(s[0]), __uint_as_float(s[1]));
  auto t = __builtin_amdgcn_permlane32_swap(__float_as_uint(x), __float_as_uint(x), false, false);
  return fmaxf(__uint_as_float(t[0]), __uint_as_float(t[1]));
}
__device__ __forceinline__ float xrow16_sum(float x) {
  auto s = __builtin_amdgcn_permlane16_swap(__float_as_uint(x), __float_as_uint(x), false, false);
  x = __uint_as_float(s[0]) + __uint_as_float(s[1]);
  auto t = __builtin_amdgcn_permlane32_swap(__float_as_uint(x), __float_as_uint(x), false, false);
  return __uint_as_float(t[0]) + __uint_as_float(t[1]);
}
__device__ __forceinline__ float wave_sum(float x) {
  x += dpp<XOR1>(x); x += dpp<XOR2>(x); x += dpp<XOR7>(x); x += dpp<XOR8>(x);
  return xrow16_sum(x);
}

__device__ __forceinline__ void dma4(unsigned v0, unsigned v1, unsigned v2, unsigned v3, u32x4 rs, unsigned so, unsigned ldsaddr) {
  unsigned keep;
  asm volatile(
    "s_mov_b32 %0, m0\n\t"
    "s_mov_b32 m0, %7\n\t"
    "s_nop 0\n\t"
    "buffer_load_dwordx4 %1, %5, %6 offen nt lds\n\t"
    "buffer_load_dwordx4 %2, %5, %6 offen offset:1024 nt lds\n\t"
    "buffer_load_dwordx4 %3, %5, %6 offen offset:2048 nt lds\n\t"
    "buffer_load_dwordx4 %4, %5, %6 offen offset:3072 nt lds\n\t"
    "s_mov_b32 m0, %0"
    : "=&s"(keep) : "v"(v0), "v"(v1), "v"(v2), "v"(v3), "s"(rs), "s"(so), "s"(ldsaddr) : "memory");
}
__device__ __forceinline__ void dma1(unsigned v0, u32x4 rs, unsigned so, unsigned ldsaddr) {
  unsigned keep;
  asm volatile(
    "s_mov_b32 %0, m0\n\t"
    "s_mov_b32 m0, %4\n\t"
    "s_nop 0\n\t"
    "buffer_load_dwordx4 %1, %2, %3 offen lds\n\t"
    "s_mov_b32 m0, %0"
    : "=&s"(keep) : "v"(v0), "s"(rs), "s"(so), "s"(ldsaddr) : "memory");
}
#define WAIT_VM(n) asm volatile("s_waitcnt vmcnt(" #n ")" ::: "memory")
#define WAIT_VMI(n) asm volatile("s_waitcnt vmcnt(%0)" :: "i"(n) : "memory")
#define WAIT_LGKM0() asm volatile("s_waitcnt lgkmcnt(0)" ::: "memory")

__device__ __forceinline__ unsigned lds_ld(LAS unsigned char* L, int idx) { return __hip_atomic_load((LAS unsigned*)(L + L_CTL) + idx, RLX_WG); }
__device__ __forceinline__ unsigned lds_ldu(LAS unsigned char* L, int idx) { return (unsigned)__builtin_amdgcn_readfirstlane(lds_ld(L, idx)); }
__device__ __forceinline__ void lds_st(LAS unsigned char* L, int idx, unsigned v) { __hip_atomic_store((LAS unsigned*)(L + L_CTL) + idx, v, RLX_WG); }
__device__ __forceinline__ unsigned lds_add(LAS unsigned char* L, int idx) { return __hip_atomic_fetch_add((LAS unsigned*)(L + L_CTL) + idx, 1u, RLX_WG); }
template <int SLP = 1>
__device__ __forceinline__ void lds_wait_ge(LAS unsigned char* L, int idx, unsigned want) {
  while (lds_ldu(L, idx) < want) __builtin_amdgcn_s_sleep(SLP);
  asm volatile("" ::: "memory");
}
constexpr unsigned SPIN_CAP = 1u << 14;
__device__ __forceinline__ unsigned nosent(unsigned u) { return u == 0xffffffffu ? 0x7fc07fc0u : u; }
__device__ __forceinline__ int opaque(int v) { asm volatile("" : "+s"(v)); return v; }

#define STAMP(ws, w, k) do {} while (0)

struct AttnState { f32x4 acc[8]; float m, l; };
struct AttnConst { u32x4 rsK, rsV; unsigned gbase, ring, vk0, vv0, kaddr, vaddr; bf16x8 qf[4]; };

#define DMA_K(c_, so_, ld_) dma4((c_).vk0, (c_).vk0 ^ 64, (c_).vk0 ^ 128, (c_).vk0 ^ 192, (c_).rsK, (so_), (ld_))
#define DMA_V(c_, so_, ld_) dma4((c_).vv0, (c_).vv0 ^ 128, (c_).vv0, (c_).vv0 ^ 128, (c_).rsV, (so_), (ld_))

template <int SLOT>
__device__ __forceinline__ void attn_tile(LAS unsigned char* L, const AttnConst& c, AttnState& st, const int t, const int rem) {
  constexpr float SCALE = 0.088388347648318440f, LOG2E = 1.4426950408889634f;
  const unsigned so2 = c.gbase + (unsigned)(t + 2) * 32768u, ld2 = c.ring + SLOT * 16384;
  if (rem >= 1) WAIT_VM(24); else WAIT_VM(8);
  f32x4 s0 = {0.f, 0.f, 0.f, 0.f}, s1 = {0.f, 0.f, 0.f, 0.f};
#pragma unroll
  for (int s = 0; s < 4; ++s) {
    const bf16x8 k0 = *(LAS bf16x8*)(L + (c.kaddr ^ (s << 6)) + SLOT * 16384);
    const bf16x8 k1 = *(LAS bf16x8*)(L + (c.kaddr ^ (s << 6)) + SLOT * 16384 + 4096);
    s0 = __builtin_amdgcn_mfma_f32_16x16x32_bf16(k0, c.qf[s], s0, 0, 0, 0);
    s1 = __builtin_amdgcn_mfma_f32_16x16x32_bf16(k1, c.qf[s], s1, 0, 0, 0);
  }
  WAIT_LGKM0();
  if (rem >= 2) { WAIT_VMI(CAPG); DMA_K(c, so2, ld2); WAIT_VMI(CAPG); DMA_K(c, so2 + 16384u, ld2 + 4096); }
  float x[8];
  { unsigned u;
    u = cvtpk(s0[0], s0[1]); u = cvtpk(lo16(u) * SCALE, hi16(u) * SCALE); x[0] = lo16(u); x[1] = hi16(u);
    u = cvtpk(s0[2], s0[3]); u = cvtpk(lo16(u) * SCALE, hi16(u) * SCALE); x[2] = lo16(u); x[3] = hi16(u);
    u = cvtpk(s1[0], s1[1]); u = cvtpk(lo16(u) * SCALE, hi16(u) * SCALE); x[4] = lo16(u); x[5] = hi16(u);
    u = cvtpk(s1[2], s1[3]); u = cvtpk(lo16(u) * SCALE, hi16(u) * SCALE); x[6] = lo16(u); x[7] = hi16(u); }
  float pm = fmaxf(fmaxf(fmaxf(x[0], x[1]), fmaxf(x[2], x[3])), fmaxf(fmaxf(x[4], x[5]), fmaxf(x[6], x[7])));
  pm = xrow16_max(pm);
  if (__any(pm > st.m)) {
    const float mn = fmaxf(st.m, pm);
    const float alpha = __builtin_amdgcn_exp2f((st.m - mn) * LOG2E);
    st.l *= alpha; st.m = mn;
#pragma unroll
    for (int d = 0; d < 8; ++d) st.acc[d] *= alpha;
  }
  const float mb = st.m * LOG2E;
  float p[8];
#pragma unroll
  for (int e = 0; e < 8; ++e) p[e] = __builtin_amdgcn_exp2f(fmaf(x[e], LOG2E, -mb));
  st.l += ((p[0] + p[1]) + (p[2] + p[3])) + ((p[4] + p[5]) + (p[6] + p[7]));
  const u32x4 pw = {cvtpk(p[0], p[1]), cvtpk(p[2], p[3]), cvtpk(p[4], p[5]), cvtpk(p[6], p[7])};
  const bf16x8 pf = __builtin_bit_cast(bf16x8, pw);
  if (rem >= 2) WAIT_VM(24); else if (rem == 1) WAIT_VM(16); else WAIT_VM(0);
#pragma unroll
  for (int d = 0; d < 8; ++d) {
    const bf16x4 v0 = __builtin_amdgcn_ds_read_tr16_b64_v4bf16((LAS bf16x4*)(L + (c.vaddr ^ (d << 5)) + SLOT * 16384));
    const bf16x4 v1 = __builtin_amdgcn_ds_read_tr16_b64_v4bf16((LAS bf16x4*)(L + (c.vaddr ^ (d << 5)) + SLOT * 16384 + 4096));
    const bf16x8 vf = __builtin_shufflevector(v0, v1, 0, 1, 2, 3, 4, 5, 6, 7);
    st.acc[d] = __builtin_amdgcn_mfma_f32_16x16x32_bf16(vf, pf, st.acc[d], 0, 0, 0);
  }
  WAIT_LGKM0();
  if (rem >= 2) { WAIT_VMI(CAPG); DMA_V(c, so2, ld2 + 8192); WAIT_VMI(CAPG); DMA_V(c, so2 + 16384u, ld2 + 8192 + 4096); }
}

template <int TR>
__device__ __forceinline__ f32x4 proj16(LAS unsigned char* L, int lane) {
  const int g = lane >> 4, i = lane & 15;
  unsigned aa = L_XN + (i & 7) * XN_STRIDE + g * 16;
  unsigned ba = L_STG + g * 256 + i * 8;
  f32x4 d0 = {0.f, 0.f, 0.f, 0.f}, d1 = {0.f, 0.f, 0.f, 0.f};
#pragma unroll 1
  for (int s = 0; s < 4; ++s) {
    bf16x8 a[4], bb[4];
#pragma unroll
    for (int k = 0; k < 4; ++k) {
      a[k] = *(LAS bf16x8*)(L + aa + k * 64);
      const bf16x4 b0 = __builtin_amdgcn_ds_read_tr16_b64_v4bf16((LAS bf16x4*)(L + ba + k * 1024));
      const bf16x4 b1 = __builtin_amdgcn_ds_read_tr16_b64_v4bf16((LAS bf16x4*)(L + ba + k * 1024 + 128));
      bb[k] = __builtin_shufflevector(b0, b1, 0, 1, 2, 3, 4, 5, 6, 7);
    }
    if (TR) {
      d0 = __builtin_amdgcn_mfma_f32_16x16x32_bf16(bb[0], a[0], d0, 0, 0, 0); d1 = __builtin_amdgcn_mfma_f32_16x16x32_bf16(bb[1], a[1], d1, 0, 0, 0);
      d0 = __builtin_amdgcn_mfma_f32_16x16x32_bf16(bb[2], a[2], d0, 0, 0, 0); d1 = __builtin_amdgcn_mfma_f32_16x16x32_bf16(bb[3], a[3], d1, 0, 0, 0);
    } else {
      d0 = __builtin_amdgcn_mfma_f32_16x16x32_bf16(a[0], bb[0], d0, 0, 0, 0); d1 = __builtin_amdgcn_mfma_f32_16x16x32_bf16(a[1], bb[1], d1, 0, 0, 0);
      d0 = __builtin_amdgcn_mfma_f32_16x16x32_bf16(a[2], bb[2], d0, 0, 0, 0); d1 = __builtin_amdgcn_mfma_f32_16x16x32_bf16(a[3], bb[3], d1, 0, 0, 0);
    }
    aa += 256; ba += 4096;
  }
  return d0 + d1;
}

__global__ void __launch_bounds__(NTHR, 3)
fused_decode(const unsigned short* __restrict__ x, const unsigned short* __restrict__ kc, const unsigned short* __restrict__ vc,
             const unsigned short* __restrict__ wq, const unsigned short* __restrict__ wo, const unsigned short* __restrict__ wg,
             const unsigned short* __restrict__ wu, const unsigned short* __restrict__ wd, unsigned short* __restrict__ out, unsigned char* ws) {
  extern __shared__ __attribute__((aligned(16))) unsigned char lds[];
  LAS unsigned char* L = (LAS unsigned char*)lds;
  const int w = blockIdx.x;
  const int tid = threadIdx.x, lane = tid & 63, wave = __builtin_amdgcn_readfirstlane(tid >> 6);
  const int g = lane >> 4, i = lane & 15;
  const int xc = w & 7, r = w >> 3;
  const int kvh = xc, b = r & 7, sp = r >> 3;
  const __amdgpu_buffer_rsrc_t rsW = __builtin_amdgcn_make_buffer_rsrc(ws, 0, WS_END, 0x00020000);

  if (wave < 4) {
    const int aw = wave;
    const u64 ka = (u64)kc, va = (u64)vc;
    AttnConst c;
    c.rsK = u32x4{(unsigned)ka, (unsigned)(ka >> 32), 128u << 20, 0x00020000u};
    c.rsV = u32x4{(unsigned)va, (unsigned)(va >> 32), 128u << 20, 0x00020000u};
    c.gbase = (unsigned)(((b * HKV + kvh) * SK + sp * 2048) * 256) + aw * 4096;
    c.ring = L_RING + aw * 32768;
    c.vk0 = g * 256 + ((i ^ g) << 4);
    c.vv0 = g * 256 + ((i ^ (2 * g)) << 4);
    c.kaddr = c.ring + i * 256 + ((i ^ g) << 4);
    const int q4 = i >> 2, pp = lane & 3;
    c.vaddr = c.ring + 8192 + (4 * g + q4) * 256 + ((8 * (g & 1) + 2 * q4) << 4) + 8 * pp;
    __builtin_amdgcn_s_barrier();
    if (aw == 0) STAMP(ws, w, 0);
#define PCAPW() do { if (lds_ldu(L, C_QREADY)) WAIT_VMI(CAPG); else WAIT_VMI(PCAP); } while (0)
#pragma unroll 1
    for (int u = 0; u < 2; ++u) {
      const unsigned so = c.gbase + u * 32768u, ld = c.ring + u * 16384;
      PCAPW(); DMA_K(c, so, ld);
      PCAPW(); DMA_K(c, so + 16384u, ld + 4096);
      PCAPW(); DMA_V(c, so, ld + 8192);
      if (u == 1 && lane == 0) lds_add(L, C_RING);
      PCAPW(); DMA_V(c, so + 16384u, ld + 8192 + 4096);
    }
    if (aw == 0) STAMP(ws, w, 1);
    lds_wait_ge(L, C_QREADY, 1);
    if (aw == 0) STAMP(ws, w, 2);
#pragma unroll
    for (int s = 0; s < 4; ++s) { u32x4 v = *(LAS u32x4*)(L + L_Q + (i & 3) * 256 + 64 * s + 16 * g); if (i >= 4) v = u32x4{0, 0, 0, 0}; c.qf[s] = __builtin_bit_cast(bf16x8, v); }
    AttnState st;
#pragma unroll
    for (int d = 0; d < 8; ++d) st.acc[d] = f32x4{0.f, 0.f, 0.f, 0.f};
    st.m = -INFINITY; st.l = 0.f;
#pragma unroll 1
    for (int t = 0; t < NT_TILES; t += 2) {
      if (aw == 0 && lane == 0) lds_st(L, C_PROG, t);
      if (t == NT_TILES - 2) { if (lane == 0) lds_st(L, C_ATTEND, 1); lds_wait_ge(L, C_WOREAD, 4); }
      attn_tile<0>(L, c, st, t, NT_TILES - 1 - t);
      attn_tile<1>(L, c, st, t + 1, NT_TILES - 2 - t);
    }
    if (aw == 0) STAMP(ws, w, 3);
    const float lsum = xrow16_sum(st.l);
    LAS float* mr = (LAS float*)(L + L_STG + aw * MRG_W);
    if (i < 4) {
#pragma unroll
      for (int d = 0; d < 8; ++d) *(LAS f32x4*)(mr + i * 128 + 16 * d + 4 * g) = st.acc[d];
      if (g == 0) { mr[512 + i] = st.m; mr[516 + i] = lsum; }
    }
    WAIT_LGKM0();
    unsigned old = 0;
    if (lane == 0) old = lds_add(L, C_MRG);
    old = __builtin_amdgcn_readfirstlane(old);
    asm volatile("" ::: "memory");
    if (old == 3u) {
      const unsigned T32 = lds_ldu(L, C_TLO);
      float M = -INFINITY;
#pragma unroll
      for (int ww = 0; ww < 4; ++ww) M = fmaxf(M, ((LAS float*)(L + L_STG + ww * MRG_W))[512 + g]);
      float Ls = 0.f; f32x4 o0 = {0.f, 0.f, 0.f, 0.f}, o1 = {0.f, 0.f, 0.f, 0.f};
#pragma unroll
      for (int ww = 0; ww < 4; ++ww) { LAS float* rec = (LAS float*)(L + L_STG + ww * MRG_W);
        const float wt = __builtin_amdgcn_exp2f((rec[512 + g] - M) * 1.4426950408889634f);
        Ls = fmaf(wt, rec[516 + g], Ls);
        o0 += wt * *(LAS f32x4*)(rec + g * 128 + 4 * i); o1 += wt * *(LAS f32x4*)(rec + g * 128 + 64 + 4 * i); }
      const unsigned po = WS_PART + ((b * 32 + kvh * 4 + g) * 4 + sp) * 512 + i * 16;
      __builtin_amdgcn_raw_buffer_store_b128(u32x4{cvtpk(o0[0], o0[1]), T32, cvtpk(o0[2], o0[3]), T32}, rsW, po, 0, 16);
      __builtin_amdgcn_raw_buffer_store_b128(u32x4{cvtpk(o1[0], o1[1]), T32, cvtpk(o1[2], o1[3]), T32}, rsW, po + 256, 0, 16);
      if (i == 0) __builtin_amdgcn_raw_buffer_store_b128(u32x4{__float_as_uint(M), T32, __float_as_uint(Ls), T32}, rsW, WS_ML + (((kvh * 4 + g) * 8 + b) * 4 + sp) * 16, 0, 16);
      STAMP(ws, w, 4);
    }
    return;
  }

  const int hw = wave - 4;
  if (hw == 0) STAMP(ws, w, 8);
  u32x4 xv = *(const GAS u32x4*)(x + hw * DM + lane * 8);
  u64 E = 0;
  if (hw == 0) asm volatile("global_load_dwordx2 %0, %1, %2 sc1" : "=v"(E) : "v"(0u), "s"(ws) : "memory");
  {
    const u64 qa = (u64)wq;
    const u32x4 rsQ = {(unsigned)qa, (unsigned)(qa >> 32), 4u << 20, 0x00020000u};
    const unsigned n0 = xc * 512 + r * 16;
    const unsigned vo = (lane >> 1) * 8192 + (lane & 1) * 16;
    dma1(vo, rsQ, (2 * hw) * 262144u + n0 * 2, L_STG + (2 * hw) * 1024);
    dma1(vo, rsQ, (2 * hw + 1) * 262144u + n0 * 2, L_STG + (2 * hw + 1) * 1024);
  }
  if (hw == 3) {
    if (lane < 32) lds_st(L, lane, 0u);
    WAIT_LGKM0();
  }
  __builtin_amdgcn_s_barrier();
  asm volatile("" : "+v"(xv) :: "memory");
  {
    float f[8];
#pragma unroll
    for (int k = 0; k < 4; ++k) { f[2 * k] = lo16(xv[k]); f[2 * k + 1] = hi16(xv[k]); }
    float s = ((f[0] + f[1]) + (f[2] + f[3])) + ((f[4] + f[5]) + (f[6] + f[7]));
    s = wave_sum(s);
    const float mean = s * (1.f / DM);
    float q = 0.f;
#pragma unroll
    for (int k = 0; k < 8; ++k) { const float d = f[k] - mean; q = fmaf(d, d, q); }
    q = wave_sum(q);
    const float var = rbf(q * (1.f / DM));
    const float mb = rbf(mean);
    const float sd = rbf(sqrtf(rbf(var + 1e-5f)));
    u32x4 o;
#pragma unroll
    for (int k = 0; k < 4; ++k) o[k] = cvtpk(rbf(f[2 * k] - mb) / sd, rbf(f[2 * k + 1] - mb) / sd);
    *(LAS u32x4*)(L + L_XN + hw * XN_STRIDE + lane * 16) = o;
  }
  asm volatile("s_waitcnt vmcnt(0)" : "+v"(E) :: "memory");
  if (hw == 0 && lane == 0) { const u64 T = E + 1; lds_st(L, C_TLO, (unsigned)T); lds_st(L, C_THI, (unsigned)(T >> 32)); }
  WAIT_LGKM0();
  if (lane == 0) lds_add(L, C_LN);
  if (hw == 0) lds_wait_ge<1>(L, C_LN, 8); else lds_wait_ge<6>(L, C_LN, 8);
  const unsigned Tlo = lds_ldu(L, C_TLO), Thi = lds_ldu(L, C_THI);
  const unsigned tg_lo = Tlo << 16, tg_hi = (Thi << 16) | (Tlo >> 16);
  const u64 Tu = ((u64)Thi << 32) | Tlo;
#define TAG_OK(lo_, hi_) ((((lo_) ^ tg_lo) >> 16) == 0 && (hi_) == tg_hi)
  const int gam = xc + 8 * sp, f0 = gam * 64 + (r & 7) * 8;
  const int hd = 4 * xc + sp, cbk = r & 7;

  if (hw == 0) {
    STAMP(ws, w, 9);
    const f32x4 d = proj16<1>(L, lane);
    const unsigned pk = i < 8 ? cvtpk(d[0], d[1]) : cvtpk(d[2], d[3]);
    __builtin_amdgcn_raw_buffer_store_b128(u32x4{tg_lo | (pk & 0xffffu), tg_hi, tg_lo | (pk >> 16), tg_hi}, rsW,
                                           WS_QG + ((i & 7) * 4096 + xc * 512 + r * 16 + 4 * g + 2 * (i >> 3)) * 8, 0, 16);
    if (lane == 0) lds_st(L, C_STGFREE, 1);
    STAMP(ws, w, 10);
  } else if (hw == 1) {
    lds_wait_ge<8>(L, C_GUST, 4);
    if (gam < 28) {
      const f32x4 d = proj16<0>(L, lane);
      float hv[2];
#pragma unroll
      for (int k = 0; k < 2; ++k) {
        const float own = rbf(g < 2 ? d[k] : d[2 + k]);
        const float oth = dpp<XOR8>(own);
        hv[k] = own > 0.f ? own * oth : 0.f;
      }
      const unsigned pk = cvtpk(hv[0], hv[1]);
      const int b0 = 4 * (g & 1) + (g < 2 ? 0 : 2);
      if (i < 8) __builtin_amdgcn_raw_buffer_store_b128(u32x4{tg_lo | (pk & 0xffffu), tg_hi, tg_lo | (pk >> 16), tg_hi}, rsW, WS_HG + ((f0 + i) * 8 + b0) * 8, 0, 16);
    }
    if (lane == 0) lds_st(L, C_HDONE, 1);
    STAMP(ws, w, 17);
  } else if (hw == 2) {
    const unsigned qo = WS_QG + (b * 4096 + kvh * 512) * 8 + lane * 16;
    bool okk[4] = {false, false, false, false};
    lds_wait_ge<4>(L, C_STGFREE, 1);
#pragma unroll 1
    for (unsigned spins = 0; spins < SPIN_CAP; ++spins) {
      asm volatile("" ::: "memory");
      u32x4 v[4];
#pragma unroll
      for (int k = 0; k < 4; ++k) if (!okk[k]) v[k] = __builtin_amdgcn_raw_buffer_load_b128(rsW, qo + k * 1024, 0, 16);
#pragma unroll
      for (int k = 0; k < 4; ++k) if (!okk[k]) {
        okk[k] = TAG_OK(v[k][0], v[k][1]) && TAG_OK(v[k][2], v[k][3]);
        *(LAS unsigned*)(L + L_Q + (lane + 64 * k) * 4) = (v[k][0] & 0xffffu) | (v[k][2] << 16);
      }
      if (__all(okk[0] && okk[1] && okk[2] && okk[3])) break;
      __builtin_amdgcn_s_sleep(1);
    }
    WAIT_LGKM0();
    if (lane == 0) lds_st(L, C_QREADY, 1);
    STAMP(ws, w, 11);
  } else if (hw == 3) {
  } else {
    const int h4 = hw - 4;
    const __amdgpu_buffer_rsrc_t rsD = __builtin_amdgcn_make_buffer_rsrc((void*)wd, 0, FF * DM * 2, 0x00020000);
    const GAS unsigned short* sgu = (const GAS unsigned short*)((lane & 1) ? wu : wg) + (size_t)(128 * h4 + (lane >> 1)) * FF + (gam < 28 ? f0 : 0);
    const GAS unsigned short* swo = (const GAS unsigned short*)wo + (size_t)(128 * hd + 32 * h4 + (lane >> 3)) * DM + cbk * 64 + (lane & 7) * 8;
    u32x4 rg[4], ro[4], rd_[2];
    lds_wait_ge<8>(L, C_RING, RINGW);
#pragma unroll
    for (int u = 0; u < 4; ++u) rg[u] = *(const GAS u32x4*)(sgu + (size_t)u * 32 * FF);
#pragma unroll
    for (int u = 0; u < 4; ++u) ro[u] = __builtin_nontemporal_load((const GAS u32x4*)(swo + (size_t)u * 8 * DM));
#pragma unroll
    for (int u = 0; u < 2; ++u) rd_[u] = __builtin_amdgcn_raw_buffer_load_b128(rsD, (unsigned)((56 * hd + 16 * h4 + 8 * u + (lane >> 3)) * 1024 + cbk * 128 + (lane & 7) * 16), 0, 2);
    lds_wait_ge(L, C_STGFREE, 1);
#pragma unroll
    for (int u = 0; u < 4; ++u) *(LAS u32x4*)(L + L_STG + (4 * h4 + u) * 1024 + lane * 16) = rg[u];
    WAIT_LGKM0();
    if (lane == 0) lds_add(L, C_GUST);
    lds_wait_ge<4>(L, C_HDONE, 1);
#pragma unroll
    for (int u = 0; u < 4; ++u) *(LAS u32x4*)(L + L_STG + (4 * h4 + u) * 1024 + lane * 16) = ro[u];
#pragma unroll
    for (int u = 0; u < 2; ++u) *(LAS u32x4*)(L + L_XN + (2 * h4 + u) * 1024 + lane * 16) = rd_[u];
    WAIT_LGKM0();
    if (lane == 0) lds_add(L, C_WOLD);
  }
  {
    const int fl = 7 * hw + (lane >> 3), bb = lane & 7;
    const unsigned ho = WS_HG + ((56 * hd + 7 * hw) * 8 + (lane < 56 ? lane : 0)) * 8;
    u32x2 hv = {0u, 0u};
    bool ok = false;
    lds_wait_ge<32>(L, C_PROG, GATE_H);
#pragma unroll 1
    for (unsigned spins = 0; spins < SPIN_CAP; ++spins) {
      asm volatile("" ::: "memory");
      if (!ok) { hv = __builtin_amdgcn_raw_buffer_load_b64(rsW, ho, 0, 16); ok = TAG_OK(hv[0], hv[1]); }
      if (__all(ok)) break;
      __builtin_amdgcn_s_sleep(SLP_MID);
    }
    if (lane < 56) *(LAS unsigned short*)(L + L_OA + bb * OA_STRIDE + fl * 2) = (unsigned short)(hv[0] & 0xffffu);
    if (lane < 4) *(LAS unsigned*)(L + L_OA + hw * OA_STRIDE + 112 + lane * 4) = 0u;
    WAIT_LGKM0();
    if (lane == 0) lds_add(L, C_OG);
  }
  const int bf = r >> 2, cb = 4 * xc + (r & 3), n1 = cb * 16;
  const unsigned ba = (g * 8 + (i >> 2)) * 128 + (lane & 3) * 8 + (hw & 3) * 32;
  const unsigned oa = L_OA + (i & 7) * OA_STRIDE + g * 16;
  bf16x8 wf[4];
  unsigned y2p = 0;
  float xr[2] = {0.f, 0.f};
  if (hw < 4) {
    lds_wait_ge<8>(L, C_WOLD, 4);
    lds_wait_ge<4>(L, C_OG, 8);
    f32x4 d = {0.f, 0.f, 0.f, 0.f};
#pragma unroll
    for (int s = 0; s < 2; ++s) {
      const bf16x4 a0 = __builtin_amdgcn_ds_read_tr16_b64_v4bf16((LAS bf16x4*)(L + L_XN + ba + s * 4096));
      const bf16x4 a1 = __builtin_amdgcn_ds_read_tr16_b64_v4bf16((LAS bf16x4*)(L + L_XN + ba + s * 4096 + 512));
      d = __builtin_amdgcn_mfma_f32_16x16x32_bf16(__builtin_shufflevector(a0, a1, 0, 1, 2, 3, 4, 5, 6, 7), *(LAS bf16x8*)(L + oa + s * 64), d, 0, 0, 0);
    }
    y2p = i < 8 ? cvtpk(d[0], d[1]) : cvtpk(d[2], d[3]);
#pragma unroll
    for (int s = 0; s < 4; ++s) {
      const bf16x4 a0 = __builtin_amdgcn_ds_read_tr16_b64_v4bf16((LAS bf16x4*)(L + L_STG + ba + s * 4096));
      const bf16x4 a1 = __builtin_amdgcn_ds_read_tr16_b64_v4bf16((LAS bf16x4*)(L + L_STG + ba + s * 4096 + 512));
      wf[s] = __builtin_shufflevector(a0, a1, 0, 1, 2, 3, 4, 5, 6, 7);
    }
    asm volatile("s_waitcnt lgkmcnt(0)" : "+v"(wf[0]), "+v"(wf[1]), "+v"(wf[2]), "+v"(wf[3]), "+v"(y2p) :: "memory");
    if (lane == 0) lds_add(L, C_WOREAD);
    if (hw == 0) STAMP(ws, w, 12);
  } else if (hw == 6) {
    const unsigned xq = *(const GAS unsigned*)(x + bf * DM + n1 + (lane & 7) * 2);
    xr[0] = lo16(xq); xr[1] = hi16(xq);
  } else if (hw == 5 && w == 0) {
#pragma unroll 1
    for (unsigned spins = 0; spins < SPIN_CAP; ++spins) {
      bool ok = true;
#pragma unroll
      for (int k = 0; k < 4; ++k) { const u32x2 v = __builtin_amdgcn_raw_buffer_load_b64(rsW, WS_QG + (lane + 64 * k) * 128, 0, 16); ok &= TAG_OK(v[0], v[1]); }
      if (__all(ok)) break;
      __builtin_amdgcn_s_sleep(SLP_MID);
      asm volatile("" ::: "memory");
    }
    if (lane == 0) __hip_atomic_store((GAS u64*)(ws + WS_EPOCH), Tu, RLX_AGENT);
  }
  {
    lds_wait_ge<4>(L, C_WOREAD, 4);
    lds_wait_ge<16>(L, C_ATTEND, 1);
    if (hw == 0) STAMP(ws, w, 13);
    const unsigned po = WS_PART + ((hw * 32 + hd) * 4 + g) * 512 + i * 16;
    const unsigned mo = WS_ML + ((hd * 8 + hw) * 4 + g) * 16;
    u32x4 pv0 = {0u, 0u, 0u, 0u}, pv1 = {0u, 0u, 0u, 0u}, ml = {0u, 0u, 0u, 0u};
    if (hw == 7) {
      bool ok = lane >= 32;
#pragma unroll 1
      for (unsigned spins = 0; spins < SPIN_CAP; ++spins) {
        asm volatile("" ::: "memory");
        if (!ok) {
          ml = __builtin_amdgcn_raw_buffer_load_b128(rsW, WS_ML + hd * 512 + lane * 16, 0, 16);
          ok = ml[1] == Tlo && ml[3] == Tlo;
        }
        const unsigned miss = ~(unsigned)__ballot(ok);
        if (lane == 0) lds_st(L, C_MLMASK, ~miss);
        const unsigned two = (miss & (miss >> 1) & 0x77777777u) | (miss & (miss >> 2) & 0x33333333u) | (miss & (miss >> 3) & 0x11111111u);
        if (two == 0u) break;
        __builtin_amdgcn_s_sleep(SLP_PART);
      }
      if (lane == 0) lds_st(L, C_MLMASK, 0xffffffffu);
    } else {
      while (__builtin_popcount((lds_ldu(L, C_MLMASK) >> (4 * hw)) & 15u) < 3) __builtin_amdgcn_s_sleep(1);
      asm volatile("" ::: "memory");
    }
    bool ok = false;
#pragma unroll 1
    for (unsigned spins = 0; spins < SPIN_CAP; ++spins) {
      asm volatile("" ::: "memory");
      if (!ok) {
        pv0 = __builtin_amdgcn_raw_buffer_load_b128(rsW, po, 0, 16);
        pv1 = __builtin_amdgcn_raw_buffer_load_b128(rsW, po + 256, 0, 16);
        ml = __builtin_amdgcn_raw_buffer_load_b128(rsW, mo, 0, 16);
        ok = pv0[1] == Tlo && pv0[3] == Tlo && pv1[1] == Tlo && pv1[3] == Tlo && ml[1] == Tlo && ml[3] == Tlo;
      }
      if (__all(ok)) break;
      __builtin_amdgcn_s_sleep(SLP_DIR);
    }
    if (hw == 0) STAMP(ws, w, 14);
    const float mj = __uint_as_float(ml[0]);
    const float M = xrow16_max(mj);
    const float wt = __builtin_amdgcn_exp2f((mj - M) * 1.4426950408889634f);
    const float inv = 1.f / xrow16_sum(wt * __uint_as_float(ml[2]));
    const u32x2 oa0 = {cvtpk(xrow16_sum(wt * lo16(pv0[0])) * inv, xrow16_sum(wt * hi16(pv0[0])) * inv), cvtpk(xrow16_sum(wt * lo16(pv0[2])) * inv, xrow16_sum(wt * hi16(pv0[2])) * inv)};
    const u32x2 oa1 = {cvtpk(xrow16_sum(wt * lo16(pv1[0])) * inv, xrow16_sum(wt * hi16(pv1[0])) * inv), cvtpk(xrow16_sum(wt * lo16(pv1[2])) * inv, xrow16_sum(wt * hi16(pv1[2])) * inv)};
    if (g == 0) { *(LAS u32x2*)(L + L_OA + hw * OA_STRIDE + i * 8) = oa0; *(LAS u32x2*)(L + L_OA + hw * OA_STRIDE + 128 + i * 8) = oa1; }
    WAIT_LGKM0();
    if (lane == 0) lds_add(L, C_OG);
  }
  if (hw < 4) {
    lds_wait_ge(L, C_OG, 16);
    f32x4 d = {0.f, 0.f, 0.f, 0.f};
#pragma unroll
    for (int s = 0; s < 4; ++s) d = __builtin_amdgcn_mfma_f32_16x16x32_bf16(wf[s], *(LAS bf16x8*)(L + oa + s * 64), d, 0, 0, 0);
    const unsigned yp = i < 8 ? cvtpk(d[0], d[1]) : cvtpk(d[2], d[3]);
    const unsigned yo = ((((i & 7) * 32 + 4 * cbk + hw) * 32 + hd) * 16 + 4 * g + 2 * (i >> 3)) * 8;
    __builtin_amdgcn_raw_buffer_store_b128(u32x4{(yp & 0xffffu) | (y2p << 16), Tlo, (yp >> 16) | (y2p & 0xffff0000u), Tlo}, rsW, WS_Y + yo, 0, 16);
    if (hw == 0) STAMP(ws, w, 16);
  } else if (hw == 6) {
    const unsigned io = WS_Y + (bf * 32 + cb) * 4096 + lane * 16;
    u32x4 y[4];
    bool okk[4] = {false, false, false, false};
#pragma unroll 1
    for (unsigned spins = 0; spins < SPIN_CAP; ++spins) {
      asm volatile("" ::: "memory");
#pragma unroll
      for (int k = 0; k < 4; ++k) if (!okk[k]) y[k] = __builtin_amdgcn_raw_buffer_load_b128(rsW, io + k * 1024, 0, 16);
#pragma unroll
      for (int k = 0; k < 4; ++k) okk[k] = y[k][1] == Tlo && y[k][3] == Tlo;
      if (__all(okk[0] && okk[1] && okk[2] && okk[3])) break;
      __builtin_amdgcn_s_sleep(SLP_Y);
    }
    float o[2];
#pragma unroll
    for (int k = 0; k < 2; ++k) {
      float t = (lo16(y[0][2 * k]) + lo16(y[1][2 * k])) + (lo16(y[2][2 * k]) + lo16(y[3][2 * k]));
      float u = (hi16(y[0][2 * k]) + hi16(y[1][2 * k])) + (hi16(y[2][2 * k]) + hi16(y[3][2 * k]));
      t += dpp<XOR8>(t); t = rbf(xrow16_sum(t));
      u += dpp<XOR8>(u); u = rbf(xrow16_sum(u));
      const float gate = rbf(u / (1.f + __expf(-u)));
      o[k] = xr[k] + rbf(t * gate);
    }
    if (lane < 8) *(GAS unsigned*)(out + bf * DM + n1 + lane * 2) = cvtpk(o[0], o[1]);
    STAMP(ws, w, 15);
  }
}

extern "C" void kernel_launch(void* const* d_in, const int* in_sizes, int n_in, void* d_out, int out_size, void* d_ws, size_t ws_size, hipStream_t stream) {
  const unsigned short* x = (const unsigned short*)d_in[0];
  const unsigned short* kc = (const unsigned short*)d_in[1];
  const unsigned short* vc = (const unsigned short*)d_in[2];
  const unsigned short* wq = (const unsigned short*)d_in[3];
  const unsigned short* wo = (const unsigned short*)d_in[4];
  const unsigned short* wg = (const unsigned short*)d_in[6];
  const unsigned short* wu = (const unsigned short*)d_in[7];
  const unsigned short* wd = (const unsigned short*)d_in[8];
  (void)hipFuncSetAttribute((const void*)fused_decode, hipFuncAttributeMaxDynamicSharedMemorySize, L_END);
  fused_decode<<<dim3(NWG), dim3(NTHR), L_END, stream>>>(x, kc, vc, wq, wo, wg, wu, wd, (unsigned short*)d_out, (unsigned char*)d_ws);
}
```

```cpp
#include <hip/hip_runtime.h>
#include <hip/hip_bf16.h>
#include <cstdint>

typedef __bf16 bf16x2 __attribute__((ext_vector_type(2)));
typedef __bf16 bf16x4 __attribute__((ext_vector_type(4)));
typedef __bf16 bf16x8 __attribute__((ext_vector_type(8)));
typedef unsigned u32x2 __attribute__((ext_vector_type(2)));
typedef unsigned u32x4 __attribute__((ext_vector_type(4)));
typedef float f32x2 __attribute__((ext_vector_type(2)));
typedef float f32x4 __attribute__((ext_vector_type(4)));
typedef unsigned long long u64;
#define LAS __attribute__((address_space(3)))
#define GAS __attribute__((address_space(1)))
#define RLX_AGENT __ATOMIC_RELAXED, __HIP_MEMORY_SCOPE_AGENT
#define RLX_WG __ATOMIC_RELAXED, __HIP_MEMORY_SCOPE_WORKGROUP

constexpr int NB = 8, DM = 512, HKV = 8, SK = 8192, FF = 1792;
constexpr int NWG = 256, NTHR = 768;
constexpr int NT_TILES = 16;

#define CAPG 6
#define PCAP 2
#define RINGW 4
#define WEARLY 2
#define PRIO 0
#define PUNR 2
#define NSTAT 16
#define GATE_H 4
#define GATE_Y2 8
#define SLP_PART 4
#define SLP_DIR 2
#define SLP_Y 2
#define SLP_MID 64

constexpr unsigned WS_EPOCH = 0;
constexpr unsigned WS_CNT = 24576;
constexpr unsigned WS_ML = 8192;
constexpr unsigned WS_QG = 32768;
constexpr unsigned WS_HG = WS_QG + NB * 4096 * 8;
constexpr unsigned WS_PART = WS_HG + NB * FF * 8;
constexpr unsigned WS_Y = WS_PART + NWG * 2048;
constexpr unsigned WS_END = WS_Y + (1u << 20);
constexpr unsigned WS_STAMP = 4u << 20;

constexpr unsigned L_RING = 0;
constexpr unsigned L_STG = 131072;
constexpr unsigned L_XN = L_STG + 16384;
constexpr unsigned XN_STRIDE = 1040;
constexpr unsigned L_Q = L_XN + 8 * XN_STRIDE;
constexpr unsigned L_OA = L_Q + 1024;
constexpr unsigned OA_STRIDE = 272;
constexpr unsigned L_CTL = L_OA + 8 * OA_STRIDE;
constexpr unsigned L_END = L_CTL + 128;
static_assert(L_END <= 163840, "LDS");
constexpr unsigned MRG_W = (512 + 8) * 4;
enum { C_LN = 0, C_STGFREE, C_QREADY, C_HDONE, C_MRG, C_ATTEND, C_TLO, C_THI, C_WOLD, C_Y2DONE, C_WOREAD, C_OG, C_PROG, C_MLMASK, C_RING, C_GUST, C_CLEAN };

__device__ __forceinline__ unsigned cvtpk(float a, float b) {
  bf16x2 t = __builtin_convertvector(f32x2{a, b}, bf16x2);
  return __builtin_bit_cast(unsigned, t);
}
__device__ __forceinline__ float lo16(unsigned u) { return __builtin_bit_cast(float, u << 16); }
__device__ __forceinline__ float hi16(unsigned u) { return __builtin_bit_cast(float, u & 0xffff0000u); }
__device__ __forceinline__ float rbf(float f) { return lo16(cvtpk(f, f)); }
template <int CTRL> __device__ __forceinline__ float dpp(float x) {
  return __builtin_bit_cast(float, __builtin_amdgcn_mov_dpp(__builtin_bit_cast(int, x), CTRL, 0xf, 0xf, true));
}
constexpr int XOR1 = 0xB1, XOR2 = 0x4E, XOR7 = 0x141, XOR8 = 0x128;
__device__ __forceinline__ float xrow16_max(float x) {
  auto s = __builtin_amdgcn_permlane16_swap(__float_as_uint(x), __float_as_uint(x), false, false);
  x = fmaxf(__uint_as_float(s[0]), __uint_as_float(s[1]));
  auto t = __builtin_amdgcn_permlane32_swap(__float_as_uint(x), __float_as_uint(x), false, false);
  return fmaxf(__uint_as_float(t[0]), __uint_as_float(t[1]));
}
__device__ __forceinline__ float xrow16_sum(float x) {
  auto s = __builtin_amdgcn_permlane16_swap(__float_as_uint(x), __float_as_uint(x), false, false);
  x = __uint_as_float(s[0]) + __uint_as_float(s[1]);
  auto t = __builtin_amdgcn_permlane32_swap(__float_as_uint(x), __float_as_uint(x), false, false);
  return __uint_as_float(t[0]) + __uint_as_float(t[1]);
}
__device__ __forceinline__ float wave_sum(float x) {
  x += dpp<XOR1>(x); x += dpp<XOR2>(x); x += dpp<XOR7>(x); x += dpp<XOR8>(x);
  return xrow16_sum(x);
}

__device__ __forceinline__ void dma4(unsigned v0, unsigned v1, unsigned v2, unsigned v3, u32x4 rs, unsigned so, unsigned ldsaddr) {
  unsigned keep;
  asm volatile(
    "s_mov_b32 %0, m0\n\t"
    "s_mov_b32 m0, %7\n\t"
    "s_nop 0\n\t"
    "buffer_load_dwordx4 %1, %5, %6 offen nt lds\n\t"
    "buffer_load_dwordx4 %2, %5, %6 offen offset:1024 nt lds\n\t"
    "buffer_load_dwordx4 %3, %5, %6 offen offset:2048 nt lds\n\t"
    "buffer_load_dwordx4 %4, %5, %6 offen offset:3072 nt lds\n\t"
    "s_mov_b32 m0, %0"
    : "=&s"(keep) : "v"(v0), "v"(v1), "v"(v2), "v"(v3), "s"(rs), "s"(so), "s"(ldsaddr) : "memory");
}
__device__ __forceinline__ void dma1(unsigned v0, u32x4 rs, unsigned so, unsigned ldsaddr) {
  unsigned keep;
  asm volatile(
    "s_mov_b32 %0, m0\n\t"
    "s_mov_b32 m0, %4\n\t"
    "s_nop 0\n\t"
    "buffer_load_dwordx4 %1, %2, %3 offen lds\n\t"
    "s_mov_b32 m0, %0"
    : "=&s"(keep) : "v"(v0), "s"(rs), "s"(so), "s"(ldsaddr) : "memory");
}
#define WAIT_VM(n) asm volatile("s_waitcnt vmcnt(" #n ")" ::: "memory")
#define WAIT_VMI(n) asm volatile("s_waitcnt vmcnt(%0)" :: "i"(n) : "memory")
#define WAIT_LGKM0() asm volatile("s_waitcnt lgkmcnt(0)" ::: "memory")

__device__ __forceinline__ unsigned lds_ld(LAS unsigned char* L, int idx) { return __hip_atomic_load((LAS unsigned*)(L + L_CTL) + idx, RLX_WG); }
__device__ __forceinline__ unsigned lds_ldu(LAS unsigned char* L, int idx) { return (unsigned)__builtin_amdgcn_readfirstlane(lds_ld(L, idx)); }
__device__ __forceinline__ void lds_st(LAS unsigned char* L, int idx, unsigned v) { __hip_atomic_store((LAS unsigned*)(L + L_CTL) + idx, v, RLX_WG); }
__device__ __forceinline__ unsigned lds_add(LAS unsigned char* L, int idx) { return __hip_atomic_fetch_add((LAS unsigned*)(L + L_CTL) + idx, 1u, RLX_WG); }
template <int SLP = 1>
__device__ __forceinline__ void lds_wait_ge(LAS unsigned char* L, int idx, unsigned want) {
  while (lds_ldu(L, idx) < want) __builtin_amdgcn_s_sleep(SLP);
  asm volatile("" ::: "memory");
}
constexpr unsigned SPIN_CAP = 1u << 14;
__device__ __forceinline__ unsigned nosent(unsigned u) { return u == 0xffffffffu ? 0x7fc07fc0u : u; }
__device__ __forceinline__ int opaque(int v) { asm volatile("" : "+s"(v)); return v; }

#define STAMP(ws, w, k) do {} while (0)

struct AttnState { f32x4 acc[8]; float m, l; };
struct AttnConst { u32x4 rsK, rsV; const unsigned char* cnt; unsigned gbase, gstat, ring, vk0, vv0, kaddr, vaddr; bf16x8 qf[4]; };

#define DMA_K(c_, so_, ld_) dma4((c_).vk0, (c_).vk0 ^ 64, (c_).vk0 ^ 128, (c_).vk0 ^ 192, (c_).rsK, (so_), (ld_))
#define DMA_V(c_, so_, ld_) dma4((c_).vv0, (c_).vv0 ^ 128, (c_).vv0, (c_).vv0 ^ 128, (c_).rsV, (so_), (ld_))

template <int SLOT>
__device__ __forceinline__ bool attn_tile(LAS unsigned char* L, const AttnConst& c, AttnState& st, const int t, const bool v1, unsigned& tkv, const int lane) {
  constexpr float SCALE = 0.088388347648318440f, LOG2E = 1.4426950408889634f;
  const unsigned ld2 = c.ring + SLOT * 16384;
  if (v1 && (NSTAT >= 16 || t + 2 < NSTAT)) WAIT_VM(24); else WAIT_VM(8);
  f32x4 s0 = {0.f, 0.f, 0.f, 0.f}, s1 = {0.f, 0.f, 0.f, 0.f};
#pragma unroll
  for (int s = 0; s < 4; ++s) {
    const bf16x8 k0 = *(LAS bf16x8*)(L + (c.kaddr ^ (s << 6)) + SLOT * 16384);
    const bf16x8 k1 = *(LAS bf16x8*)(L + (c.kaddr ^ (s << 6)) + SLOT * 16384 + 4096);
    s0 = __builtin_amdgcn_mfma_f32_16x16x32_bf16(k0, c.qf[s], s0, 0, 0, 0);
    s1 = __builtin_amdgcn_mfma_f32_16x16x32_bf16(k1, c.qf[s], s1, 0, 0, 0);
  }
  WAIT_LGKM0();
  bool v2 = false; unsigned so2 = 0;
  if (v1) {
    if (t + 2 < NSTAT) { v2 = true; so2 = c.gstat + (unsigned)(t + 2) * 32768u; }
    else if (NSTAT < 16) {
      const unsigned tau = 16u * NSTAT + (unsigned)__builtin_amdgcn_readfirstlane(tkv);
      v2 = tau < 256u; so2 = c.gbase + (tau >> 2) * 32768u + (tau & 3u) * 4096u;
    }
  }
  if (v2) {
    WAIT_VMI(CAPG); DMA_K(c, so2, ld2); WAIT_VMI(CAPG); DMA_K(c, so2 + 16384u, ld2 + 4096);
    if (NSTAT < 16 && t + 3 >= NSTAT) {
      if (lane == 0) asm volatile("global_atomic_add %0, %1, %2, %3 sc0" : "=v"(tkv) : "v"(0u), "v"(1u), "s"(c.cnt) : "memory");
    }
  }
  float x[8];
  { unsigned u;
    u = cvtpk(s0[0], s0[1]); u = cvtpk(lo16(u) * SCALE, hi16(u) * SCALE); x[0] = lo16(u); x[1] = hi16(u);
    u = cvtpk(s0[2], s0[3]); u = cvtpk(lo16(u) * SCALE, hi16(u) * SCALE); x[2] = lo16(u); x[3] = hi16(u);
    u = cvtpk(s1[0], s1[1]); u = cvtpk(lo16(u) * SCALE, hi16(u) * SCALE); x[4] = lo16(u); x[5] = hi16(u);
    u = cvtpk(s1[2], s1[3]); u = cvtpk(lo16(u) * SCALE, hi16(u) * SCALE); x[6] = lo16(u); x[7] = hi16(u); }
  float pm = fmaxf(fmaxf(fmaxf(x[0], x[1]), fmaxf(x[2], x[3])), fmaxf(fmaxf(x[4], x[5]), fmaxf(x[6], x[7])));
  pm = xrow16_max(pm);
  if (__any(pm > st.m)) {
    const float mn = fmaxf(st.m, pm);
    const float alpha = __builtin_amdgcn_exp2f((st.m - mn) * LOG2E);
    st.l *= alpha; st.m = mn;
#pragma unroll
    for (int d = 0; d < 8; ++d) st.acc[d] *= alpha;
  }
  const float mb = st.m * LOG2E;
  float p[8];
#pragma unroll
  for (int e = 0; e < 8; ++e) p[e] = __builtin_amdgcn_exp2f(fmaf(x[e], LOG2E, -mb));
  st.l += ((p[0] + p[1]) + (p[2] + p[3])) + ((p[4] + p[5]) + (p[6] + p[7]));
  const u32x4 pw = {cvtpk(p[0], p[1]), cvtpk(p[2], p[3]), cvtpk(p[4], p[5]), cvtpk(p[6], p[7])};
  const bf16x8 pf = __builtin_bit_cast(bf16x8, pw);
  if (v2) WAIT_VM(24); else if (v1) WAIT_VM(16); else WAIT_VM(0);
#pragma unroll
  for (int d = 0; d < 8; ++d) {
    const bf16x4 v0 = __builtin_amdgcn_ds_read_tr16_b64_v4bf16((LAS bf16x4*)(L + (c.vaddr ^ (d << 5)) + SLOT * 16384));
    const bf16x4 v1_ = __builtin_amdgcn_ds_read_tr16_b64_v4bf16((LAS bf16x4*)(L + (c.vaddr ^ (d << 5)) + SLOT * 16384 + 4096));
    const bf16x8 vf = __builtin_shufflevector(v0, v1_, 0, 1, 2, 3, 4, 5, 6, 7);
    st.acc[d] = __builtin_amdgcn_mfma_f32_16x16x32_bf16(vf, pf, st.acc[d], 0, 0, 0);
  }
  WAIT_LGKM0();
  if (v2) { WAIT_VMI(CAPG); DMA_V(c, so2, ld2 + 8192); WAIT_VMI(CAPG); DMA_V(c, so2 + 16384u, ld2 + 8192 + 4096); }
  return v2;
}

template <int TR>
__device__ __forceinline__ f32x4 proj16(LAS unsigned char* L, int lane) {
  const int g = lane >> 4, i = lane & 15;
  unsigned aa = L_XN + (i & 7) * XN_STRIDE + g * 16;
  unsigned ba = L_STG + g * 256 + i * 8;
  f32x4 d0 = {0.f, 0.f, 0.f, 0.f}, d1 = {0.f, 0.f, 0.f, 0.f};
constexpr int kPunr = PUNR;
#pragma unroll kPunr
  for (int s = 0; s < 4; ++s) {
    bf16x8 a[4], bb[4];
#pragma unroll
    for (int k = 0; k < 4; ++k) {
      a[k] = *(LAS bf16x8*)(L + aa + k * 64);
      const bf16x4 b0 = __builtin_amdgcn_ds_read_tr16_b64_v4bf16((LAS bf16x4*)(L + ba + k * 1024));
      const bf16x4 b1 = __builtin_amdgcn_ds_read_tr16_b64_v4bf16((LAS bf16x4*)(L + ba + k * 1024 + 128));
      bb[k] = __builtin_shufflevector(b0, b1, 0, 1, 2, 3, 4, 5, 6, 7);
    }
    if (TR) {
      d0 = __builtin_amdgcn_mfma_f32_16x16x32_bf16(bb[0], a[0], d0, 0, 0, 0); d1 = __builtin_amdgcn_mfma_f32_16x16x32_bf16(bb[1], a[1], d1, 0, 0, 0);
      d0 = __builtin_amdgcn_mfma_f32_16x16x32_bf16(bb[2], a[2], d0, 0, 0, 0); d1 = __builtin_amdgcn_mfma_f32_16x16x32_bf16(bb[3], a[3], d1, 0, 0, 0);
    } else {
      d0 = __builtin_amdgcn_mfma_f32_16x16x32_bf16(a[0], bb[0], d0, 0, 0, 0); d1 = __builtin_amdgcn_mfma_f32_16x16x32_bf16(a[1], bb[1], d1, 0, 0, 0);
      d0 = __builtin_amdgcn_mfma_f32_16x16x32_bf16(a[2], bb[2], d0, 0, 0, 0); d1 = __builtin_amdgcn_mfma_f32_16x16x32_bf16(a[3], bb[3], d1, 0, 0, 0);
    }
    aa += 256; ba += 4096;
  }
  return d0 + d1;
}

__global__ void __launch_bounds__(NTHR, 3)
fused_decode(const unsigned short* __restrict__ x, const unsigned short* __restrict__ kc, const unsigned short* __restrict__ vc,
             const unsigned short* __restrict__ wq, const unsigned short* __restrict__ wo, const unsigned short* __restrict__ wg,
             const unsigned short* __restrict__ wu, const unsigned short* __restrict__ wd, unsigned short* __restrict__ out, unsigned char* ws) {
  extern __shared__ __attribute__((aligned(16))) unsigned char lds[];
  LAS unsigned char* L = (LAS unsigned char*)lds;
  const int w = blockIdx.x;
  const int tid = threadIdx.x, lane = tid & 63, wave = __builtin_amdgcn_readfirstlane(tid >> 6);
  const int g = lane >> 4, i = lane & 15;
  const int xc = w & 7, r = w >> 3;
#define XSH 2
#define BSH 0
  const int b = r & 7, sp = r >> 3, kvh = (xc + XSH * sp + BSH * b) & 7;
  const __amdgpu_buffer_rsrc_t rsW = __builtin_amdgcn_make_buffer_rsrc(ws, 0, WS_END, 0x00020000);

  if (wave < 4) {
    const int aw = wave;
    const u64 ka = (u64)kc, va = (u64)vc;
    AttnConst c;
    c.rsK = u32x4{(unsigned)ka, (unsigned)(ka >> 32), 128u << 20, 0x00020000u};
    c.rsV = u32x4{(unsigned)va, (unsigned)(va >> 32), 128u << 20, 0x00020000u};
    c.gbase = (unsigned)((b * HKV + kvh) * SK * 256);
    c.gstat = c.gbase + (unsigned)(sp * NSTAT) * 32768u + aw * 4096;
    c.cnt = ws + WS_CNT + (b * 8 + kvh) * 128;
    c.ring = L_RING + aw * 32768;
    c.vk0 = g * 256 + ((i ^ g) << 4);
    c.vv0 = g * 256 + ((i ^ (2 * g)) << 4);
    c.kaddr = c.ring + i * 256 + ((i ^ g) << 4);
    const int q4 = i >> 2, pp = lane & 3;
    c.vaddr = c.ring + 8192 + (4 * g + q4) * 256 + ((8 * (g & 1) + 2 * q4) << 4) + 8 * pp;
    __builtin_amdgcn_s_barrier();
    if (aw == 0) STAMP(ws, w, 0);
#define PCAPW() do { if (lds_ldu(L, C_QREADY)) WAIT_VMI(CAPG); else WAIT_VMI(PCAP); } while (0)
#pragma unroll 1
    for (int u = 0; u < 2; ++u) {
      const unsigned so = c.gstat + u * 32768u, ld = c.ring + u * 16384;
      PCAPW(); DMA_K(c, so, ld);
      PCAPW(); DMA_K(c, so + 16384u, ld + 4096);
      PCAPW(); DMA_V(c, so, ld + 8192);
      if (u == 1 && lane == 0) lds_add(L, C_RING);
      PCAPW(); DMA_V(c, so + 16384u, ld + 8192 + 4096);
    }
    if (aw == 0) STAMP(ws, w, 1);
    lds_wait_ge(L, C_QREADY, 1);
    if (aw == 0) STAMP(ws, w, 2);
#pragma unroll
    for (int s = 0; s < 4; ++s) { u32x4 v = *(LAS u32x4*)(L + L_Q + (i & 3) * 256 + 64 * s + 16 * g); if (i >= 4) v = u32x4{0, 0, 0, 0}; c.qf[s] = __builtin_bit_cast(bf16x8, v); }
    AttnState st;
#pragma unroll
    for (int d = 0; d < 8; ++d) st.acc[d] = f32x4{0.f, 0.f, 0.f, 0.f};
    st.m = -INFINITY; st.l = 0.f;
    unsigned tkv = 0xffffu;
    bool v1 = true;
#pragma unroll 1
    for (int t = 0;; t += 2) {
      if (aw == 0 && lane == 0) lds_st(L, C_PROG, t);
      const bool v2 = attn_tile<0>(L, c, st, t, v1, tkv, lane);
      if (!v2 && lane == 0) lds_st(L, C_ATTEND, 1);
      if (!v1) break;
      const bool v3 = attn_tile<1>(L, c, st, t + 1, v2, tkv, lane);
      if (!v3 && lane == 0) lds_st(L, C_ATTEND, 1);
      if (!v2) break;
      v1 = v3;
    }
    lds_wait_ge(L, C_WOREAD, 4);
    if (aw == 0) STAMP(ws, w, 3);
    const float lsum = xrow16_sum(st.l);
    LAS float* mr = (LAS float*)(L + L_STG + aw * MRG_W);
    if (i < 4) {
#pragma unroll
      for (int d = 0; d < 8; ++d) *(LAS f32x4*)(mr + i * 128 + 16 * d + 4 * g) = st.acc[d];
      if (g == 0) { mr[512 + i] = st.m; mr[516 + i] = lsum; }
    }
    WAIT_LGKM0();
    unsigned old = 0;
    if (lane == 0) old = lds_add(L, C_MRG);
    old = __builtin_amdgcn_readfirstlane(old);
    asm volatile("" ::: "memory");
    if (old == 3u) {
      const unsigned T32 = lds_ldu(L, C_TLO);
      float M = -INFINITY;
#pragma unroll
      for (int ww = 0; ww < 4; ++ww) M = fmaxf(M, ((LAS float*)(L + L_STG + ww * MRG_W))[512 + g]);
      float Ls = 0.f; f32x4 o0 = {0.f, 0.f, 0.f, 0.f}, o1 = {0.f, 0.f, 0.f, 0.f};
#pragma unroll
      for (int ww = 0; ww < 4; ++ww) { LAS float* rec = (LAS float*)(L + L_STG + ww * MRG_W);
        const float wt = __builtin_amdgcn_exp2f((rec[512 + g] - M) * 1.4426950408889634f);
        Ls = fmaf(wt, rec[516 + g], Ls);
        o0 += wt * *(LAS f32x4*)(rec + g * 128 + 4 * i); o1 += wt * *(LAS f32x4*)(rec + g * 128 + 64 + 4 * i); }
      const unsigned po = WS_PART + ((b * 32 + kvh * 4 + g) * 4 + sp) * 512 + i * 16;
      __builtin_amdgcn_raw_buffer_store_b128(u32x4{cvtpk(o0[0], o0[1]), T32, cvtpk(o0[2], o0[3]), T32}, rsW, po, 0, 16);
      __builtin_amdgcn_raw_buffer_store_b128(u32x4{cvtpk(o1[0], o1[1]), T32, cvtpk(o1[2], o1[3]), T32}, rsW, po + 256, 0, 16);
      if (i == 0) __builtin_amdgcn_raw_buffer_store_b128(u32x4{__float_as_uint(M), T32, __float_as_uint(Ls), T32}, rsW, WS_ML + (((kvh * 4 + g) * 8 + b) * 4 + sp) * 16, 0, 16);
      STAMP(ws, w, 4);
    }
    return;
  }

  const int hw = wave - 4;
  if (hw == 0) STAMP(ws, w, 8);
  u32x4 xv = *(const GAS u32x4*)(x + hw * DM + lane * 8);
  u64 E = 0;
  if (hw == 0) asm volatile("global_load_dwordx2 %0, %1, %2 sc1" : "=v"(E) : "v"(0u), "s"(ws) : "memory");
  {
    const u64 qa = (u64)wq;
    const u32x4 rsQ = {(unsigned)qa, (unsigned)(qa >> 32), 4u << 20, 0x00020000u};
    const unsigned n0 = xc * 512 + r * 16;
    const unsigned vo = (lane >> 1) * 8192 + (lane & 1) * 16;
    dma1(vo, rsQ, (2 * hw) * 262144u + n0 * 2, L_STG + (2 * hw) * 1024);
    dma1(vo, rsQ, (2 * hw + 1) * 262144u + n0 * 2, L_STG + (2 * hw + 1) * 1024);
  }
  if (hw == 3) {
    if (lane < 32) lds_st(L, lane, 0u);
    if (NSTAT < 16 && r < 8 && lane == 0) __hip_atomic_store((GAS unsigned*)(ws + WS_CNT + (r * 8 + xc) * 128), 0u, RLX_AGENT);
    WAIT_LGKM0();
  }
  __builtin_amdgcn_s_barrier();
  asm volatile("" : "+v"(xv) :: "memory");
  {
    float f[8];
#pragma unroll
    for (int k = 0; k < 4; ++k) { f[2 * k] = lo16(xv[k]); f[2 * k + 1] = hi16(xv[k]); }
    float s = ((f[0] + f[1]) + (f[2] + f[3])) + ((f[4] + f[5]) + (f[6] + f[7]));
    s = wave_sum(s);
    const float mean = s * (1.f / DM);
    float q = 0.f;
#pragma unroll
    for (int k = 0; k < 8; ++k) { const float d = f[k] - mean; q = fmaf(d, d, q); }
    q = wave_sum(q);
    const float var = rbf(q * (1.f / DM));
    const float mb = rbf(mean);
    const float sd = rbf(sqrtf(rbf(var + 1e-5f)));
    u32x4 o;
#pragma unroll
    for (int k = 0; k < 4; ++k) o[k] = cvtpk(rbf(f[2 * k] - mb) / sd, rbf(f[2 * k + 1] - mb) / sd);
    *(LAS u32x4*)(L + L_XN + hw * XN_STRIDE + lane * 16) = o;
  }
  asm volatile("s_waitcnt vmcnt(0)" : "+v"(E) :: "memory");
  if (hw == 0 && lane == 0) { const u64 T = E + 1; lds_st(L, C_TLO, (unsigned)T); lds_st(L, C_THI, (unsigned)(T >> 32)); }
  WAIT_LGKM0();
  if (lane == 0) lds_add(L, C_LN);
  if (hw == 0) lds_wait_ge<1>(L, C_LN, 8); else lds_wait_ge<6>(L, C_LN, 8);
  const unsigned Tlo = lds_ldu(L, C_TLO), Thi = lds_ldu(L, C_THI);
  const unsigned tg_lo = Tlo << 16, tg_hi = (Thi << 16) | (Tlo >> 16);
  const u64 Tu = ((u64)Thi << 32) | Tlo;
#define TAG_OK(lo_, hi_) ((((lo_) ^ tg_lo) >> 16) == 0 && (hi_) == tg_hi)
  const int gam = xc + 8 * sp, f0 = gam * 64 + (r & 7) * 8;
  const int hd = 4 * xc + sp, cbk = r & 7;

  if (hw == 0) {
    if (PRIO) __builtin_amdgcn_s_setprio(3);
    STAMP(ws, w, 9);
    const f32x4 d = proj16<1>(L, lane);
    const unsigned pk = i < 8 ? cvtpk(d[0], d[1]) : cvtpk(d[2], d[3]);
    __builtin_amdgcn_raw_buffer_store_b128(u32x4{tg_lo | (pk & 0xffffu), tg_hi, tg_lo | (pk >> 16), tg_hi}, rsW,
                                           WS_QG + ((i & 7) * 4096 + xc * 512 + r * 16 + 4 * g + 2 * (i >> 3)) * 8, 0, 16);
    if (lane == 0) lds_st(L, C_STGFREE, 1);
    if (PRIO) __builtin_amdgcn_s_setprio(0);
    STAMP(ws, w, 10);
  } else if (hw == 1) {
    lds_wait_ge<8>(L, C_GUST, 4);
    if (gam < 28) {
      const f32x4 d = proj16<0>(L, lane);
      float hv[2];
#pragma unroll
      for (int k = 0; k < 2; ++k) {
        const float own = rbf(g < 2 ? d[k] : d[2 + k]);
        const float oth = dpp<XOR8>(own);
        hv[k] = own > 0.f ? own * oth : 0.f;
      }
      const unsigned pk = cvtpk(hv[0], hv[1]);
      const int b0 = 4 * (g & 1) + (g < 2 ? 0 : 2);
      if (i < 8) __builtin_amdgcn_raw_buffer_store_b128(u32x4{tg_lo | (pk & 0xffffu), tg_hi, tg_lo | (pk >> 16), tg_hi}, rsW, WS_HG + ((f0 + i) * 8 + b0) * 8, 0, 16);
    }
    if (lane == 0) lds_st(L, C_HDONE, 1);
    STAMP(ws, w, 17);
  } else if (hw == 2) {
    const unsigned qo = WS_QG + (b * 4096 + kvh * 512) * 8 + lane * 16;
    bool okk[4] = {false, false, false, false};
    lds_wait_ge<4>(L, C_STGFREE, 1);
#pragma unroll 1
    for (unsigned spins = 0; spins < SPIN_CAP; ++spins) {
      asm volatile("" ::: "memory");
      u32x4 v[4];
#pragma unroll
      for (int k = 0; k < 4; ++k) if (!okk[k]) v[k] = __builtin_amdgcn_raw_buffer_load_b128(rsW, qo + k * 1024, 0, 16);
#pragma unroll
      for (int k = 0; k < 4; ++k) if (!okk[k]) {
        okk[k] = TAG_OK(v[k][0], v[k][1]) && TAG_OK(v[k][2], v[k][3]);
        *(LAS unsigned*)(L + L_Q + (lane + 64 * k) * 4) = (v[k][0] & 0xffffu) | (v[k][2] << 16);
      }
      if (__all(okk[0] && okk[1] && okk[2] && okk[3])) break;
      __builtin_amdgcn_s_sleep(1);
    }
    WAIT_LGKM0();
    if (lane == 0) lds_st(L, C_QREADY, 1);
    STAMP(ws, w, 11);
  } else if (hw == 3) {
  } else {
    const int h4 = hw - 4;
    const __amdgpu_buffer_rsrc_t rsD = __builtin_amdgcn_make_buffer_rsrc((void*)wd, 0, FF * DM * 2, 0x00020000);
    const GAS unsigned short* sgu = (const GAS unsigned short*)((lane & 1) ? wu : wg) + (size_t)(128 * h4 + (lane >> 1)) * FF + (gam < 28 ? f0 : 0);
    const GAS unsigned short* swo = (const GAS unsigned short*)wo + (size_t)(128 * hd + 32 * h4 + (lane >> 3)) * DM + cbk * 64 + (lane & 7) * 8;
    u32x4 rg[4], ro[4], rd_[2];
#pragma unroll
    for (int u = 0; u < 4; ++u) ro[u] = __builtin_nontemporal_load((const GAS u32x4*)(swo + (size_t)u * 8 * DM));
#pragma unroll
    for (int u = 0; u < 2; ++u) rd_[u] = __builtin_amdgcn_raw_buffer_load_b128(rsD, (unsigned)((56 * hd + 16 * h4 + 8 * u + (lane >> 3)) * 1024 + cbk * 128 + (lane & 7) * 16), 0, 2);
    lds_wait_ge<8>(L, C_QREADY, 1);
#pragma unroll
    for (int u = 0; u < 4; ++u) rg[u] = *(const GAS u32x4*)(sgu + (size_t)u * 32 * FF);
    lds_wait_ge(L, C_STGFREE, 1);
#pragma unroll
    for (int u = 0; u < 4; ++u) *(LAS u32x4*)(L + L_STG + (4 * h4 + u) * 1024 + lane * 16) = rg[u];
    WAIT_LGKM0();
    if (lane == 0) lds_add(L, C_GUST);
    lds_wait_ge<4>(L, C_HDONE, 1);
#pragma unroll
    for (int u = 0; u < 4; ++u) *(LAS u32x4*)(L + L_STG + (4 * h4 + u) * 1024 + lane * 16) = ro[u];
#pragma unroll
    for (int u = 0; u < 2; ++u) *(LAS u32x4*)(L + L_XN + (2 * h4 + u) * 1024 + lane * 16) = rd_[u];
    WAIT_LGKM0();
    if (lane == 0) lds_add(L, C_WOLD);
  }
  {
    const int fl = 7 * hw + (lane >> 3), bb = lane & 7;
    const unsigned ho = WS_HG + ((56 * hd + 7 * hw) * 8 + (lane < 56 ? lane : 0)) * 8;
    u32x2 hv = {0u, 0u};
    bool ok = false;
    lds_wait_ge<32>(L, C_PROG, GATE_H);
#pragma unroll 1
    for (unsigned spins = 0; spins < SPIN_CAP; ++spins) {
      asm volatile("" ::: "memory");
      if (!ok) { hv = __builtin_amdgcn_raw_buffer_load_b64(rsW, ho, 0, 16); ok = TAG_OK(hv[0], hv[1]); }
      if (__all(ok)) break;
      __builtin_amdgcn_s_sleep(SLP_MID);
    }
    if (lane < 56) *(LAS unsigned short*)(L + L_OA + bb * OA_STRIDE + fl * 2) = (unsigned short)(hv[0] & 0xffffu);
    if (lane < 4) *(LAS unsigned*)(L + L_OA + hw * OA_STRIDE + 112 + lane * 4) = 0u;
    WAIT_LGKM0();
    if (lane == 0) lds_add(L, C_OG);
  }
  const int bf = r >> 2, cb = 4 * xc + (r & 3), n1 = cb * 16;
  const unsigned ba = (g * 8 + (i >> 2)) * 128 + (lane & 3) * 8 + (hw & 3) * 32;
  const unsigned oa = L_OA + (i & 7) * OA_STRIDE + g * 16;
  bf16x8 wf[4];
  unsigned y2p = 0;
  float xr[2] = {0.f, 0.f};
  if (hw < 4) {
    lds_wait_ge<8>(L, C_WOLD, 4);
    lds_wait_ge<4>(L, C_OG, 8);
    f32x4 d = {0.f, 0.f, 0.f, 0.f};
#pragma unroll
    for (int s = 0; s < 2; ++s) {
      const bf16x4 a0 = __builtin_amdgcn_ds_read_tr16_b64_v4bf16((LAS bf16x4*)(L + L_XN + ba + s * 4096));
      const bf16x4 a1 = __builtin_amdgcn_ds_read_tr16_b64_v4bf16((LAS bf16x4*)(L + L_XN + ba + s * 4096 + 512));
      d = __builtin_amdgcn_mfma_f32_16x16x32_bf16(__builtin_shufflevector(a0, a1, 0, 1, 2, 3, 4, 5, 6, 7), *(LAS bf16x8*)(L + oa + s * 64), d, 0, 0, 0);
    }
    y2p = i < 8 ? cvtpk(d[0], d[1]) : cvtpk(d[2], d[3]);
#pragma unroll
    for (int s = 0; s < 4; ++s) {
      const bf16x4 a0 = __builtin_amdgcn_ds_read_tr16_b64_v4bf16((LAS bf16x4*)(L + L_STG + ba + s * 4096));
      const bf16x4 a1 = __builtin_amdgcn_ds_read_tr16_b64_v4bf16((LAS bf16x4*)(L + L_STG + ba + s * 4096 + 512));
      wf[s] = __builtin_shufflevector(a0, a1, 0, 1, 2, 3, 4, 5, 6, 7);
    }
    asm volatile("s_waitcnt lgkmcnt(0)" : "+v"(wf[0]), "+v"(wf[1]), "+v"(wf[2]), "+v"(wf[3]), "+v"(y2p) :: "memory");
    if (lane == 0) lds_add(L, C_WOREAD);
    if (hw == 0) STAMP(ws, w, 12);
  } else if (hw == 6) {
    const unsigned xq = *(const GAS unsigned*)(x + bf * DM + n1 + (lane & 7) * 2);
    xr[0] = lo16(xq); xr[1] = hi16(xq);
  } else if (hw == 5 && w == 0) {
#pragma unroll 1
    for (unsigned spins = 0; spins < SPIN_CAP; ++spins) {
      bool ok = true;
#pragma unroll
      for (int k = 0; k < 4; ++k) { const u32x2 v = __builtin_amdgcn_raw_buffer_load_b64(rsW, WS_QG + (lane + 64 * k) * 128, 0, 16); ok &= TAG_OK(v[0], v[1]); }
      if (__all(ok)) break;
      __builtin_amdgcn_s_sleep(SLP_MID);
      asm volatile("" ::: "memory");
    }
    if (lane == 0) __hip_atomic_store((GAS u64*)(ws + WS_EPOCH), Tu, RLX_AGENT);
  }
  {
    lds_wait_ge<4>(L, C_WOREAD, 4);
    lds_wait_ge<16>(L, C_ATTEND, 1);
    if (hw == 0) STAMP(ws, w, 13);
    const unsigned po = WS_PART + ((hw * 32 + hd) * 4 + g) * 512 + i * 16;
    const unsigned mo = WS_ML + ((hd * 8 + hw) * 4 + g) * 16;
    u32x4 pv0 = {0u, 0u, 0u, 0u}, pv1 = {0u, 0u, 0u, 0u}, ml = {0u, 0u, 0u, 0u};
    if (hw == 7) {
      bool ok = lane >= 32;
#pragma unroll 1
      for (unsigned spins = 0; spins < SPIN_CAP; ++spins) {
        asm volatile("" ::: "memory");
        if (!ok) {
          ml = __builtin_amdgcn_raw_buffer_load_b128(rsW, WS_ML + hd * 512 + lane * 16, 0, 16);
          ok = ml[1] == Tlo && ml[3] == Tlo;
        }
        const unsigned miss = ~(unsigned)__ballot(ok);
        if (lane == 0) lds_st(L, C_MLMASK, ~miss);
        const unsigned two = (miss & (miss >> 1) & 0x77777777u) | (miss & (miss >> 2) & 0x33333333u) | (miss & (miss >> 3) & 0x11111111u);
        if (two == 0u) break;
        __builtin_amdgcn_s_sleep(SLP_PART);
      }
      if (lane == 0) lds_st(L, C_MLMASK, 0xffffffffu);
    } else {
      while (__builtin_popcount((lds_ldu(L, C_MLMASK) >> (4 * hw)) & 15u) < 3) __builtin_amdgcn_s_sleep(1);
      asm volatile("" ::: "memory");
    }
    bool ok = false;
#pragma unroll 1
    for (unsigned spins = 0; spins < SPIN_CAP; ++spins) {
      asm volatile("" ::: "memory");
      if (!ok) {
        pv0 = __builtin_amdgcn_raw_buffer_load_b128(rsW, po, 0, 16);
        pv1 = __builtin_amdgcn_raw_buffer_load_b128(rsW, po + 256, 0, 16);
        ml = __builtin_amdgcn_raw_buffer_load_b128(rsW, mo, 0, 16);
        ok = pv0[1] == Tlo && pv0[3] == Tlo && pv1[1] == Tlo && pv1[3] == Tlo && ml[1] == Tlo && ml[3] == Tlo;
      }
      if (__all(ok)) break;
      __builtin_amdgcn_s_sleep(SLP_DIR);
    }
    if (hw == 0) STAMP(ws, w, 14);
    const float mj = __uint_as_float(ml[0]);
    const float M = xrow16_max(mj);
    const float wt = __builtin_amdgcn_exp2f((mj - M) * 1.4426950408889634f);
    const float inv = 1.f / xrow16_sum(wt * __uint_as_float(ml[2]));
    const u32x2 oa0 = {cvtpk(xrow16_sum(wt * lo16(pv0[0])) * inv, xrow16_sum(wt * hi16(pv0[0])) * inv), cvtpk(xrow16_sum(wt * lo16(pv0[2])) * inv, xrow16_sum(wt * hi16(pv0[2])) * inv)};
    const u32x2 oa1 = {cvtpk(xrow16_sum(wt * lo16(pv1[0])) * inv, xrow16_sum(wt * hi16(pv1[0])) * inv), cvtpk(xrow16_sum(wt * lo16(pv1[2])) * inv, xrow16_sum(wt * hi16(pv1[2])) * inv)};
    if (g == 0) { *(LAS u32x2*)(L + L_OA + hw * OA_STRIDE + i * 8) = oa0; *(LAS u32x2*)(L + L_OA + hw * OA_STRIDE + 128 + i * 8) = oa1; }
    WAIT_LGKM0();
    if (lane == 0) lds_add(L, C_OG);
  }
  if (hw < 4) {
    lds_wait_ge(L, C_OG, 16);
    f32x4 d = {0.f, 0.f, 0.f, 0.f};
#pragma unroll
    for (int s = 0; s < 4; ++s) d = __builtin_amdgcn_mfma_f32_16x16x32_bf16(wf[s], *(LAS bf16x8*)(L + oa + s * 64), d, 0, 0, 0);
    const unsigned yp = i < 8 ? cvtpk(d[0], d[1]) : cvtpk(d[2], d[3]);
    const unsigned yo = ((((i & 7) * 32 + 4 * cbk + hw) * 32 + hd) * 16 + 4 * g + 2 * (i >> 3)) * 8;
    __builtin_amdgcn_raw_buffer_store_b128(u32x4{(yp & 0xffffu) | (y2p << 16), Tlo, (yp >> 16) | (y2p & 0xffff0000u), Tlo}, rsW, WS_Y + yo, 0, 16);
    if (hw == 0) STAMP(ws, w, 16);
  } else if (hw == 6) {
    const unsigned io = WS_Y + (bf * 32 + cb) * 4096 + lane * 16;
    u32x4 y[4];
    bool okk[4] = {false, false, false, false};
#pragma unroll 1
    for (unsigned spins = 0; spins < SPIN_CAP; ++spins) {
      asm volatile("" ::: "memory");
#pragma unroll
      for (int k = 0; k < 4; ++k) if (!okk[k]) y[k] = __builtin_amdgcn_raw_buffer_load_b128(rsW, io + k * 1024, 0, 16);
#pragma unroll
      for (int k = 0; k < 4; ++k) okk[k] = y[k][1] == Tlo && y[k][3] == Tlo;
      if (__all(okk[0] && okk[1] && okk[2] && okk[3])) break;
      __builtin_amdgcn_s_sleep(SLP_Y);
    }
    float o[2];
#pragma unroll
    for (int k = 0; k < 2; ++k) {
      float t = (lo16(y[0][2 * k]) + lo16(y[1][2 * k])) + (lo16(y[2][2 * k]) + lo16(y[3][2 * k]));
      float u = (hi16(y[0][2 * k]) + hi16(y[1][2 * k])) + (hi16(y[2][2 * k]) + hi16(y[3][2 * k]));
      t += dpp<XOR8>(t); t = rbf(xrow16_sum(t));
      u += dpp<XOR8>(u); u = rbf(xrow16_sum(u));
      const float gate = rbf(u / (1.f + __expf(-u)));
      o[k] = xr[k] + rbf(t * gate);
    }
    if (lane < 8) *(GAS unsigned*)(out + bf * DM + n1 + lane * 2) = cvtpk(o[0], o[1]);
    STAMP(ws, w, 15);
  }
}

extern "C" void kernel_launch(void* const* d_in, const int* in_sizes, int n_in, void* d_out, int out_size, void* d_ws, size_t ws_size, hipStream_t stream) {
  const unsigned short* x = (const unsigned short*)d_in[0];
  const unsigned short* kc = (const unsigned short*)d_in[1];
  const unsigned short* vc = (const unsigned short*)d_in[2];
  const unsigned short* wq = (const unsigned short*)d_in[3];
  const unsigned short* wo = (const unsigned short*)d_in[4];
  const unsigned short* wg = (const unsigned short*)d_in[6];
  const unsigned short* wu = (const unsigned short*)d_in[7];
  const unsigned short* wd = (const unsigned short*)d_in[8];
  (void)hipFuncSetAttribute((const void*)fused_decode, hipFuncAttributeMaxDynamicSharedMemorySize, L_END);
  fused_decode<<<dim3(NWG), dim3(NTHR), L_END, stream>>>(x, kc, vc, wq, wo, wg, wu, wd, (unsigned short*)d_out, (unsigned char*)d_ws);
}
```

```cpp
#include <hip/hip_runtime.h>
#include <hip/hip_bf16.h>
#include <cstdint>

typedef __bf16 bf16x2 __attribute__((ext_vector_type(2)));
typedef __bf16 bf16x4 __attribute__((ext_vector_type(4)));
typedef __bf16 bf16x8 __attribute__((ext_vector_type(8)));
typedef unsigned u32x2 __attribute__((ext_vector_type(2)));
typedef unsigned u32x4 __attribute__((ext_vector_type(4)));
typedef float f32x2 __attribute__((ext_vector_type(2)));
typedef float f32x4 __attribute__((ext_vector_type(4)));
typedef unsigned long long u64;
#define LAS __attribute__((address_space(3)))
#define GAS __attribute__((address_space(1)))
#define RLX_AGENT __ATOMIC_RELAXED, __HIP_MEMORY_SCOPE_AGENT
#define RLX_WG __ATOMIC_RELAXED, __HIP_MEMORY_SCOPE_WORKGROUP

constexpr int NB = 8, DM = 512, HKV = 8, SK = 8192, FF = 1792;
constexpr int NWG = 256, NTHR = 768;
constexpr int NT_TILES = 16;

#define CAPG 6
#define PCAP 2
#define RINGW 4
#define WEARLY 2
#define PRIO 0
#define PUNR 2
#define NSTAT 16
#define MLG 3
#define XT 1
#define XTW 4
#define HCAP 2
#define GATE_H 4
#define GATE_Y2 8
#define SLP_PART 4
#define SLP_DIR 2
#define SLP_Y 2
#define SLP_MID 64

constexpr unsigned WS_EPOCH = 0;
constexpr unsigned WS_CNT = 24576;
constexpr unsigned WS_ML = 8192;
constexpr unsigned WS_QG = 32768;
constexpr unsigned WS_HG = WS_QG + NB * 4096 * 8;
constexpr unsigned WS_PART = WS_HG + NB * FF * 8;
constexpr unsigned WS_Y = WS_PART + NWG * 2048;
constexpr unsigned WS_END = WS_Y + (1u << 20);
constexpr unsigned WS_STAMP = 4u << 20;

constexpr unsigned L_RING = 0;
constexpr unsigned L_STG = 131072;
constexpr unsigned L_XN = L_STG + 16384;
constexpr unsigned XN_STRIDE = 1040;
constexpr unsigned L_Q = L_XN + 8 * XN_STRIDE;
constexpr unsigned L_OA = L_Q + 1024;
constexpr unsigned OA_STRIDE = 272;
constexpr unsigned L_CTL = L_OA + 8 * OA_STRIDE;
constexpr unsigned L_STAMP = L_CTL + 128;
constexpr unsigned L_END = L_STAMP + 256;
static_assert(L_END <= 163840, "LDS");
constexpr unsigned MRG_W = (512 + 8) * 4;
enum { C_LN = 0, C_STGFREE, C_QREADY, C_HDONE, C_MRG, C_ATTEND, C_TLO, C_THI, C_WOLD, C_Y2DONE, C_WOREAD, C_OG, C_PROG, C_MLMASK, C_RING, C_GUST, C_CLEAN, C_XF = 20  , C_XR = 24   };

__device__ __forceinline__ unsigned cvtpk(float a, float b) {
  bf16x2 t = __builtin_convertvector(f32x2{a, b}, bf16x2);
  return __builtin_bit_cast(unsigned, t);
}
__device__ __forceinline__ float lo16(unsigned u) { return __builtin_bit_cast(float, u << 16); }
__device__ __forceinline__ float hi16(unsigned u) { return __builtin_bit_cast(float, u & 0xffff0000u); }
__device__ __forceinline__ float rbf(float f) { return lo16(cvtpk(f, f)); }
template <int CTRL> __device__ __forceinline__ float dpp(float x) {
  return __builtin_bit_cast(float, __builtin_amdgcn_mov_dpp(__builtin_bit_cast(int, x), CTRL, 0xf, 0xf, true));
}
constexpr int XOR1 = 0xB1, XOR2 = 0x4E, XOR7 = 0x141, XOR8 = 0x128;
__device__ __forceinline__ float xrow16_max(float x) {
  auto s = __builtin_amdgcn_permlane16_swap(__float_as_uint(x), __float_as_uint(x), false, false);
  x = fmaxf(__uint_as_float(s[0]), __uint_as_float(s[1]));
  auto t = __builtin_amdgcn_permlane32_swap(__float_as_uint(x), __float_as_uint(x), false, false);
  return fmaxf(__uint_as_float(t[0]), __uint_as_float(t[1]));
}
__device__ __forceinline__ float xrow16_sum(float x) {
  auto s = __builtin_amdgcn_permlane16_swap(__float_as_uint(x), __float_as_uint(x), false, false);
  x = __uint_as_float(s[0]) + __uint_as_float(s[1]);
  auto t = __builtin_amdgcn_permlane32_swap(__float_as_uint(x), __float_as_uint(x), false, false);
  return __uint_as_float(t[0]) + __uint_as_float(t[1]);
}
__device__ __forceinline__ float wave_sum(float x) {
  x += dpp<XOR1>(x); x += dpp<XOR2>(x); x += dpp<XOR7>(x); x += dpp<XOR8>(x);
  return xrow16_sum(x);
}

__device__ __forceinline__ void dma4(unsigned v0, unsigned v1, unsigned v2, unsigned v3, u32x4 rs, unsigned so, unsigned ldsaddr) {
  unsigned keep;
  asm volatile(
    "s_mov_b32 %0, m0\n\t"
    "s_mov_b32 m0, %7\n\t"
    "s_nop 0\n\t"
    "buffer_load_dwordx4 %1, %5, %6 offen nt lds\n\t"
    "buffer_load_dwordx4 %2, %5, %6 offen offset:1024 nt lds\n\t"
    "buffer_load_dwordx4 %3, %5, %6 offen offset:2048 nt lds\n\t"
    "buffer_load_dwordx4 %4, %5, %6 offen offset:3072 nt lds\n\t"
    "s_mov_b32 m0, %0"
    : "=&s"(keep) : "v"(v0), "v"(v1), "v"(v2), "v"(v3), "s"(rs), "s"(so), "s"(ldsaddr) : "memory");
}
__device__ __forceinline__ void dma4d(unsigned v0, unsigned v1, unsigned v2, unsigned v3, u32x4 rs, unsigned so, unsigned ldsaddr) {
  unsigned keep;
  asm volatile(
    "s_mov_b32 %0, m0\n\t"
    "s_mov_b32 m0, %7\n\t"
    "s_nop 0\n\t"
    "buffer_load_dwordx4 %1, %5, %6 offen lds\n\t"
    "buffer_load_dwordx4 %2, %5, %6 offen offset:1024 lds\n\t"
    "buffer_load_dwordx4 %3, %5, %6 offen offset:2048 lds\n\t"
    "buffer_load_dwordx4 %4, %5, %6 offen offset:3072 lds\n\t"
    "s_mov_b32 m0, %0"
    : "=&s"(keep) : "v"(v0), "v"(v1), "v"(v2), "v"(v3), "s"(rs), "s"(so), "s"(ldsaddr) : "memory");
}
__device__ __forceinline__ void dma1(unsigned v0, u32x4 rs, unsigned so, unsigned ldsaddr) {
  unsigned keep;
  asm volatile(
    "s_mov_b32 %0, m0\n\t"
    "s_mov_b32 m0, %4\n\t"
    "s_nop 0\n\t"
    "buffer_load_dwordx4 %1, %2, %3 offen lds\n\t"
    "s_mov_b32 m0, %0"
    : "=&s"(keep) : "v"(v0), "s"(rs), "s"(so), "s"(ldsaddr) : "memory");
}
#define WAIT_VM(n) asm volatile("s_waitcnt vmcnt(" #n ")" ::: "memory")
#define WAIT_VMI(n) asm volatile("s_waitcnt vmcnt(%0)" :: "i"(n) : "memory")
#define WAIT_LGKM0() asm volatile("s_waitcnt lgkmcnt(0)" ::: "memory")

__device__ __forceinline__ unsigned lds_ld(LAS unsigned char* L, int idx) { return __hip_atomic_load((LAS unsigned*)(L + L_CTL) + idx, RLX_WG); }
__device__ __forceinline__ unsigned lds_ldu(LAS unsigned char* L, int idx) { return (unsigned)__builtin_amdgcn_readfirstlane(lds_ld(L, idx)); }
__device__ __forceinline__ void lds_st(LAS unsigned char* L, int idx, unsigned v) { __hip_atomic_store((LAS unsigned*)(L + L_CTL) + idx, v, RLX_WG); }
__device__ __forceinline__ unsigned lds_add(LAS unsigned char* L, int idx) { return __hip_atomic_fetch_add((LAS unsigned*)(L + L_CTL) + idx, 1u, RLX_WG); }
template <int SLP = 1>
__device__ __forceinline__ void lds_wait_ge(LAS unsigned char* L, int idx, unsigned want) {
  while (lds_ldu(L, idx) < want) __builtin_amdgcn_s_sleep(SLP);
  asm volatile("" ::: "memory");
}
constexpr unsigned SPIN_CAP = 1u << 14;
__device__ __forceinline__ unsigned nosent(unsigned u) { return u == 0xffffffffu ? 0x7fc07fc0u : u; }
__device__ __forceinline__ int opaque(int v) { asm volatile("" : "+s"(v)); return v; }

#define STAMP(ws, w, k) do {} while (0)
#define STAMP_FLUSH(ws, w) do {} while (0)

struct AttnState { f32x4 acc[8]; float m, l; };
struct AttnConst { u32x4 rsK, rsV; const unsigned char* cnt; unsigned gbase, gstat, ring, vk0, vv0, kaddr, vaddr; int kdef, vdef; bf16x8 qf[4]; };

#define KDEF(b) 0
#define VDEF(b) 0
#define DMA_K(c_, so_, ld_) do { if ((c_).kdef) dma4d((c_).vk0, (c_).vk0 ^ 64, (c_).vk0 ^ 128, (c_).vk0 ^ 192, (c_).rsK, (so_), (ld_)); else dma4((c_).vk0, (c_).vk0 ^ 64, (c_).vk0 ^ 128, (c_).vk0 ^ 192, (c_).rsK, (so_), (ld_)); } while (0)
#define DMA_V(c_, so_, ld_) do { if ((c_).vdef) dma4d((c_).vv0, (c_).vv0 ^ 128, (c_).vv0, (c_).vv0 ^ 128, (c_).rsV, (so_), (ld_)); else dma4((c_).vv0, (c_).vv0 ^ 128, (c_).vv0, (c_).vv0 ^ 128, (c_).rsV, (so_), (ld_)); } while (0)

template <int SLOT>
__device__ __forceinline__ bool attn_tile(LAS unsigned char* L, const AttnConst& c, AttnState& st, const int t, const bool v1, unsigned& tkv, const int lane, const int aw) {
  constexpr float SCALE = 0.088388347648318440f, LOG2E = 1.4426950408889634f;
  const unsigned ld2 = c.ring + SLOT * 16384;
  if (v1 && (NSTAT >= 16 || t + 2 < NSTAT)) WAIT_VM(24); else WAIT_VM(8);
  if (XT && SLOT == 0 && t == 2) lds_wait_ge(L, C_XR + aw, 1);
  if (XT == 2 && SLOT == 1 && t == 3) lds_wait_ge(L, C_XR + aw, 3);
  f32x4 s0 = {0.f, 0.f, 0.f, 0.f}, s1 = {0.f, 0.f, 0.f, 0.f};
#pragma unroll
  for (int s = 0; s < 4; ++s) {
    const bf16x8 k0 = *(LAS bf16x8*)(L + (c.kaddr ^ (s << 6)) + SLOT * 16384);
    const bf16x8 k1 = *(LAS bf16x8*)(L + (c.kaddr ^ (s << 6)) + SLOT * 16384 + 4096);
    s0 = __builtin_amdgcn_mfma_f32_16x16x32_bf16(k0, c.qf[s], s0, 0, 0, 0);
    s1 = __builtin_amdgcn_mfma_f32_16x16x32_bf16(k1, c.qf[s], s1, 0, 0, 0);
  }
  WAIT_LGKM0();
  bool v2 = false; unsigned so2 = 0;
  if (v1) {
    if (t + 2 < NSTAT) { v2 = true; so2 = c.gstat + (unsigned)(t + 2) * 32768u; }
    else if (NSTAT < 16) {
      const unsigned tau = 16u * NSTAT + (unsigned)__builtin_amdgcn_readfirstlane(tkv);
      v2 = tau < 256u; so2 = c.gbase + (tau >> 2) * 32768u + (tau & 3u) * 4096u;
    }
  }
  if (XT && SLOT == 0 && t == 0) { if (lane == 0) lds_st(L, C_XF + aw, 1); }
  else if (XT == 2 && SLOT == 1 && t == 1) { if (lane == 0) lds_st(L, C_XF + aw, 3); }
  else if (v2) {
    WAIT_VMI(CAPG); DMA_K(c, so2, ld2); WAIT_VMI(CAPG); DMA_K(c, so2 + 16384u, ld2 + 4096);
    if (NSTAT < 16 && t + 3 >= NSTAT) {
      if (lane == 0) asm volatile("global_atomic_add %0, %1, %2, %3 sc0" : "=v"(tkv) : "v"(0u), "v"(1u), "s"(c.cnt) : "memory");
    }
  }
  float x[8];
  { unsigned u;
    u = cvtpk(s0[0], s0[1]); u = cvtpk(lo16(u) * SCALE, hi16(u) * SCALE); x[0] = lo16(u); x[1] = hi16(u);
    u = cvtpk(s0[2], s0[3]); u = cvtpk(lo16(u) * SCALE, hi16(u) * SCALE); x[2] = lo16(u); x[3] = hi16(u);
    u = cvtpk(s1[0], s1[1]); u = cvtpk(lo16(u) * SCALE, hi16(u) * SCALE); x[4] = lo16(u); x[5] = hi16(u);
    u = cvtpk(s1[2], s1[3]); u = cvtpk(lo16(u) * SCALE, hi16(u) * SCALE); x[6] = lo16(u); x[7] = hi16(u); }
  float pm = fmaxf(fmaxf(fmaxf(x[0], x[1]), fmaxf(x[2], x[3])), fmaxf(fmaxf(x[4], x[5]), fmaxf(x[6], x[7])));
  pm = xrow16_max(pm);
  if (__any(pm > st.m)) {
    const float mn = fmaxf(st.m, pm);
    const float alpha = __builtin_amdgcn_exp2f((st.m - mn) * LOG2E);
    st.l *= alpha; st.m = mn;
#pragma unroll
    for (int d = 0; d < 8; ++d) st.acc[d] *= alpha;
  }
  const float mb = st.m * LOG2E;
  float p[8];
#pragma unroll
  for (int e = 0; e < 8; ++e) p[e] = __builtin_amdgcn_exp2f(fmaf(x[e], LOG2E, -mb));
  st.l += ((p[0] + p[1]) + (p[2] + p[3])) + ((p[4] + p[5]) + (p[6] + p[7]));
  const u32x4 pw = {cvtpk(p[0], p[1]), cvtpk(p[2], p[3]), cvtpk(p[4], p[5]), cvtpk(p[6], p[7])};
  const bf16x8 pf = __builtin_bit_cast(bf16x8, pw);
  if (v2) WAIT_VM(24); else if (v1) WAIT_VM(16); else WAIT_VM(0);
  if (XT && SLOT == 0 && t == 2) lds_wait_ge(L, C_XR + aw, 2);
  if (XT == 2 && SLOT == 1 && t == 3) lds_wait_ge(L, C_XR + aw, 4);
#pragma unroll
  for (int d = 0; d < 8; ++d) {
    const bf16x4 v0 = __builtin_amdgcn_ds_read_tr16_b64_v4bf16((LAS bf16x4*)(L + (c.vaddr ^ (d << 5)) + SLOT * 16384));
    const bf16x4 v1_ = __builtin_amdgcn_ds_read_tr16_b64_v4bf16((LAS bf16x4*)(L + (c.vaddr ^ (d << 5)) + SLOT * 16384 + 4096));
    const bf16x8 vf = __builtin_shufflevector(v0, v1_, 0, 1, 2, 3, 4, 5, 6, 7);
    st.acc[d] = __builtin_amdgcn_mfma_f32_16x16x32_bf16(vf, pf, st.acc[d], 0, 0, 0);
  }
  WAIT_LGKM0();
  if (XT && SLOT == 0 && t == 0) { if (lane == 0) lds_st(L, C_XF + aw, 2); }
  else if (XT == 2 && SLOT == 1 && t == 1) { if (lane == 0) lds_st(L, C_XF + aw, 4); }
  else if (v2) { WAIT_VMI(CAPG); DMA_V(c, so2, ld2 + 8192); WAIT_VMI(CAPG); DMA_V(c, so2 + 16384u, ld2 + 8192 + 4096); }
  return v2;
}

template <int TR>
__device__ __forceinline__ f32x4 proj16(LAS unsigned char* L, int lane) {
  const int g = lane >> 4, i = lane & 15;
  unsigned aa = L_XN + (i & 7) * XN_STRIDE + g * 16;
  unsigned ba = L_STG + g * 256 + i * 8;
  f32x4 d0 = {0.f, 0.f, 0.f, 0.f}, d1 = {0.f, 0.f, 0.f, 0.f};
constexpr int kPunr = PUNR;
#pragma unroll kPunr
  for (int s = 0; s < 4; ++s) {
    bf16x8 a[4], bb[4];
#pragma unroll
    for (int k = 0; k < 4; ++k) {
      a[k] = *(LAS bf16x8*)(L + aa + k * 64);
      const bf16x4 b0 = __builtin_amdgcn_ds_read_tr16_b64_v4bf16((LAS bf16x4*)(L + ba + k * 1024));
      const bf16x4 b1 = __builtin_amdgcn_ds_read_tr16_b64_v4bf16((LAS bf16x4*)(L + ba + k * 1024 + 128));
      bb[k] = __builtin_shufflevector(b0, b1, 0, 1, 2, 3, 4, 5, 6, 7);
    }
    if (TR) {
      d0 = __builtin_amdgcn_mfma_f32_16x16x32_bf16(bb[0], a[0], d0, 0, 0, 0); d1 = __builtin_amdgcn_mfma_f32_16x16x32_bf16(bb[1], a[1], d1, 0, 0, 0);
      d0 = __builtin_amdgcn_mfma_f32_16x16x32_bf16(bb[2], a[2], d0, 0, 0, 0); d1 = __builtin_amdgcn_mfma_f32_16x16x32_bf16(bb[3], a[3], d1, 0, 0, 0);
    } else {
      d0 = __builtin_amdgcn_mfma_f32_16x16x32_bf16(a[0], bb[0], d0, 0, 0, 0); d1 = __builtin_amdgcn_mfma_f32_16x16x32_bf16(a[1], bb[1], d1, 0, 0, 0);
      d0 = __builtin_amdgcn_mfma_f32_16x16x32_bf16(a[2], bb[2], d0, 0, 0, 0); d1 = __builtin_amdgcn_mfma_f32_16x16x32_bf16(a[3], bb[3], d1, 0, 0, 0);
    }
    aa += 256; ba += 4096;
  }
  return d0 + d1;
}

__global__ void __launch_bounds__(NTHR, 3)
fused_decode(const unsigned short* __restrict__ x, const unsigned short* __restrict__ kc, const unsigned short* __restrict__ vc,
             const unsigned short* __restrict__ wq, const unsigned short* __restrict__ wo, const unsigned short* __restrict__ wg,
             const unsigned short* __restrict__ wu, const unsigned short* __restrict__ wd, unsigned short* __restrict__ out, unsigned char* ws) {
  extern __shared__ __attribute__((aligned(16))) unsigned char lds[];
  LAS unsigned char* L = (LAS unsigned char*)lds;
  const int w = blockIdx.x;
  const int tid = threadIdx.x, lane = tid & 63, wave = __builtin_amdgcn_readfirstlane(tid >> 6);
  const int g = lane >> 4, i = lane & 15;
  const int xc = w & 7, r = w >> 3;
#define XSH 2
#define BSH 0
  const int b = r & 7, sp = r >> 3, kvh = (xc + XSH * sp + BSH * b) & 7;
  const __amdgpu_buffer_rsrc_t rsW = __builtin_amdgcn_make_buffer_rsrc(ws, 0, WS_END, 0x00020000);

  if (wave < 4) {
    const int aw = wave;
    const u64 ka = (u64)kc, va = (u64)vc;
    AttnConst c;
    c.rsK = u32x4{(unsigned)ka, (unsigned)(ka >> 32), 128u << 20, 0x00020000u};
    c.rsV = u32x4{(unsigned)va, (unsigned)(va >> 32), 128u << 20, 0x00020000u};
    c.gbase = (unsigned)((b * HKV + kvh) * SK * 256);
    c.gstat = c.gbase + (unsigned)(sp * NSTAT) * 32768u + aw * 4096;
    c.cnt = ws + WS_CNT + (b * 8 + kvh) * 128;
    c.ring = L_RING + aw * 32768;
    c.kdef = KDEF(b); c.vdef = VDEF(b);
    c.vk0 = g * 256 + ((i ^ g) << 4);
    c.vv0 = g * 256 + ((i ^ (2 * g)) << 4);
    c.kaddr = c.ring + i * 256 + ((i ^ g) << 4);
    const int q4 = i >> 2, pp = lane & 3;
    c.vaddr = c.ring + 8192 + (4 * g + q4) * 256 + ((8 * (g & 1) + 2 * q4) << 4) + 8 * pp;
    __builtin_amdgcn_s_barrier();
    if (aw == 0) STAMP(ws, w, 0);
#define PCAPW() do { if (lds_ldu(L, C_QREADY)) WAIT_VMI(CAPG); else WAIT_VMI(PCAP); } while (0)
#pragma unroll 1
    for (int u = 0; u < 2; ++u) {
      const unsigned so = c.gstat + u * 32768u, ld = c.ring + u * 16384;
      PCAPW(); DMA_K(c, so, ld);
      PCAPW(); DMA_K(c, so + 16384u, ld + 4096);
      PCAPW(); DMA_V(c, so, ld + 8192);
      if (u == 1 && lane == 0) lds_add(L, C_RING);
      PCAPW(); DMA_V(c, so + 16384u, ld + 8192 + 4096);
    }
    if (aw == 0) STAMP(ws, w, 1);
    lds_wait_ge(L, C_QREADY, 1);
    if (aw == 0) STAMP(ws, w, 2);
#pragma unroll
    for (int s = 0; s < 4; ++s) { u32x4 v = *(LAS u32x4*)(L + L_Q + (i & 3) * 256 + 64 * s + 16 * g); if (i >= 4) v = u32x4{0, 0, 0, 0}; c.qf[s] = __builtin_bit_cast(bf16x8, v); }
    AttnState st;
#pragma unroll
    for (int d = 0; d < 8; ++d) st.acc[d] = f32x4{0.f, 0.f, 0.f, 0.f};
    st.m = -INFINITY; st.l = 0.f;
    unsigned tkv = 0xffffu;
    bool v1 = true;
    if (XT) WAIT_VM(0);
#pragma unroll 1
    for (int t = 0;; t += 2) {
      if (aw == 0 && lane == 0) lds_st(L, C_PROG, t);
      const bool v2 = attn_tile<0>(L, c, st, t, v1, tkv, lane, aw);
      if (!v2 && lane == 0) lds_st(L, C_ATTEND, 1);
      if (!v1) break;
      const bool v3 = attn_tile<1>(L, c, st, t + 1, v2, tkv, lane, aw);
      if (!v3 && lane == 0) lds_st(L, C_ATTEND, 1);
      if (!v2) break;
      v1 = v3;
    }
    lds_wait_ge(L, C_WOREAD, 4);
    if (aw == 0) STAMP(ws, w, 3);
    const float lsum = xrow16_sum(st.l);
    LAS float* mr = (LAS float*)(L + L_STG + aw * MRG_W);
    if (i < 4) {
#pragma unroll
      for (int d = 0; d < 8; ++d) *(LAS f32x4*)(mr + i * 128 + 16 * d + 4 * g) = st.acc[d];
      if (g == 0) { mr[512 + i] = st.m; mr[516 + i] = lsum; }
    }
    WAIT_LGKM0();
    unsigned old = 0;
    if (lane == 0) old = lds_add(L, C_MRG);
    old = __builtin_amdgcn_readfirstlane(old);
    asm volatile("" ::: "memory");
    if (old == 3u) {
      const unsigned T32 = lds_ldu(L, C_TLO);
      float M = -INFINITY;
#pragma unroll
      for (int ww = 0; ww < 4; ++ww) M = fmaxf(M, ((LAS float*)(L + L_STG + ww * MRG_W))[512 + g]);
      float Ls = 0.f; f32x4 o0 = {0.f, 0.f, 0.f, 0.f}, o1 = {0.f, 0.f, 0.f, 0.f};
#pragma unroll
      for (int ww = 0; ww < 4; ++ww) { LAS float* rec = (LAS float*)(L + L_STG + ww * MRG_W);
        const float wt = __builtin_amdgcn_exp2f((rec[512 + g] - M) * 1.4426950408889634f);
        Ls = fmaf(wt, rec[516 + g], Ls);
        o0 += wt * *(LAS f32x4*)(rec + g * 128 + 4 * i); o1 += wt * *(LAS f32x4*)(rec + g * 128 + 64 + 4 * i); }
      const unsigned po = WS_PART + ((b * 32 + kvh * 4 + g) * 4 + sp) * 512 + i * 16;
      __builtin_amdgcn_raw_buffer_store_b128(u32x4{cvtpk(o0[0], o0[1]), T32, cvtpk(o0[2], o0[3]), T32}, rsW, po, 0, 16);
      __builtin_amdgcn_raw_buffer_store_b128(u32x4{cvtpk(o1[0], o1[1]), T32, cvtpk(o1[2], o1[3]), T32}, rsW, po + 256, 0, 16);
      if (i == 0) __builtin_amdgcn_raw_buffer_store_b128(u32x4{__float_as_uint(M), T32, __float_as_uint(Ls), T32}, rsW, WS_ML + (((kvh * 4 + g) * 8 + b) * 4 + sp) * 16, 0, 16);
      STAMP(ws, w, 4);
    }
    return;
  }

  const int hw = wave - 4;
  if (hw == 0) STAMP(ws, w, 8);
  u32x4 xv = *(const GAS u32x4*)(x + hw * DM + lane * 8);
  u64 E = 0;
  if (hw == 0) asm volatile("global_load_dwordx2 %0, %1, %2 sc1" : "=v"(E) : "v"(0u), "s"(ws) : "memory");
  {
    const u64 qa = (u64)wq;
    const u32x4 rsQ = {(unsigned)qa, (unsigned)(qa >> 32), 4u << 20, 0x00020000u};
    const unsigned n0 = xc * 512 + r * 16;
    const unsigned vo = (lane >> 1) * 8192 + (lane & 1) * 16;
    dma1(vo, rsQ, (2 * hw) * 262144u + n0 * 2, L_STG + (2 * hw) * 1024);
    dma1(vo, rsQ, (2 * hw + 1) * 262144u + n0 * 2, L_STG + (2 * hw + 1) * 1024);
  }
  if (hw == 3) {
    if (lane < 32) lds_st(L, lane, 0u);
    if (NSTAT < 16 && r < 8 && lane == 0) __hip_atomic_store((GAS unsigned*)(ws + WS_CNT + (r * 8 + xc) * 128), 0u, RLX_AGENT);
    WAIT_LGKM0();
  }
  __builtin_amdgcn_s_barrier();
  asm volatile("" : "+v"(xv) :: "memory");
  {
    float f[8];
#pragma unroll
    for (int k = 0; k < 4; ++k) { f[2 * k] = lo16(xv[k]); f[2 * k + 1] = hi16(xv[k]); }
    float s = ((f[0] + f[1]) + (f[2] + f[3])) + ((f[4] + f[5]) + (f[6] + f[7]));
    s = wave_sum(s);
    const float mean = s * (1.f / DM);
    float q = 0.f;
#pragma unroll
    for (int k = 0; k < 8; ++k) { const float d = f[k] - mean; q = fmaf(d, d, q); }
    q = wave_sum(q);
    const float var = rbf(q * (1.f / DM));
    const float mb = rbf(mean);
    const float sd = rbf(sqrtf(rbf(var + 1e-5f)));
    u32x4 o;
#pragma unroll
    for (int k = 0; k < 4; ++k) o[k] = cvtpk(rbf(f[2 * k] - mb) / sd, rbf(f[2 * k + 1] - mb) / sd);
    *(LAS u32x4*)(L + L_XN + hw * XN_STRIDE + lane * 16) = o;
  }
  asm volatile("s_waitcnt vmcnt(0)" : "+v"(E) :: "memory");
  if (hw == 0 && lane == 0) { const u64 T = E + 1; lds_st(L, C_TLO, (unsigned)T); lds_st(L, C_THI, (unsigned)(T >> 32)); }
  WAIT_LGKM0();
  if (lane == 0) lds_add(L, C_LN);
  if (hw == 0) lds_wait_ge<1>(L, C_LN, 8); else lds_wait_ge<6>(L, C_LN, 8);
  const unsigned Tlo = lds_ldu(L, C_TLO), Thi = lds_ldu(L, C_THI);
  const unsigned tg_lo = Tlo << 16, tg_hi = (Thi << 16) | (Tlo >> 16);
  const u64 Tu = ((u64)Thi << 32) | Tlo;
#define TAG_OK(lo_, hi_) ((((lo_) ^ tg_lo) >> 16) == 0 && (hi_) == tg_hi)
  const int gam = xc + 8 * sp, f0 = gam * 64 + (r & 7) * 8;
  const int hd = 4 * xc + sp, cbk = r & 7;

  if (hw == 0) {
    if (PRIO) __builtin_amdgcn_s_setprio(3);
    STAMP(ws, w, 9);
    const f32x4 d = proj16<1>(L, lane);
    const unsigned pk = i < 8 ? cvtpk(d[0], d[1]) : cvtpk(d[2], d[3]);
    __builtin_amdgcn_raw_buffer_store_b128(u32x4{tg_lo | (pk & 0xffffu), tg_hi, tg_lo | (pk >> 16), tg_hi}, rsW,
                                           WS_QG + ((i & 7) * 4096 + xc * 512 + r * 16 + 4 * g + 2 * (i >> 3)) * 8, 0, 16);
    if (lane == 0) lds_st(L, C_STGFREE, 1);
    if (PRIO) __builtin_amdgcn_s_setprio(0);
    STAMP(ws, w, 10);
  } else if (hw == 1) {
    lds_wait_ge<8>(L, C_GUST, 4);
    if (gam < 28) {
      const f32x4 d = proj16<0>(L, lane);
      float hv[2];
#pragma unroll
      for (int k = 0; k < 2; ++k) {
        const float own = rbf(g < 2 ? d[k] : d[2 + k]);
        const float oth = dpp<XOR8>(own);
        hv[k] = own > 0.f ? own * oth : 0.f;
      }
      const unsigned pk = cvtpk(hv[0], hv[1]);
      const int b0 = 4 * (g & 1) + (g < 2 ? 0 : 2);
      if (i < 8) __builtin_amdgcn_raw_buffer_store_b128(u32x4{tg_lo | (pk & 0xffffu), tg_hi, tg_lo | (pk >> 16), tg_hi}, rsW, WS_HG + ((f0 + i) * 8 + b0) * 8, 0, 16);
    }
    if (lane == 0) lds_st(L, C_HDONE, 1);
    STAMP(ws, w, 17);
  } else if (hw == 2) {
    const unsigned qo = WS_QG + (b * 4096 + kvh * 512) * 8 + lane * 16;
    bool okk[4] = {false, false, false, false};
    lds_wait_ge<4>(L, C_STGFREE, 1);
#pragma unroll 1
    for (unsigned spins = 0; spins < SPIN_CAP; ++spins) {
      asm volatile("" ::: "memory");
      u32x4 v[4];
#pragma unroll
      for (int k = 0; k < 4; ++k) if (!okk[k]) v[k] = __builtin_amdgcn_raw_buffer_load_b128(rsW, qo + k * 1024, 0, 16);
#pragma unroll
      for (int k = 0; k < 4; ++k) if (!okk[k]) {
        okk[k] = TAG_OK(v[k][0], v[k][1]) && TAG_OK(v[k][2], v[k][3]);
        *(LAS unsigned*)(L + L_Q + (lane + 64 * k) * 4) = (v[k][0] & 0xffffu) | (v[k][2] << 16);
      }
      if (__all(okk[0] && okk[1] && okk[2] && okk[3])) break;
      __builtin_amdgcn_s_sleep(1);
    }
    WAIT_LGKM0();
    if (lane == 0) lds_st(L, C_QREADY, 1);
    STAMP(ws, w, 11);
  } else if (hw == 3) {
  } else {
    const int h4 = hw - 4;
    const __amdgpu_buffer_rsrc_t rsD = __builtin_amdgcn_make_buffer_rsrc((void*)wd, 0, FF * DM * 2, 0x00020000);
    const GAS unsigned short* sgu = (const GAS unsigned short*)((lane & 1) ? wu : wg) + (size_t)(128 * h4 + (lane >> 1)) * FF + (gam < 28 ? f0 : 0);
    const GAS unsigned short* swo = (const GAS unsigned short*)wo + (size_t)(128 * hd + 32 * h4 + (lane >> 3)) * DM + cbk * 64 + (lane & 7) * 8;
    u32x4 rg[4], ro[4], rd_[2];
    u32x4 xk[8], xvv[8];
    {
      const __amdgpu_buffer_rsrc_t rK = __builtin_amdgcn_make_buffer_rsrc((void*)kc, 0, 128u << 20, 0x00020000), rV = __builtin_amdgcn_make_buffer_rsrc((void*)vc, 0, 128u << 20, 0x00020000);
      const unsigned so = (unsigned)((b * HKV + kvh) * SK * 256) + (unsigned)(sp * NSTAT + 2) * 32768u + h4 * 4096;
      const unsigned vk0 = g * 256 + ((i ^ g) << 4), vv0 = g * 256 + ((i ^ (2 * g)) << 4);
      lds_wait_ge<4>(L, C_RING, XTW);
#pragma unroll
      for (int u = 0; u < 8; ++u) { if ((u & 3) == 0) WAIT_VMI(HCAP); xk[u] = __builtin_amdgcn_raw_buffer_load_b128(rK, (vk0 ^ ((u & 3) << 6)) + (u & 3) * 1024 + (u >> 2) * 16384, so, 2); }
#pragma unroll
      for (int u = 0; u < 8; ++u) { if ((u & 3) == 0) WAIT_VMI(HCAP); xvv[u] = __builtin_amdgcn_raw_buffer_load_b128(rV, (vv0 ^ ((u & 1) << 7)) + (u & 3) * 1024 + (u >> 2) * 16384, so, 2); }
      WAIT_VMI(HCAP);
    }
#pragma unroll
    for (int u = 0; u < 4; ++u) ro[u] = __builtin_nontemporal_load((const GAS u32x4*)(swo + (size_t)u * 8 * DM));
#pragma unroll
    for (int u = 0; u < 2; ++u) rd_[u] = __builtin_amdgcn_raw_buffer_load_b128(rsD, (unsigned)((56 * hd + 16 * h4 + 8 * u + (lane >> 3)) * 1024 + cbk * 128 + (lane & 7) * 16), 0, 2);
    lds_wait_ge<2>(L, C_XF + h4, 1);
#pragma unroll
    for (int u = 0; u < 8; ++u) *(LAS u32x4*)(L + L_RING + h4 * 32768 + u * 1024 + lane * 16) = xk[u];
    WAIT_LGKM0();
    if (lane == 0) lds_st(L, C_XR + h4, 1);
    lds_wait_ge<1>(L, C_XF + h4, 2);
#pragma unroll
    for (int u = 0; u < 8; ++u) *(LAS u32x4*)(L + L_RING + h4 * 32768 + 8192 + u * 1024 + lane * 16) = xvv[u];
    WAIT_LGKM0();
    if (lane == 0) lds_st(L, C_XR + h4, 2);
#pragma unroll
    for (int u = 0; u < 4; ++u) rg[u] = *(const GAS u32x4*)(sgu + (size_t)u * 32 * FF);
    lds_wait_ge(L, C_STGFREE, 1);
#pragma unroll
    for (int u = 0; u < 4; ++u) *(LAS u32x4*)(L + L_STG + (4 * h4 + u) * 1024 + lane * 16) = rg[u];
    WAIT_LGKM0();
    if (lane == 0) lds_add(L, C_GUST);
    lds_wait_ge<4>(L, C_HDONE, 1);
#pragma unroll
    for (int u = 0; u < 4; ++u) *(LAS u32x4*)(L + L_STG + (4 * h4 + u) * 1024 + lane * 16) = ro[u];
#pragma unroll
    for (int u = 0; u < 2; ++u) *(LAS u32x4*)(L + L_XN + (2 * h4 + u) * 1024 + lane * 16) = rd_[u];
    WAIT_LGKM0();
    if (lane == 0) lds_add(L, C_WOLD);
  }
  {
    const int fl = 7 * hw + (lane >> 3), bb = lane & 7;
    const unsigned ho = WS_HG + ((56 * hd + 7 * hw) * 8 + (lane < 56 ? lane : 0)) * 8;
    u32x2 hv = {0u, 0u};
    bool ok = false;
    lds_wait_ge<32>(L, C_PROG, GATE_H);
#pragma unroll 1
    for (unsigned spins = 0; spins < SPIN_CAP; ++spins) {
      asm volatile("" ::: "memory");
      if (!ok) { hv = __builtin_amdgcn_raw_buffer_load_b64(rsW, ho, 0, 16); ok = TAG_OK(hv[0], hv[1]); }
      if (__all(ok)) break;
      __builtin_amdgcn_s_sleep(SLP_MID);
    }
    if (lane < 56) *(LAS unsigned short*)(L + L_OA + bb * OA_STRIDE + fl * 2) = (unsigned short)(hv[0] & 0xffffu);
    if (lane < 4) *(LAS unsigned*)(L + L_OA + hw * OA_STRIDE + 112 + lane * 4) = 0u;
    WAIT_LGKM0();
    if (lane == 0) lds_add(L, C_OG);
  }
  const int bf = r >> 2, cb = 4 * xc + (r & 3), n1 = cb * 16;
  const unsigned ba = (g * 8 + (i >> 2)) * 128 + (lane & 3) * 8 + (hw & 3) * 32;
  const unsigned oa = L_OA + (i & 7) * OA_STRIDE + g * 16;
  bf16x8 wf[4];
  unsigned y2p = 0;
  float xr[2] = {0.f, 0.f};
  if (hw < 4) {
    lds_wait_ge<8>(L, C_WOLD, 4);
    lds_wait_ge<4>(L, C_OG, 8);
    f32x4 d = {0.f, 0.f, 0.f, 0.f};
#pragma unroll
    for (int s = 0; s < 2; ++s) {
      const bf16x4 a0 = __builtin_amdgcn_ds_read_tr16_b64_v4bf16((LAS bf16x4*)(L + L_XN + ba + s * 4096));
      const bf16x4 a1 = __builtin_amdgcn_ds_read_tr16_b64_v4bf16((LAS bf16x4*)(L + L_XN + ba + s * 4096 + 512));
      d = __builtin_amdgcn_mfma_f32_16x16x32_bf16(__builtin_shufflevector(a0, a1, 0, 1, 2, 3, 4, 5, 6, 7), *(LAS bf16x8*)(L + oa + s * 64), d, 0, 0, 0);
    }
    y2p = i < 8 ? cvtpk(d[0], d[1]) : cvtpk(d[2], d[3]);
#pragma unroll
    for (int s = 0; s < 4; ++s) {
      const bf16x4 a0 = __builtin_amdgcn_ds_read_tr16_b64_v4bf16((LAS bf16x4*)(L + L_STG + ba + s * 4096));
      const bf16x4 a1 = __builtin_amdgcn_ds_read_tr16_b64_v4bf16((LAS bf16x4*)(L + L_STG + ba + s * 4096 + 512));
      wf[s] = __builtin_shufflevector(a0, a1, 0, 1, 2, 3, 4, 5, 6, 7);
    }
    asm volatile("s_waitcnt lgkmcnt(0)" : "+v"(wf[0]), "+v"(wf[1]), "+v"(wf[2]), "+v"(wf[3]), "+v"(y2p) :: "memory");
    if (lane == 0) lds_add(L, C_WOREAD);
    if (hw == 0) STAMP(ws, w, 12);
  } else if (hw == 6) {
    const unsigned xq = *(const GAS unsigned*)(x + bf * DM + n1 + (lane & 7) * 2);
    xr[0] = lo16(xq); xr[1] = hi16(xq);
  } else if (hw == 5 && w == 0) {
#pragma unroll 1
    for (unsigned spins = 0; spins < SPIN_CAP; ++spins) {
      bool ok = true;
#pragma unroll
      for (int k = 0; k < 4; ++k) { const u32x2 v = __builtin_amdgcn_raw_buffer_load_b64(rsW, WS_QG + (lane + 64 * k) * 128, 0, 16); ok &= TAG_OK(v[0], v[1]); }
      if (__all(ok)) break;
      __builtin_amdgcn_s_sleep(SLP_MID);
      asm volatile("" ::: "memory");
    }
    if (lane == 0) __hip_atomic_store((GAS u64*)(ws + WS_EPOCH), Tu, RLX_AGENT);
  }
  {
    lds_wait_ge<4>(L, C_WOREAD, 4);
    lds_wait_ge<16>(L, C_ATTEND, 1);
    if (hw == 0) STAMP(ws, w, 13);
    const unsigned po = WS_PART + ((hw * 32 + hd) * 4 + g) * 512 + i * 16;
    const unsigned mo = WS_ML + ((hd * 8 + hw) * 4 + g) * 16;
    u32x4 pv0 = {0u, 0u, 0u, 0u}, pv1 = {0u, 0u, 0u, 0u}, ml = {0u, 0u, 0u, 0u};
    if (hw == 7) {
      bool ok = lane >= 32;
#pragma unroll 1
      for (unsigned spins = 0; spins < SPIN_CAP; ++spins) {
        asm volatile("" ::: "memory");
        if (!ok) {
          ml = __builtin_amdgcn_raw_buffer_load_b128(rsW, WS_ML + hd * 512 + lane * 16, 0, 16);
          ok = ml[1] == Tlo && ml[3] == Tlo;
        }
        const unsigned miss = ~(unsigned)__ballot(ok);
        if (lane == 0) lds_st(L, C_MLMASK, ~miss);
        unsigned cnt = (miss & 0x55555555u) + ((miss >> 1) & 0x55555555u);
        cnt = (cnt & 0x33333333u) + ((cnt >> 2) & 0x33333333u);
        if ((((cnt + (0x11111111u * (3u + MLG))) & 0x88888888u)) == 0u) break;
        __builtin_amdgcn_s_sleep(SLP_PART);
      }
      if (lane == 0) lds_st(L, C_MLMASK, 0xffffffffu);
    } else {
      while (__builtin_popcount((lds_ldu(L, C_MLMASK) >> (4 * hw)) & 15u) < MLG) __builtin_amdgcn_s_sleep(1);
      asm volatile("" ::: "memory");
    }
    bool ok = false;
#pragma unroll 1
    for (unsigned spins = 0; spins < SPIN_CAP; ++spins) {
      asm volatile("" ::: "memory");
      if (!ok) {
        pv0 = __builtin_amdgcn_raw_buffer_load_b128(rsW, po, 0, 16);
        pv1 = __builtin_amdgcn_raw_buffer_load_b128(rsW, po + 256, 0, 16);
        ml = __builtin_amdgcn_raw_buffer_load_b128(rsW, mo, 0, 16);
        ok = pv0[1] == Tlo && pv0[3] == Tlo && pv1[1] == Tlo && pv1[3] == Tlo && ml[1] == Tlo && ml[3] == Tlo;
      }
      if (__all(ok)) break;
      __builtin_amdgcn_s_sleep(SLP_DIR);
    }
    if (hw == 0) STAMP(ws, w, 14);
    const float mj = __uint_as_float(ml[0]);
    const float M = xrow16_max(mj);
    const float wt = __builtin_amdgcn_exp2f((mj - M) * 1.4426950408889634f);
    const float inv = 1.f / xrow16_sum(wt * __uint_as_float(ml[2]));
    const u32x2 oa0 = {cvtpk(xrow16_sum(wt * lo16(pv0[0])) * inv, xrow16_sum(wt * hi16(pv0[0])) * inv), cvtpk(xrow16_sum(wt * lo16(pv0[2])) * inv, xrow16_sum(wt * hi16(pv0[2])) * inv)};
    const u32x2 oa1 = {cvtpk(xrow16_sum(wt * lo16(pv1[0])) * inv, xrow16_sum(wt * hi16(pv1[0])) * inv), cvtpk(xrow16_sum(wt * lo16(pv1[2])) * inv, xrow16_sum(wt * hi16(pv1[2])) * inv)};
    if (g == 0) { *(LAS u32x2*)(L + L_OA + hw * OA_STRIDE + i * 8) = oa0; *(LAS u32x2*)(L + L_OA + hw * OA_STRIDE + 128 + i * 8) = oa1; }
    WAIT_LGKM0();
    if (lane == 0) lds_add(L, C_OG);
  }
  if (hw < 4) {
    lds_wait_ge(L, C_OG, 16);
    f32x4 d = {0.f, 0.f, 0.f, 0.f};
#pragma unroll
    for (int s = 0; s < 4; ++s) d = __builtin_amdgcn_mfma_f32_16x16x32_bf16(wf[s], *(LAS bf16x8*)(L + oa + s * 64), d, 0, 0, 0);
    const unsigned yp = i < 8 ? cvtpk(d[0], d[1]) : cvtpk(d[2], d[3]);
    const unsigned yo = ((((i & 7) * 32 + 4 * cbk + hw) * 32 + hd) * 16 + 4 * g + 2 * (i >> 3)) * 8;
    __builtin_amdgcn_raw_buffer_store_b128(u32x4{(yp & 0xffffu) | (y2p << 16), Tlo, (yp >> 16) | (y2p & 0xffff0000u), Tlo}, rsW, WS_Y + yo, 0, 16);
    if (hw == 0) STAMP(ws, w, 16);
  } else if (hw == 6) {
    const unsigned io = WS_Y + (bf * 32 + cb) * 4096 + lane * 16;
    u32x4 y[4];
    bool okk[4] = {false, false, false, false};
#pragma unroll 1
    for (unsigned spins = 0; spins < SPIN_CAP; ++spins) {
      asm volatile("" ::: "memory");
#pragma unroll
      for (int k = 0; k < 4; ++k) if (!okk[k]) y[k] = __builtin_amdgcn_raw_buffer_load_b128(rsW, io + k * 1024, 0, 16);
#pragma unroll
      for (int k = 0; k < 4; ++k) okk[k] = y[k][1] == Tlo && y[k][3] == Tlo;
      if (__all(okk[0] && okk[1] && okk[2] && okk[3])) break;
      __builtin_amdgcn_s_sleep(SLP_Y);
    }
    float o[2];
#pragma unroll
    for (int k = 0; k < 2; ++k) {
      float t = (lo16(y[0][2 * k]) + lo16(y[1][2 * k])) + (lo16(y[2][2 * k]) + lo16(y[3][2 * k]));
      float u = (hi16(y[0][2 * k]) + hi16(y[1][2 * k])) + (hi16(y[2][2 * k]) + hi16(y[3][2 * k]));
      t += dpp<XOR8>(t); t = rbf(xrow16_sum(t));
      u += dpp<XOR8>(u); u = rbf(xrow16_sum(u));
      const float gate = rbf(u / (1.f + __expf(-u)));
      o[k] = xr[k] + rbf(t * gate);
    }
    if (lane < 8) *(GAS unsigned*)(out + bf * DM + n1 + lane * 2) = cvtpk(o[0], o[1]);
    STAMP(ws, w, 15);
    STAMP_FLUSH(ws, w);
  }
}

extern "C" void kernel_launch(void* const* d_in, const int* in_sizes, int n_in, void* d_out, int out_size, void* d_ws, size_t ws_size, hipStream_t stream) {
  const unsigned short* x = (const unsigned short*)d_in[0];
  const unsigned short* kc = (const unsigned short*)d_in[1];
  const unsigned short* vc = (const unsigned short*)d_in[2];
  const unsigned short* wq = (const unsigned short*)d_in[3];
  const unsigned short* wo = (const unsigned short*)d_in[4];
  const unsigned short* wg = (const unsigned short*)d_in[6];
  const unsigned short* wu = (const unsigned short*)d_in[7];
  const unsigned short* wd = (const unsigned short*)d_in[8];
  (void)hipFuncSetAttribute((const void*)fused_decode, hipFuncAttributeMaxDynamicSharedMemorySize, L_END);
  fused_decode<<<dim3(NWG), dim3(NTHR), L_END, stream>>>(x, kc, vc, wq, wo, wg, wu, wd, (unsigned short*)d_out, (unsigned char*)d_ws);
}
```

```cpp
#include <hip/hip_runtime.h>
#include <hip/hip_bf16.h>
#include <cstdint>

typedef __bf16 bf16x2 __attribute__((ext_vector_type(2)));
typedef __bf16 bf16x4 __attribute__((ext_vector_type(4)));
typedef __bf16 bf16x8 __attribute__((ext_vector_type(8)));
typedef unsigned u32x2 __attribute__((ext_vector_type(2)));
typedef unsigned u32x4 __attribute__((ext_vector_type(4)));
typedef float f32x2 __attribute__((ext_vector_type(2)));
typedef float f32x4 __attribute__((ext_vector_type(4)));
typedef unsigned long long u64;
#define LAS __attribute__((address_space(3)))
#define GAS __attribute__((address_space(1)))
#define RLX_AGENT __ATOMIC_RELAXED, __HIP_MEMORY_SCOPE_AGENT
#define RLX_WG __ATOMIC_RELAXED, __HIP_MEMORY_SCOPE_WORKGROUP

constexpr int NB = 8, DM = 512, HKV = 8, SK = 8192, FF = 1792;
constexpr int NWG = 256, NTHR = 768;
constexpr int NT_TILES = 16;

#define DLYQ 200
#define CAPG 6
#define PCAP 2
#define HCAP 2
#define GATE_H 4
#define SLP_PART 4
#define SLP_DIR 2
#define SLP_Y 2
#define SLP_MID 64
#define EARLYK 0

constexpr unsigned WS_EPOCH = 0;
constexpr unsigned WS_ML = 8192;
constexpr unsigned WS_QG = 32768;
constexpr unsigned WS_HG = WS_QG + NB * 4096 * 8;
constexpr unsigned WS_PART = WS_HG + NB * FF * 8;
constexpr unsigned WS_Y = WS_PART + NWG * 2048;
constexpr unsigned WS_END = WS_Y + (1u << 20);
constexpr unsigned WS_STAMP = 4u << 20;

constexpr unsigned L_RING = 0;
constexpr unsigned L_STG = 131072;
constexpr unsigned L_XN = L_STG + 16384;
constexpr unsigned XN_STRIDE = 1040;
constexpr unsigned L_Q = L_XN + 8 * XN_STRIDE;
constexpr unsigned L_OA = L_Q + 1024;
constexpr unsigned OA_STRIDE = 272;
constexpr unsigned L_CTL = L_OA + 8 * OA_STRIDE;
constexpr unsigned L_STAMP = L_CTL + 128;
constexpr unsigned L_END = L_STAMP + 256;
static_assert(L_END <= 163840, "LDS");
enum { C_LN = 0, C_STGFREE, C_QREADY, C_HDONE, C_MRG, C_ATTEND, C_TLO, C_THI, C_WOLD, C_WOREAD, C_OG, C_PROG, C_MLMASK, C_RING, C_GUST,
       C_XF = 20  , C_XR = 24   };

__device__ __forceinline__ unsigned cvtpk(float a, float b) {
  bf16x2 t = __builtin_convertvector(f32x2{a, b}, bf16x2);
  return __builtin_bit_cast(unsigned, t);
}
__device__ __forceinline__ float lo16(unsigned u) { return __builtin_bit_cast(float, u << 16); }
__device__ __forceinline__ float hi16(unsigned u) { return __builtin_bit_cast(float, u & 0xffff0000u); }
__device__ __forceinline__ float rbf(float f) { return lo16(cvtpk(f, f)); }
template <int CTRL> __device__ __forceinline__ float dpp(float x) {
  return __builtin_bit_cast(float, __builtin_amdgcn_mov_dpp(__builtin_bit_cast(int, x), CTRL, 0xf, 0xf, true));
}
constexpr int XOR1 = 0xB1, XOR2 = 0x4E, XOR7 = 0x141, XOR8 = 0x128;
__device__ __forceinline__ float xrow16_max(float x) {
  auto s = __builtin_amdgcn_permlane16_swap(__float_as_uint(x), __float_as_uint(x), false, false);
  x = fmaxf(__uint_as_float(s[0]), __uint_as_float(s[1]));
  auto t = __builtin_amdgcn_permlane32_swap(__float_as_uint(x), __float_as_uint(x), false, false);
  return fmaxf(__uint_as_float(t[0]), __uint_as_float(t[1]));
}
__device__ __forceinline__ float xrow16_sum(float x) {
  auto s = __builtin_amdgcn_permlane16_swap(__float_as_uint(x), __float_as_uint(x), false, false);
  x = __uint_as_float(s[0]) + __uint_as_float(s[1]);
  auto t = __builtin_amdgcn_permlane32_swap(__float_as_uint(x), __float_as_uint(x), false, false);
  return __uint_as_float(t[0]) + __uint_as_float(t[1]);
}
__device__ __forceinline__ float wave_sum(float x) {
  x += dpp<XOR1>(x); x += dpp<XOR2>(x); x += dpp<XOR7>(x); x += dpp<XOR8>(x);
  return xrow16_sum(x);
}

__device__ __forceinline__ void dma4(unsigned v0, unsigned v1, unsigned v2, unsigned v3, u32x4 rs, unsigned so, unsigned ldsaddr) {
  unsigned keep;
  asm volatile(
    "s_mov_b32 %0, m0\n\t"
    "s_mov_b32 m0, %7\n\t"
    "s_nop 0\n\t"
    "buffer_load_dwordx4 %1, %5, %6 offen nt lds\n\t"
    "buffer_load_dwordx4 %2, %5, %6 offen offset:1024 nt lds\n\t"
    "buffer_load_dwordx4 %3, %5, %6 offen offset:2048 nt lds\n\t"
    "buffer_load_dwordx4 %4, %5, %6 offen offset:3072 nt lds\n\t"
    "s_mov_b32 m0, %0"
    : "=&s"(keep) : "v"(v0), "v"(v1), "v"(v2), "v"(v3), "s"(rs), "s"(so), "s"(ldsaddr) : "memory");
}
__device__ __forceinline__ void dma1(unsigned v0, u32x4 rs, unsigned so, unsigned ldsaddr) {
  unsigned keep;
  asm volatile(
    "s_mov_b32 %0, m0\n\t"
    "s_mov_b32 m0, %4\n\t"
    "s_nop 0\n\t"
    "buffer_load_dwordx4 %1, %2, %3 offen lds\n\t"
    "s_mov_b32 m0, %0"
    : "=&s"(keep) : "v"(v0), "s"(rs), "s"(so), "s"(ldsaddr) : "memory");
}
#define WAIT_VM(n) asm volatile("s_waitcnt vmcnt(" #n ")" ::: "memory")
#define WAIT_VMI(n) asm volatile("s_waitcnt vmcnt(%0)" :: "i"(n) : "memory")
#define WAIT_LGKM0() asm volatile("s_waitcnt lgkmcnt(0)" ::: "memory")

__device__ __forceinline__ unsigned lds_ld(LAS unsigned char* L, int idx) { return __hip_atomic_load((LAS unsigned*)(L + L_CTL) + idx, RLX_WG); }
__device__ __forceinline__ unsigned lds_ldu(LAS unsigned char* L, int idx) { return (unsigned)__builtin_amdgcn_readfirstlane(lds_ld(L, idx)); }
__device__ __forceinline__ void lds_st(LAS unsigned char* L, int idx, unsigned v) { __hip_atomic_store((LAS unsigned*)(L + L_CTL) + idx, v, RLX_WG); }
__device__ __forceinline__ unsigned lds_add(LAS unsigned char* L, int idx) { return __hip_atomic_fetch_add((LAS unsigned*)(L + L_CTL) + idx, 1u, RLX_WG); }
template <int SLP = 1>
__device__ __forceinline__ void lds_wait_ge(LAS unsigned char* L, int idx, unsigned want) {
  while (lds_ldu(L, idx) < want) __builtin_amdgcn_s_sleep(SLP);
  asm volatile("" ::: "memory");
}
constexpr unsigned SPIN_CAP = 1u << 15;

#define STAMP(k) do {} while (0)
#define STAMP_FLUSH() do {} while (0)

struct AttnState { f32x4 acc[8]; float m, l; };
struct AttnConst { u32x4 rsK, rsV; unsigned gbase, ring, vk0, vv0, kaddr, vaddr; bf16x8 qf[4]; };

#define DMA_K(c_, so_, ld_) dma4((c_).vk0, (c_).vk0 ^ 64, (c_).vk0 ^ 128, (c_).vk0 ^ 192, (c_).rsK, (so_), (ld_))
#define DMA_V(c_, so_, ld_) dma4((c_).vv0, (c_).vv0 ^ 128, (c_).vv0, (c_).vv0 ^ 128, (c_).rsV, (so_), (ld_))

template <int SLOT>
__device__ __forceinline__ void attn_tile(LAS unsigned char* L, const AttnConst& c, AttnState& st, const int t, const int rem, const int lane, const int aw) {
  constexpr float SCALE = 0.088388347648318440f, LOG2E = 1.4426950408889634f;
  const unsigned so2 = c.gbase + (unsigned)(t + 2) * 32768u, ld2 = c.ring + SLOT * 16384;
  if (rem >= 1) WAIT_VM(24); else WAIT_VM(8);
  if (SLOT == 0 && t == 2) lds_wait_ge(L, C_XR + aw, 1);
  f32x4 s0 = {0.f, 0.f, 0.f, 0.f}, s1 = {0.f, 0.f, 0.f, 0.f};
#pragma unroll
  for (int s = 0; s < 4; ++s) {
    const bf16x8 k0 = *(LAS bf16x8*)(L + (c.kaddr ^ (s << 6)) + SLOT * 16384);
    const bf16x8 k1 = *(LAS bf16x8*)(L + (c.kaddr ^ (s << 6)) + SLOT * 16384 + 4096);
    s0 = __builtin_amdgcn_mfma_f32_16x16x32_bf16(k0, c.qf[s], s0, 0, 0, 0);
    s1 = __builtin_amdgcn_mfma_f32_16x16x32_bf16(k1, c.qf[s], s1, 0, 0, 0);
  }
  WAIT_LGKM0();
  if (SLOT == 0 && t == 0) { if (lane == 0) lds_st(L, C_XF + aw, 1); }
  else if (rem >= 2) { WAIT_VMI(CAPG); DMA_K(c, so2, ld2); WAIT_VMI(CAPG); DMA_K(c, so2 + 16384u, ld2 + 4096); }
  float x[8];
  { unsigned u;
    u = cvtpk(s0[0], s0[1]); u = cvtpk(lo16(u) * SCALE, hi16(u) * SCALE); x[0] = lo16(u); x[1] = hi16(u);
    u = cvtpk(s0[2], s0[3]); u = cvtpk(lo16(u) * SCALE, hi16(u) * SCALE); x[2] = lo16(u); x[3] = hi16(u);
    u = cvtpk(s1[0], s1[1]); u = cvtpk(lo16(u) * SCALE, hi16(u) * SCALE); x[4] = lo16(u); x[5] = hi16(u);
    u = cvtpk(s1[2], s1[3]); u = cvtpk(lo16(u) * SCALE, hi16(u) * SCALE); x[6] = lo16(u); x[7] = hi16(u); }
  float pm = fmaxf(fmaxf(fmaxf(x[0], x[1]), fmaxf(x[2], x[3])), fmaxf(fmaxf(x[4], x[5]), fmaxf(x[6], x[7])));
  pm = xrow16_max(pm);
  if (__any(pm > st.m)) {
    const float mn = fmaxf(st.m, pm);
    const float alpha = __builtin_amdgcn_exp2f((st.m - mn) * LOG2E);
    st.l *= alpha; st.m = mn;
#pragma unroll
    for (int d = 0; d < 8; ++d) st.acc[d] *= alpha;
  }
  const float mb = st.m * LOG2E;
  float p[8];
#pragma unroll
  for (int e = 0; e < 8; ++e) p[e] = __builtin_amdgcn_exp2f(fmaf(x[e], LOG2E, -mb));
  st.l += ((p[0] + p[1]) + (p[2] + p[3])) + ((p[4] + p[5]) + (p[6] + p[7]));
  const u32x4 pw = {cvtpk(p[0], p[1]), cvtpk(p[2], p[3]), cvtpk(p[4], p[5]), cvtpk(p[6], p[7])};
  const bf16x8 pf = __builtin_bit_cast(bf16x8, pw);
  if (rem >= 2) WAIT_VM(24); else if (rem == 1) WAIT_VM(16); else WAIT_VM(0);
  if (SLOT == 0 && t == 2) lds_wait_ge(L, C_XR + aw, 2);
#pragma unroll
  for (int d = 0; d < 8; ++d) {
    const bf16x4 v0 = __builtin_amdgcn_ds_read_tr16_b64_v4bf16((LAS bf16x4*)(L + (c.vaddr ^ (d << 5)) + SLOT * 16384));
    const bf16x4 v1 = __builtin_amdgcn_ds_read_tr16_b64_v4bf16((LAS bf16x4*)(L + (c.vaddr ^ (d << 5)) + SLOT * 16384 + 4096));
    const bf16x8 vf = __builtin_shufflevector(v0, v1, 0, 1, 2, 3, 4, 5, 6, 7);
    st.acc[d] = __builtin_amdgcn_mfma_f32_16x16x32_bf16(vf, pf, st.acc[d], 0, 0, 0);
  }
  WAIT_LGKM0();
  if (SLOT == 0 && t == 0) { if (lane == 0) lds_st(L, C_XF + aw, 2); }
  else if (rem >= 2) { WAIT_VMI(CAPG); DMA_V(c, so2, ld2 + 8192); WAIT_VMI(CAPG); DMA_V(c, so2 + 16384u, ld2 + 8192 + 4096); }
}

template <int TR>
__device__ __forceinline__ f32x4 proj16(LAS unsigned char* L, int lane) {
  const int g = lane >> 4, i = lane & 15;
  unsigned aa = L_XN + (i & 7) * XN_STRIDE + g * 16;
  unsigned ba = L_STG + g * 256 + i * 8;
  f32x4 d0 = {0.f, 0.f, 0.f, 0.f}, d1 = {0.f, 0.f, 0.f, 0.f};
#pragma unroll 2
  for (int s = 0; s < 4; ++s) {
    bf16x8 a[4], bb[4];
#pragma unroll
    for (int k = 0; k < 4; ++k) {
      a[k] = *(LAS bf16x8*)(L + aa + k * 64);
      const bf16x4 b0 = __builtin_amdgcn_ds_read_tr16_b64_v4bf16((LAS bf16x4*)(L + ba + k * 1024));
      const bf16x4 b1 = __builtin_amdgcn_ds_read_tr16_b64_v4bf16((LAS bf16x4*)(L + ba + k * 1024 + 128));
      bb[k] = __builtin_shufflevector(b0, b1, 0, 1, 2, 3, 4, 5, 6, 7);
    }
    if (TR) {
      d0 = __builtin_amdgcn_mfma_f32_16x16x32_bf16(bb[0], a[0], d0, 0, 0, 0); d1 = __builtin_amdgcn_mfma_f32_16x16x32_bf16(bb[1], a[1], d1, 0, 0, 0);
      d0 = __builtin_amdgcn_mfma_f32_16x16x32_bf16(bb[2], a[2], d0, 0, 0, 0); d1 = __builtin_amdgcn_mfma_f32_16x16x32_bf16(bb[3], a[3], d1, 0, 0, 0);
    } else {
      d0 = __builtin_amdgcn_mfma_f32_16x16x32_bf16(a[0], bb[0], d0, 0, 0, 0); d1 = __builtin_amdgcn_mfma_f32_16x16x32_bf16(a[1], bb[1], d1, 0, 0, 0);
      d0 = __builtin_amdgcn_mfma_f32_16x16x32_bf16(a[2], bb[2], d0, 0, 0, 0); d1 = __builtin_amdgcn_mfma_f32_16x16x32_bf16(a[3], bb[3], d1, 0, 0, 0);
    }
    aa += 256; ba += 4096;
  }
  return d0 + d1;
}

__global__ void __launch_bounds__(NTHR, 3)
fused_decode(const unsigned short* __restrict__ x, const unsigned short* __restrict__ kc, const unsigned short* __restrict__ vc,
             const unsigned short* __restrict__ wq, const unsigned short* __restrict__ wo, const unsigned short* __restrict__ wg,
             const unsigned short* __restrict__ wu, const unsigned short* __restrict__ wd, unsigned short* __restrict__ out, unsigned char* ws) {
  extern __shared__ __attribute__((aligned(16))) unsigned char lds[];
  LAS unsigned char* L = (LAS unsigned char*)lds;
  const int w = blockIdx.x;
  const int tid = threadIdx.x, lane = tid & 63, wave = __builtin_amdgcn_readfirstlane(tid >> 6);
  const int g = lane >> 4, i = lane & 15;
  const int xc = w & 7, r = w >> 3;
  const int b = r & 7, sp = r >> 3, kvh = (xc + 2 * sp) & 7;
  const __amdgpu_buffer_rsrc_t rsW = __builtin_amdgcn_make_buffer_rsrc(ws, 0, WS_END, 0x00020000);
  const unsigned kv_off = (unsigned)(((b * HKV + kvh) * SK + sp * 2048) * 256);

  if (wave < 4) {
    const int aw = wave;
    const u64 ka = (u64)kc, va = (u64)vc;
    AttnConst c;
    c.rsK = u32x4{(unsigned)ka, (unsigned)(ka >> 32), 128u << 20, 0x00020000u};
    c.rsV = u32x4{(unsigned)va, (unsigned)(va >> 32), 128u << 20, 0x00020000u};
    c.gbase = kv_off + aw * 4096;
    c.ring = L_RING + aw * 32768;
    c.vk0 = g * 256 + ((i ^ g) << 4);
    c.vv0 = g * 256 + ((i ^ (2 * g)) << 4);
    c.kaddr = c.ring + i * 256 + ((i ^ g) << 4);
    const int q4 = i >> 2, pp = lane & 3;
    c.vaddr = c.ring + 8192 + (4 * g + q4) * 256 + ((8 * (g & 1) + 2 * q4) << 4) + 8 * pp;
    if (EARLYK) DMA_K(c, c.gbase, c.ring);
    __builtin_amdgcn_s_barrier();
    if (aw == 0) STAMP(0);
#define PCAPW() do { if (lds_ldu(L, C_QREADY)) WAIT_VMI(CAPG); else WAIT_VMI(PCAP); } while (0)
#pragma unroll 1
    for (int u = 0; u < 2; ++u) {
      const unsigned so = c.gbase + u * 32768u, ld = c.ring + u * 16384;
      if (!EARLYK || u) { PCAPW(); DMA_K(c, so, ld); }
      PCAPW(); DMA_K(c, so + 16384u, ld + 4096);
      PCAPW(); DMA_V(c, so, ld + 8192);
      if (u == 1 && lane == 0) lds_add(L, C_RING);
      PCAPW(); DMA_V(c, so + 16384u, ld + 8192 + 4096);
    }
    if (aw == 0) STAMP(1);
    lds_wait_ge(L, C_QREADY, 1);
    if (aw == 0) STAMP(2);
#pragma unroll
    for (int s = 0; s < 4; ++s) { u32x4 v = *(LAS u32x4*)(L + L_Q + (i & 3) * 256 + 64 * s + 16 * g); if (i >= 4) v = u32x4{0, 0, 0, 0}; c.qf[s] = __builtin_bit_cast(bf16x8, v); }
    AttnState st;
#pragma unroll
    for (int d = 0; d < 8; ++d) st.acc[d] = f32x4{0.f, 0.f, 0.f, 0.f};
    st.m = -INFINITY; st.l = 0.f;
    const unsigned T32 = lds_ldu(L, C_TLO);
    WAIT_VM(0);
#pragma unroll 1
    for (int t = 0; t < NT_TILES; t += 2) {
      if (aw == 0 && lane == 0) lds_st(L, C_PROG, t);
      if (t == NT_TILES - 2) { if (lane == 0) lds_st(L, C_ATTEND, 1); lds_wait_ge(L, C_WOREAD, 4); }
      attn_tile<0>(L, c, st, t, NT_TILES - 1 - t, lane, aw);
      attn_tile<1>(L, c, st, t + 1, NT_TILES - 2 - t, lane, aw);
    }
    if (aw == 0) STAMP(3);
    STAMP(18 + aw);
    const float lsum = xrow16_sum(st.l);
    LAS float* mr = (LAS float*)(L + L_STG + aw * 2048);
    LAS float* mlr = (LAS float*)(L + L_STG + 8192);
    if (i < 4) {
#pragma unroll
      for (int d = 0; d < 8; ++d) *(LAS f32x4*)(mr + i * 128 + 16 * d + 4 * g) = st.acc[d];
      if (g == 0) { mlr[i * 8 + aw] = st.m; mlr[i * 8 + 4 + aw] = lsum; }
    }
    WAIT_LGKM0();
    unsigned old = 0;
    if (lane == 0) old = lds_add(L, C_MRG);
    old = __builtin_amdgcn_readfirstlane(old);
    asm volatile("" ::: "memory");
    if (old == 3u) {
      const f32x4 mw = *(LAS f32x4*)(mlr + g * 8), lw = *(LAS f32x4*)(mlr + g * 8 + 4);
      f32x4 a0[4], a1[4];
#pragma unroll
      for (int ww = 0; ww < 4; ++ww) { LAS float* rec = (LAS float*)(L + L_STG + ww * 2048) + g * 128 + 4 * i; a0[ww] = *(LAS f32x4*)rec; a1[ww] = *(LAS f32x4*)(rec + 64); }
      const float M = fmaxf(fmaxf(mw[0], mw[1]), fmaxf(mw[2], mw[3]));
      float Ls = 0.f; f32x4 o0 = {0.f, 0.f, 0.f, 0.f}, o1 = {0.f, 0.f, 0.f, 0.f};
#pragma unroll
      for (int ww = 0; ww < 4; ++ww) {
        const float wt = __builtin_amdgcn_exp2f((mw[ww] - M) * 1.4426950408889634f);
        Ls = fmaf(wt, lw[ww], Ls); o0 += wt * a0[ww]; o1 += wt * a1[ww]; }
      const unsigned po = WS_PART + ((b * 32 + kvh * 4 + g) * 4 + sp) * 512 + i * 16;
      __builtin_amdgcn_raw_buffer_store_b128(u32x4{cvtpk(o0[0], o0[1]), T32, cvtpk(o0[2], o0[3]), T32}, rsW, po, 0, 16);
      __builtin_amdgcn_raw_buffer_store_b128(u32x4{cvtpk(o1[0], o1[1]), T32, cvtpk(o1[2], o1[3]), T32}, rsW, po + 256, 0, 16);
      if (i == 0) __builtin_amdgcn_raw_buffer_store_b128(u32x4{__float_as_uint(M), T32, __float_as_uint(Ls), T32}, rsW, WS_ML + (((kvh * 4 + g) * 8 + b) * 4 + sp) * 16, 0, 16);
      STAMP(4);
    }
    return;
  }

  const int hw = wave - 4;
  if (hw == 0) STAMP(8);
  u32x4 xv = *(const GAS u32x4*)(x + hw * DM + lane * 8);
  u64 E = 0;
  if (hw == 0) asm volatile("global_load_dwordx2 %0, %1, %2 sc1" : "=v"(E) : "v"(0u), "s"(ws) : "memory");
  {
    const u64 qa = (u64)wq;
    const u32x4 rsQ = {(unsigned)qa, (unsigned)(qa >> 32), 4u << 20, 0x00020000u};
    const unsigned n0 = xc * 512 + r * 16;
    const unsigned vo = (lane >> 1) * 8192 + (lane & 1) * 16;
    dma1(vo, rsQ, (2 * hw) * 262144u + n0 * 2, L_STG + (2 * hw) * 1024);
    dma1(vo, rsQ, (2 * hw + 1) * 262144u + n0 * 2, L_STG + (2 * hw + 1) * 1024);
  }
  if (hw == 3) { if (lane < 32) lds_st(L, lane, 0u); WAIT_LGKM0(); }
  __builtin_amdgcn_s_barrier();
  asm volatile("" : "+v"(xv) :: "memory");
  {
    float f[8];
#pragma unroll
    for (int k = 0; k < 4; ++k) { f[2 * k] = lo16(xv[k]); f[2 * k + 1] = hi16(xv[k]); }
    float s = ((f[0] + f[1]) + (f[2] + f[3])) + ((f[4] + f[5]) + (f[6] + f[7]));
    s = wave_sum(s);
    const float mean = s * (1.f / DM);
    float q = 0.f;
#pragma unroll
    for (int k = 0; k < 8; ++k) { const float d = f[k] - mean; q = fmaf(d, d, q); }
    q = wave_sum(q);
    const float var = rbf(q * (1.f / DM));
    const float mb = rbf(mean);
    const float sd = rbf(sqrtf(rbf(var + 1e-5f)));
    u32x4 o;
#pragma unroll
    for (int k = 0; k < 4; ++k) o[k] = cvtpk(rbf(f[2 * k] - mb) / sd, rbf(f[2 * k + 1] - mb) / sd);
    *(LAS u32x4*)(L + L_XN + hw * XN_STRIDE + lane * 16) = o;
  }
  asm volatile("s_waitcnt vmcnt(0)" : "+v"(E) :: "memory");
  if (hw == 0 && lane == 0) { const u64 T = E + 1; lds_st(L, C_TLO, (unsigned)T); lds_st(L, C_THI, (unsigned)(T >> 32)); }
  WAIT_LGKM0();
  if (lane == 0) lds_add(L, C_LN);
  if (hw == 0) lds_wait_ge<1>(L, C_LN, 8); else lds_wait_ge<6>(L, C_LN, 8);
  const unsigned Tlo = lds_ldu(L, C_TLO), Thi = lds_ldu(L, C_THI);
  const unsigned tg_lo = Tlo << 16, tg_hi = (Thi << 16) | (Tlo >> 16);
#define TAG_OK(lo_, hi_) ((((lo_) ^ tg_lo) >> 16) == 0 && (hi_) == tg_hi)
  const int gam = xc + 8 * sp, f0 = gam * 64 + (r & 7) * 8;
  const int hd = 4 * xc + sp, cbk = r & 7;

  if (hw == 0) {
    STAMP(9);
    const f32x4 d = proj16<1>(L, lane);
    { const u64 t_ = __builtin_amdgcn_s_memrealtime() + DLYQ; while (__builtin_amdgcn_s_memrealtime() < t_) __builtin_amdgcn_s_sleep(2); }
    const unsigned pk = i < 8 ? cvtpk(d[0], d[1]) : cvtpk(d[2], d[3]);
    __builtin_amdgcn_raw_buffer_store_b128(u32x4{tg_lo | (pk & 0xffffu), tg_hi, tg_lo | (pk >> 16), tg_hi}, rsW,
                                           WS_QG + ((i & 7) * 4096 + xc * 512 + r * 16 + 4 * g + 2 * (i >> 3)) * 8, 0, 16);
    if (lane == 0) lds_st(L, C_STGFREE, 1);
    STAMP(10);
  } else if (hw == 1) {
    lds_wait_ge<8>(L, C_GUST, 4);
    if (gam < 28) {
      const f32x4 d = proj16<0>(L, lane);
      float hv[2];
#pragma unroll
      for (int k = 0; k < 2; ++k) {
        const float own = rbf(g < 2 ? d[k] : d[2 + k]);
        const float oth = dpp<XOR8>(own);
        hv[k] = own > 0.f ? own * oth : 0.f;
      }
      const unsigned pk = cvtpk(hv[0], hv[1]);
      const int b0 = 4 * (g & 1) + (g < 2 ? 0 : 2);
      if (i < 8) __builtin_amdgcn_raw_buffer_store_b128(u32x4{tg_lo | (pk & 0xffffu), tg_hi, tg_lo | (pk >> 16), tg_hi}, rsW, WS_HG + ((f0 + i) * 8 + b0) * 8, 0, 16);
    }
    if (lane == 0) lds_st(L, C_HDONE, 1);
    STAMP(17);
  } else if (hw == 2) {
    const unsigned qo = WS_QG + (b * 4096 + kvh * 512) * 8 + lane * 16;
    bool okk[4] = {false, false, false, false};
    lds_wait_ge<4>(L, C_STGFREE, 1);
#pragma unroll 1
    for (unsigned spins = 0; spins < SPIN_CAP; ++spins) {
      asm volatile("" ::: "memory");
      u32x4 v[4];
#pragma unroll
      for (int k = 0; k < 4; ++k) if (!okk[k]) v[k] = __builtin_amdgcn_raw_buffer_load_b128(rsW, qo + k * 1024, 0, 16);
#pragma unroll
      for (int k = 0; k < 4; ++k) if (!okk[k]) {
        okk[k] = TAG_OK(v[k][0], v[k][1]) && TAG_OK(v[k][2], v[k][3]);
        *(LAS unsigned*)(L + L_Q + (lane + 64 * k) * 4) = (v[k][0] & 0xffffu) | (v[k][2] << 16);
      }
      if (__all(okk[0] && okk[1] && okk[2] && okk[3])) break;
      __builtin_amdgcn_s_sleep(1);
    }
    WAIT_LGKM0();
    if (lane == 0) lds_st(L, C_QREADY, 1);
    STAMP(11);
  } else if (hw >= 4) {
    const int h4 = hw - 4;
    const __amdgpu_buffer_rsrc_t rsD = __builtin_amdgcn_make_buffer_rsrc((void*)wd, 0, FF * DM * 2, 0x00020000);
    const GAS unsigned short* sgu = (const GAS unsigned short*)((lane & 1) ? wu : wg) + (size_t)(128 * h4 + (lane >> 1)) * FF + (gam < 28 ? f0 : 0);
    const GAS unsigned short* swo = (const GAS unsigned short*)wo + (size_t)(128 * hd + 32 * h4 + (lane >> 3)) * DM + cbk * 64 + (lane & 7) * 8;
    u32x4 rg[4], ro[4], rd_[2], xk[8], xvv[8];
    {
      const __amdgpu_buffer_rsrc_t rK = __builtin_amdgcn_make_buffer_rsrc((void*)kc, 0, 128u << 20, 0x00020000), rV = __builtin_amdgcn_make_buffer_rsrc((void*)vc, 0, 128u << 20, 0x00020000);
      const unsigned so = kv_off + 2u * 32768u + h4 * 4096;
      const unsigned vk0 = g * 256 + ((i ^ g) << 4), vv0 = g * 256 + ((i ^ (2 * g)) << 4);
      lds_wait_ge<4>(L, C_RING, 4);
#pragma unroll
      for (int u = 0; u < 8; ++u) { if ((u & 3) == 0) WAIT_VMI(HCAP); xk[u] = __builtin_amdgcn_raw_buffer_load_b128(rK, (vk0 ^ ((u & 3) << 6)) + (u & 3) * 1024 + (u >> 2) * 16384, so, 2); }
#pragma unroll
      for (int u = 0; u < 8; ++u) { if ((u & 3) == 0) WAIT_VMI(HCAP); xvv[u] = __builtin_amdgcn_raw_buffer_load_b128(rV, (vv0 ^ ((u & 1) << 7)) + (u & 3) * 1024 + (u >> 2) * 16384, so, 2); }
      WAIT_VMI(HCAP);
    }
#pragma unroll
    for (int u = 0; u < 4; ++u) ro[u] = __builtin_nontemporal_load((const GAS u32x4*)(swo + (size_t)u * 8 * DM));
#pragma unroll
    for (int u = 0; u < 2; ++u) rd_[u] = __builtin_amdgcn_raw_buffer_load_b128(rsD, (unsigned)((56 * hd + 16 * h4 + 8 * u + (lane >> 3)) * 1024 + cbk * 128 + (lane & 7) * 16), 0, 2);
    lds_wait_ge<2>(L, C_XF + h4, 1);
#pragma unroll
    for (int u = 0; u < 8; ++u) *(LAS u32x4*)(L + L_RING + h4 * 32768 + u * 1024 + lane * 16) = xk[u];
    WAIT_LGKM0();
    if (lane == 0) lds_st(L, C_XR + h4, 1);
    lds_wait_ge<1>(L, C_XF + h4, 2);
#pragma unroll
    for (int u = 0; u < 8; ++u) *(LAS u32x4*)(L + L_RING + h4 * 32768 + 8192 + u * 1024 + lane * 16) = xvv[u];
    WAIT_LGKM0();
    if (lane == 0) lds_st(L, C_XR + h4, 2);
#pragma unroll
    for (int u = 0; u < 4; ++u) rg[u] = *(const GAS u32x4*)(sgu + (size_t)u * 32 * FF);
    lds_wait_ge(L, C_STGFREE, 1);
#pragma unroll
    for (int u = 0; u < 4; ++u) *(LAS u32x4*)(L + L_STG + (4 * h4 + u) * 1024 + lane * 16) = rg[u];
    WAIT_LGKM0();
    if (lane == 0) lds_add(L, C_GUST);
    lds_wait_ge<4>(L, C_HDONE, 1);
#pragma unroll
    for (int u = 0; u < 4; ++u) *(LAS u32x4*)(L + L_STG + (4 * h4 + u) * 1024 + lane * 16) = ro[u];
#pragma unroll
    for (int u = 0; u < 2; ++u) *(LAS u32x4*)(L + L_XN + (2 * h4 + u) * 1024 + lane * 16) = rd_[u];
    WAIT_LGKM0();
    if (lane == 0) lds_add(L, C_WOLD);
  }
  {
    const int fl = 7 * hw + (lane >> 3), bb = lane & 7;
    const unsigned ho = WS_HG + ((56 * hd + 7 * hw) * 8 + (lane < 56 ? lane : 0)) * 8;
    u32x2 hv = {0u, 0u};
    bool ok = false;
    lds_wait_ge<32>(L, C_PROG, GATE_H);
#pragma unroll 1
    for (unsigned spins = 0; spins < SPIN_CAP; ++spins) {
      asm volatile("" ::: "memory");
      if (!ok) { hv = __builtin_amdgcn_raw_buffer_load_b64(rsW, ho, 0, 16); ok = TAG_OK(hv[0], hv[1]); }
      if (__all(ok)) break;
      __builtin_amdgcn_s_sleep(SLP_MID);
    }
    if (lane < 56) *(LAS unsigned short*)(L + L_OA + bb * OA_STRIDE + fl * 2) = (unsigned short)(hv[0] & 0xffffu);
    if (lane < 4) *(LAS unsigned*)(L + L_OA + hw * OA_STRIDE + 112 + lane * 4) = 0u;
    WAIT_LGKM0();
    if (lane == 0) lds_add(L, C_OG);
  }
  const int bf = r >> 2, cb = 4 * xc + (r & 3), n1 = cb * 16;
  const unsigned ba = (g * 8 + (i >> 2)) * 128 + (lane & 3) * 8 + (hw & 3) * 32;
  const unsigned oa = L_OA + (i & 7) * OA_STRIDE + g * 16;
  bf16x8 wf[4];
  unsigned y2p = 0;
  float xr[2] = {0.f, 0.f};
  if (hw < 4) {
    lds_wait_ge<8>(L, C_WOLD, 4);
    lds_wait_ge<4>(L, C_OG, 8);
    f32x4 d = {0.f, 0.f, 0.f, 0.f};
#pragma unroll
    for (int s = 0; s < 2; ++s) {
      const bf16x4 a0 = __builtin_amdgcn_ds_read_tr16_b64_v4bf16((LAS bf16x4*)(L + L_XN + ba + s * 4096));
      const bf16x4 a1 = __builtin_amdgcn_ds_read_tr16_b64_v4bf16((LAS bf16x4*)(L + L_XN + ba + s * 4096 + 512));
      d = __builtin_amdgcn_mfma_f32_16x16x32_bf16(__builtin_shufflevector(a0, a1, 0, 1, 2, 3, 4, 5, 6, 7), *(LAS bf16x8*)(L + oa + s * 64), d, 0, 0, 0);
    }
    y2p = i < 8 ? cvtpk(d[0], d[1]) : cvtpk(d[2], d[3]);
#pragma unroll
    for (int s = 0; s < 4; ++s) {
      const bf16x4 a0 = __builtin_amdgcn_ds_read_tr16_b64_v4bf16((LAS bf16x4*)(L + L_STG + ba + s * 4096));
      const bf16x4 a1 = __builtin_amdgcn_ds_read_tr16_b64_v4bf16((LAS bf16x4*)(L + L_STG + ba + s * 4096 + 512));
      wf[s] = __builtin_shufflevector(a0, a1, 0, 1, 2, 3, 4, 5, 6, 7);
    }
    asm volatile("s_waitcnt lgkmcnt(0)" : "+v"(wf[0]), "+v"(wf[1]), "+v"(wf[2]), "+v"(wf[3]), "+v"(y2p) :: "memory");
    if (lane == 0) lds_add(L, C_WOREAD);
    if (hw == 0) STAMP(12);
  } else if (hw == 6) {
    const unsigned xq = *(const GAS unsigned*)(x + bf * DM + n1 + (lane & 7) * 2);
    xr[0] = lo16(xq); xr[1] = hi16(xq);
  } else if (hw == 5 && w == 0) {
#pragma unroll 1
    for (unsigned spins = 0; spins < SPIN_CAP; ++spins) {
      bool ok = true;
#pragma unroll
      for (int k = 0; k < 4; ++k) { const u32x2 v = __builtin_amdgcn_raw_buffer_load_b64(rsW, WS_QG + (lane + 64 * k) * 128, 0, 16); ok &= TAG_OK(v[0], v[1]); }
      if (__all(ok)) break;
      __builtin_amdgcn_s_sleep(SLP_MID);
      asm volatile("" ::: "memory");
    }
    if (lane == 0) __hip_atomic_store((GAS u64*)(ws + WS_EPOCH), ((u64)Thi << 32) | Tlo, RLX_AGENT);
  }
  {
    lds_wait_ge<4>(L, C_WOREAD, 4);
    lds_wait_ge<16>(L, C_ATTEND, 1);
    if (hw == 0) STAMP(13);
    const unsigned po = WS_PART + ((hw * 32 + hd) * 4 + g) * 512 + i * 16;
    const unsigned mo = WS_ML + ((hd * 8 + hw) * 4 + g) * 16;
    u32x4 pv0 = {0u, 0u, 0u, 0u}, pv1 = {0u, 0u, 0u, 0u}, ml = {0u, 0u, 0u, 0u};
    if (hw == 7) {
      bool ok = lane >= 32;
#pragma unroll 1
      for (unsigned spins = 0; spins < SPIN_CAP; ++spins) {
        asm volatile("" ::: "memory");
        if (!ok) {
          ml = __builtin_amdgcn_raw_buffer_load_b128(rsW, WS_ML + hd * 512 + lane * 16, 0, 16);
          ok = ml[1] == Tlo && ml[3] == Tlo;
        }
        const unsigned miss = ~(unsigned)__ballot(ok);
        if (lane == 0) lds_st(L, C_MLMASK, ~miss);
        const unsigned two = (miss & (miss >> 1) & 0x77777777u) | (miss & (miss >> 2) & 0x33333333u) | (miss & (miss >> 3) & 0x11111111u);
        if (two == 0u) break;
        __builtin_amdgcn_s_sleep(SLP_PART);
      }
      if (lane == 0) lds_st(L, C_MLMASK, 0xffffffffu);
    } else {
      while (__builtin_popcount((lds_ldu(L, C_MLMASK) >> (4 * hw)) & 15u) < 3) __builtin_amdgcn_s_sleep(1);
      asm volatile("" ::: "memory");
    }
    bool ok = false;
#pragma unroll 1
    for (unsigned spins = 0; spins < SPIN_CAP; ++spins) {
      asm volatile("" ::: "memory");
      if (!ok) {
        pv0 = __builtin_amdgcn_raw_buffer_load_b128(rsW, po, 0, 16);
        pv1 = __builtin_amdgcn_raw_buffer_load_b128(rsW, po + 256, 0, 16);
        ml = __builtin_amdgcn_raw_buffer_load_b128(rsW, mo, 0, 16);
        ok = pv0[1] == Tlo && pv0[3] == Tlo && pv1[1] == Tlo && pv1[3] == Tlo && ml[1] == Tlo && ml[3] == Tlo;
      }
      if (__all(ok)) break;
      __builtin_amdgcn_s_sleep(SLP_DIR);
    }
    if (hw == 0) STAMP(14);
    const float mj = __uint_as_float(ml[0]);
    const float M = xrow16_max(mj);
    const float wt = __builtin_amdgcn_exp2f((mj - M) * 1.4426950408889634f);
    const float inv = 1.f / xrow16_sum(wt * __uint_as_float(ml[2]));
    const u32x2 oa0 = {cvtpk(xrow16_sum(wt * lo16(pv0[0])) * inv, xrow16_sum(wt * hi16(pv0[0])) * inv), cvtpk(xrow16_sum(wt * lo16(pv0[2])) * inv, xrow16_sum(wt * hi16(pv0[2])) * inv)};
    const u32x2 oa1 = {cvtpk(xrow16_sum(wt * lo16(pv1[0])) * inv, xrow16_sum(wt * hi16(pv1[0])) * inv), cvtpk(xrow16_sum(wt * lo16(pv1[2])) * inv, xrow16_sum(wt * hi16(pv1[2])) * inv)};
    if (g == 0) { *(LAS u32x2*)(L + L_OA + hw * OA_STRIDE + i * 8) = oa0; *(LAS u32x2*)(L + L_OA + hw * OA_STRIDE + 128 + i * 8) = oa1; }
    WAIT_LGKM0();
    if (lane == 0) lds_add(L, C_OG);
  }
  if (hw < 4) {
    lds_wait_ge(L, C_OG, 16);
    if (hw == 0) STAMP(22);
    f32x4 d = {0.f, 0.f, 0.f, 0.f};
#pragma unroll
    for (int s = 0; s < 4; ++s) d = __builtin_amdgcn_mfma_f32_16x16x32_bf16(wf[s], *(LAS bf16x8*)(L + oa + s * 64), d, 0, 0, 0);
    const unsigned yp = i < 8 ? cvtpk(d[0], d[1]) : cvtpk(d[2], d[3]);
    const unsigned yo = ((((i & 7) * 32 + 4 * cbk + hw) * 32 + hd) * 16 + 4 * g + 2 * (i >> 3)) * 8;
    __builtin_amdgcn_raw_buffer_store_b128(u32x4{(yp & 0xffffu) | (y2p << 16), Tlo, (yp >> 16) | (y2p & 0xffff0000u), Tlo}, rsW, WS_Y + yo, 0, 16);
    if (hw == 0) STAMP(16);
  } else if (hw == 6) {
    const unsigned io = WS_Y + (bf * 32 + cb) * 4096 + lane * 16;
    u32x4 y[4];
    bool okk[4] = {false, false, false, false};
#pragma unroll 1
    for (unsigned spins = 0; spins < SPIN_CAP; ++spins) {
      asm volatile("" ::: "memory");
#pragma unroll
      for (int k = 0; k < 4; ++k) if (!okk[k]) y[k] = __builtin_amdgcn_raw_buffer_load_b128(rsW, io + k * 1024, 0, 16);
#pragma unroll
      for (int k = 0; k < 4; ++k) okk[k] = y[k][1] == Tlo && y[k][3] == Tlo;
      if (__all(okk[0] && okk[1] && okk[2] && okk[3])) break;
      __builtin_amdgcn_s_sleep(SLP_Y);
    }
    STAMP(23);
    float o[2];
#pragma unroll
    for (int k = 0; k < 2; ++k) {
      float t = (lo16(y[0][2 * k]) + lo16(y[1][2 * k])) + (lo16(y[2][2 * k]) + lo16(y[3][2 * k]));
      float u = (hi16(y[0][2 * k]) + hi16(y[1][2 * k])) + (hi16(y[2][2 * k]) + hi16(y[3][2 * k]));
      t += dpp<XOR8>(t); t = rbf(xrow16_sum(t));
      u += dpp<XOR8>(u); u = rbf(xrow16_sum(u));
      const float gate = rbf(u / (1.f + __expf(-u)));
      o[k] = xr[k] + rbf(t * gate);
    }
    if (lane < 8) *(GAS unsigned*)(out + bf * DM + n1 + lane * 2) = cvtpk(o[0], o[1]);
    STAMP(15);
    STAMP_FLUSH();
  }
}

extern "C" void kernel_launch(void* const* d_in, const int* in_sizes, int n_in, void* d_out, int out_size, void* d_ws, size_t ws_size, hipStream_t stream) {
  const unsigned short* x = (const unsigned short*)d_in[0];
  const unsigned short* kc = (const unsigned short*)d_in[1];
  const unsigned short* vc = (const unsigned short*)d_in[2];
  const unsigned short* wq = (const unsigned short*)d_in[3];
  const unsigned short* wo = (const unsigned short*)d_in[4];
  const unsigned short* wg = (const unsigned short*)d_in[6];
  const unsigned short* wu = (const unsigned short*)d_in[7];
  const unsigned short* wd = (const unsigned short*)d_in[8];
  (void)hipFuncSetAttribute((const void*)fused_decode, hipFuncAttributeMaxDynamicSharedMemorySize, L_END);
  fused_decode<<<dim3(NWG), dim3(NTHR), L_END, stream>>>(x, kc, vc, wq, wo, wg, wu, wd, (unsigned short*)d_out, (unsigned char*)d_ws);
}
```

```cpp
#include <hip/hip_runtime.h>
#include <hip/hip_bf16.h>
#include <cstdint>

typedef __bf16 bf16x2 __attribute__((ext_vector_type(2)));
typedef __bf16 bf16x4 __attribute__((ext_vector_type(4)));
typedef __bf16 bf16x8 __attribute__((ext_vector_type(8)));
typedef unsigned u32x2 __attribute__((ext_vector_type(2)));
typedef unsigned u32x4 __attribute__((ext_vector_type(4)));
typedef float f32x2 __attribute__((ext_vector_type(2)));
typedef float f32x4 __attribute__((ext_vector_type(4)));
typedef unsigned long long u64;
#define LAS __attribute__((address_space(3)))
#define GAS __attribute__((address_space(1)))
#define RLX_AGENT __ATOMIC_RELAXED, __HIP_MEMORY_SCOPE_AGENT
#define RLX_WG __ATOMIC_RELAXED, __HIP_MEMORY_SCOPE_WORKGROUP

constexpr int NB = 8, DM = 512, HKV = 8, SK = 8192, FF = 1792;
constexpr int NWG = 256, NTHR = 768;
constexpr int NT_TILES = 16;

#define CAPG 6
#define PCAP 2
#define HCAP 2
#define GATE_H 4
#define SLP_PART 4
#define SLP_DIR 2
#define SLP_Y 2
#define SLP_MID 64

constexpr unsigned WS_EPOCH = 0;
constexpr unsigned WS_ML = 8192;
constexpr unsigned WS_QG = 32768;
constexpr unsigned WS_HG = WS_QG + NB * 4096 * 8;
constexpr unsigned WS_PART = WS_HG + NB * FF * 8;
constexpr unsigned WS_Y = WS_PART + NWG * 2048;
constexpr unsigned WS_END = WS_Y + (1u << 20);

constexpr unsigned L_RING = 0;
constexpr unsigned L_STG = 131072;
constexpr unsigned L_XN = L_STG + 16384;
constexpr unsigned XN_STRIDE = 1040;
constexpr unsigned L_Q = L_XN + 8 * XN_STRIDE;
constexpr unsigned L_OA = L_Q + 1024;
constexpr unsigned OA_STRIDE = 272;
constexpr unsigned L_CTL = L_OA + 8 * OA_STRIDE;
constexpr unsigned L_END = L_CTL + 128;
static_assert(L_END <= 163840, "LDS");
enum { C_LN = 0, C_STGFREE, C_QREADY, C_HDONE, C_MRG, C_ATTEND, C_TLO, C_THI, C_WOLD, C_WOREAD, C_OG, C_PROG, C_MLMASK, C_RING, C_GUST,
       C_XF = 20  , C_XR = 24   };

__device__ __forceinline__ unsigned cvtpk(float a, float b) {
  bf16x2 t = __builtin_convertvector(f32x2{a, b}, bf16x2);
  return __builtin_bit_cast(unsigned, t);
}
__device__ __forceinline__ float lo16(unsigned u) { return __builtin_bit_cast(float, u << 16); }
__device__ __forceinline__ float hi16(unsigned u) { return __builtin_bit_cast(float, u & 0xffff0000u); }
__device__ __forceinline__ float rbf(float f) { return lo16(cvtpk(f, f)); }
template <int CTRL> __device__ __forceinline__ float dpp(float x) {
  return __builtin_bit_cast(float, __builtin_amdgcn_mov_dpp(__builtin_bit_cast(int, x), CTRL, 0xf, 0xf, true));
}
constexpr int XOR1 = 0xB1, XOR2 = 0x4E, XOR7 = 0x141, XOR8 = 0x128;
__device__ __forceinline__ float xrow16_max(float x) {
  auto s = __builtin_amdgcn_permlane16_swap(__float_as_uint(x), __float_as_uint(x), false, false);
  x = fmaxf(__uint_as_float(s[0]), __uint_as_float(s[1]));
  auto t = __builtin_amdgcn_permlane32_swap(__float_as_uint(x), __float_as_uint(x), false, false);
  return fmaxf(__uint_as_float(t[0]), __uint_as_float(t[1]));
}
__device__ __forceinline__ float xrow16_sum(float x) {
  auto s = __builtin_amdgcn_permlane16_swap(__float_as_uint(x), __float_as_uint(x), false, false);
  x = __uint_as_float(s[0]) + __uint_as_float(s[1]);
  auto t = __builtin_amdgcn_permlane32_swap(__float_as_uint(x), __float_as_uint(x), false, false);
  return __uint_as_float(t[0]) + __uint_as_float(t[1]);
}
__device__ __forceinline__ float wave_sum(float x) {
  x += dpp<XOR1>(x); x += dpp<XOR2>(x); x += dpp<XOR7>(x); x += dpp<XOR8>(x);
  return xrow16_sum(x);
}

__device__ __forceinline__ void dma4(unsigned v0, unsigned v1, unsigned v2, unsigned v3, u32x4 rs, unsigned so, unsigned ldsaddr) {
  unsigned keep;
  asm volatile(
    "s_mov_b32 %0, m0\n\t"
    "s_mov_b32 m0, %7\n\t"
    "s_nop 0\n\t"
    "buffer_load_dwordx4 %1, %5, %6 offen nt lds\n\t"
    "buffer_load_dwordx4 %2, %5, %6 offen offset:1024 nt lds\n\t"
    "buffer_load_dwordx4 %3, %5, %6 offen offset:2048 nt lds\n\t"
    "buffer_load_dwordx4 %4, %5, %6 offen offset:3072 nt lds\n\t"
    "s_mov_b32 m0, %0"
    : "=&s"(keep) : "v"(v0), "v"(v1), "v"(v2), "v"(v3), "s"(rs), "s"(so), "s"(ldsaddr) : "memory");
}
__device__ __forceinline__ void dma1(unsigned v0, u32x4 rs, unsigned so, unsigned ldsaddr) {
  unsigned keep;
  asm volatile(
    "s_mov_b32 %0, m0\n\t"
    "s_mov_b32 m0, %4\n\t"
    "s_nop 0\n\t"
    "buffer_load_dwordx4 %1, %2, %3 offen lds\n\t"
    "s_mov_b32 m0, %0"
    : "=&s"(keep) : "v"(v0), "s"(rs), "s"(so), "s"(ldsaddr) : "memory");
}
#define WAIT_VM(n) asm volatile("s_waitcnt vmcnt(" #n ")" ::: "memory")
#define WAIT_VMI(n) asm volatile("s_waitcnt vmcnt(%0)" :: "i"(n) : "memory")
#define WAIT_LGKM0() asm volatile("s_waitcnt lgkmcnt(0)" ::: "memory")

__device__ __forceinline__ unsigned lds_ld(LAS unsigned char* L, int idx) { return __hip_atomic_load((LAS unsigned*)(L + L_CTL) + idx, RLX_WG); }
__device__ __forceinline__ unsigned lds_ldu(LAS unsigned char* L, int idx) { return (unsigned)__builtin_amdgcn_readfirstlane(lds_ld(L, idx)); }
__device__ __forceinline__ void lds_st(LAS unsigned char* L, int idx, unsigned v) { __hip_atomic_store((LAS unsigned*)(L + L_CTL) + idx, v, RLX_WG); }
__device__ __forceinline__ unsigned lds_add(LAS unsigned char* L, int idx) { return __hip_atomic_fetch_add((LAS unsigned*)(L + L_CTL) + idx, 1u, RLX_WG); }
template <int SLP = 1>
__device__ __forceinline__ void lds_wait_ge(LAS unsigned char* L, int idx, unsigned want) {
  while (lds_ldu(L, idx) < want) __builtin_amdgcn_s_sleep(SLP);
  asm volatile("" ::: "memory");
}
constexpr unsigned SPIN_CAP = 1u << 15;

struct AttnState { f32x4 acc[8]; float m, l; };
struct AttnConst { u32x4 rsK, rsV; unsigned gbase, ring, vk0, vv0, kaddr, vaddr; bf16x8 qf[4]; };

#define DMA_K(c_, so_, ld_) dma4((c_).vk0, (c_).vk0 ^ 64, (c_).vk0 ^ 128, (c_).vk0 ^ 192, (c_).rsK, (so_), (ld_))
#define DMA_V(c_, so_, ld_) dma4((c_).vv0, (c_).vv0 ^ 128, (c_).vv0, (c_).vv0 ^ 128, (c_).rsV, (so_), (ld_))

template <int SLOT>
__device__ __forceinline__ void attn_tile(LAS unsigned char* L, const AttnConst& c, AttnState& st, const int t, const int rem, const int lane, const int aw) {
  constexpr float SCALE = 0.088388347648318440f, LOG2E = 1.4426950408889634f;
  const unsigned so2 = c.gbase + (unsigned)(t + 2) * 32768u, ld2 = c.ring + SLOT * 16384;
  if (rem >= 1) WAIT_VM(24); else WAIT_VM(8);
  if (SLOT == 0 && t == 2) lds_wait_ge(L, C_XR + aw, 1);
  f32x4 s0 = {0.f, 0.f, 0.f, 0.f}, s1 = {0.f, 0.f, 0.f, 0.f};
#pragma unroll
  for (int s = 0; s < 4; ++s) {
    const bf16x8 k0 = *(LAS bf16x8*)(L + (c.kaddr ^ (s << 6)) + SLOT * 16384);
    const bf16x8 k1 = *(LAS bf16x8*)(L + (c.kaddr ^ (s << 6)) + SLOT * 16384 + 4096);
    s0 = __builtin_amdgcn_mfma_f32_16x16x32_bf16(k0, c.qf[s], s0, 0, 0, 0);
    s1 = __builtin_amdgcn_mfma_f32_16x16x32_bf16(k1, c.qf[s], s1, 0, 0, 0);
  }
  WAIT_LGKM0();
  if (SLOT == 0 && t == 0) { if (lane == 0) lds_st(L, C_XF + aw, 1); }
  else if (rem >= 2) { WAIT_VMI(CAPG); DMA_K(c, so2, ld2); WAIT_VMI(CAPG); DMA_K(c, so2 + 16384u, ld2 + 4096); }
  float x[8];
  { unsigned u;
    u = cvtpk(s0[0], s0[1]); u = cvtpk(lo16(u) * SCALE, hi16(u) * SCALE); x[0] = lo16(u); x[1] = hi16(u);
    u = cvtpk(s0[2], s0[3]); u = cvtpk(lo16(u) * SCALE, hi16(u) * SCALE); x[2] = lo16(u); x[3] = hi16(u);
    u = cvtpk(s1[0], s1[1]); u = cvtpk(lo16(u) * SCALE, hi16(u) * SCALE); x[4] = lo16(u); x[5] = hi16(u);
    u = cvtpk(s1[2], s1[3]); u = cvtpk(lo16(u) * SCALE, hi16(u) * SCALE); x[6] = lo16(u); x[7] = hi16(u); }
  float pm = fmaxf(fmaxf(fmaxf(x[0], x[1]), fmaxf(x[2], x[3])), fmaxf(fmaxf(x[4], x[5]), fmaxf(x[6], x[7])));
  pm = xrow16_max(pm);
  if (__any(pm > st.m)) {
    const float mn = fmaxf(st.m, pm);
    const float alpha = __builtin_amdgcn_exp2f((st.m - mn) * LOG2E);
    st.l *= alpha; st.m = mn;
#pragma unroll
    for (int d = 0; d < 8; ++d) st.acc[d] *= alpha;
  }
  const float mb = st.m * LOG2E;
  float p[8];
#pragma unroll
  for (int e = 0; e < 8; ++e) p[e] = __builtin_amdgcn_exp2f(fmaf(x[e], LOG2E, -mb));
  st.l += ((p[0] + p[1]) + (p[2] + p[3])) + ((p[4] + p[5]) + (p[6] + p[7]));
  const u32x4 pw = {cvtpk(p[0], p[1]), cvtpk(p[2], p[3]), cvtpk(p[4], p[5]), cvtpk(p[6], p[7])};
  const bf16x8 pf = __builtin_bit_cast(bf16x8, pw);
  if (rem >= 2) WAIT_VM(24); else if (rem == 1) WAIT_VM(16); else WAIT_VM(0);
  if (SLOT == 0 && t == 2) lds_wait_ge(L, C_XR + aw, 2);
#pragma unroll
  for (int d = 0; d < 8; ++d) {
    const bf16x4 v0 = __builtin_amdgcn_ds_read_tr16_b64_v4bf16((LAS bf16x4*)(L + (c.vaddr ^ (d << 5)) + SLOT * 16384));
    const bf16x4 v1 = __builtin_amdgcn_ds_read_tr16_b64_v4bf16((LAS bf16x4*)(L + (c.vaddr ^ (d << 5)) + SLOT * 16384 + 4096));
    const bf16x8 vf = __builtin_shufflevector(v0, v1, 0, 1, 2, 3, 4, 5, 6, 7);
    st.acc[d] = __builtin_amdgcn_mfma_f32_16x16x32_bf16(vf, pf, st.acc[d], 0, 0, 0);
  }
  WAIT_LGKM0();
  if (SLOT == 0 && t == 0) { if (lane == 0) lds_st(L, C_XF + aw, 2); }
  else if (rem >= 2) { WAIT_VMI(CAPG); DMA_V(c, so2, ld2 + 8192); WAIT_VMI(CAPG); DMA_V(c, so2 + 16384u, ld2 + 8192 + 4096); }
}

template <int TR>
__device__ __forceinline__ f32x4 proj16(LAS unsigned char* L, int lane) {
  const int g = lane >> 4, i = lane & 15;
  unsigned aa = L_XN + (i & 7) * XN_STRIDE + g * 16;
  unsigned ba = L_STG + g * 256 + i * 8;
  f32x4 d0 = {0.f, 0.f, 0.f, 0.f}, d1 = {0.f, 0.f, 0.f, 0.f};
#pragma unroll 2
  for (int s = 0; s < 4; ++s) {
    bf16x8 a[4], bb[4];
#pragma unroll
    for (int k = 0; k < 4; ++k) {
      a[k] = *(LAS bf16x8*)(L + aa + k * 64);
      const bf16x4 b0 = __builtin_amdgcn_ds_read_tr16_b64_v4bf16((LAS bf16x4*)(L + ba + k * 1024));
      const bf16x4 b1 = __builtin_amdgcn_ds_read_tr16_b64_v4bf16((LAS bf16x4*)(L + ba + k * 1024 + 128));
      bb[k] = __builtin_shufflevector(b0, b1, 0, 1, 2, 3, 4, 5, 6, 7);
    }
    if (TR) {
      d0 = __builtin_amdgcn_mfma_f32_16x16x32_bf16(bb[0], a[0], d0, 0, 0, 0); d1 = __builtin_amdgcn_mfma_f32_16x16x32_bf16(bb[1], a[1], d1, 0, 0, 0);
      d0 = __builtin_amdgcn_mfma_f32_16x16x32_bf16(bb[2], a[2], d0, 0, 0, 0); d1 = __builtin_amdgcn_mfma_f32_16x16x32_bf16(bb[3], a[3], d1, 0, 0, 0);
    } else {
      d0 = __builtin_amdgcn_mfma_f32_16x16x32_bf16(a[0], bb[0], d0, 0, 0, 0); d1 = __builtin_amdgcn_mfma_f32_16x16x32_bf16(a[1], bb[1], d1, 0, 0, 0);
      d0 = __builtin_amdgcn_mfma_f32_16x16x32_bf16(a[2], bb[2], d0, 0, 0, 0); d1 = __builtin_amdgcn_mfma_f32_16x16x32_bf16(a[3], bb[3], d1, 0, 0, 0);
    }
    aa += 256; ba += 4096;
  }
  return d0 + d1;
}

__global__ void __launch_bounds__(NTHR, 3)
fused_decode(const unsigned short* __restrict__ x, const unsigned short* __restrict__ kc, const unsigned short* __restrict__ vc,
             const unsigned short* __restrict__ wq, const unsigned short* __restrict__ wo, const unsigned short* __restrict__ wg,
             const unsigned short* __restrict__ wu, const unsigned short* __restrict__ wd, unsigned short* __restrict__ out, unsigned char* ws) {
  extern __shared__ __attribute__((aligned(16))) unsigned char lds[];
  LAS unsigned char* L = (LAS unsigned char*)lds;
  const int w = blockIdx.x;
  const int tid = threadIdx.x, lane = tid & 63, wave = __builtin_amdgcn_readfirstlane(tid >> 6);
  const int g = lane >> 4, i = lane & 15;
  const int xc = w & 7, r = w >> 3;
  const int b = r & 7, sp = r >> 3, kvh = (xc + 2 * sp) & 7;
  const __amdgpu_buffer_rsrc_t rsW = __builtin_amdgcn_make_buffer_rsrc(ws, 0, WS_END, 0x00020000);
  const unsigned kv_off = (unsigned)(((b * HKV + kvh) * SK + sp * 2048) * 256);

  if (wave < 4) {
    const int aw = wave;
    const u64 ka = (u64)kc, va = (u64)vc;
    AttnConst c;
    c.rsK = u32x4{(unsigned)ka, (unsigned)(ka >> 32), 128u << 20, 0x00020000u};
    c.rsV = u32x4{(unsigned)va, (unsigned)(va >> 32), 128u << 20, 0x00020000u};
    c.gbase = kv_off + aw * 4096;
    c.ring = L_RING + aw * 32768;
    c.vk0 = g * 256 + ((i ^ g) << 4);
    c.vv0 = g * 256 + ((i ^ (2 * g)) << 4);
    c.kaddr = c.ring + i * 256 + ((i ^ g) << 4);
    const int q4 = i >> 2, pp = lane & 3;
    c.vaddr = c.ring + 8192 + (4 * g + q4) * 256 + ((8 * (g & 1) + 2 * q4) << 4) + 8 * pp;
    __builtin_amdgcn_s_barrier();
#define PCAPW() do { if (lds_ldu(L, C_QREADY)) WAIT_VMI(CAPG); else WAIT_VMI(PCAP); } while (0)
#pragma unroll 1
    for (int u = 0; u < 2; ++u) {
      const unsigned so = c.gbase + u * 32768u, ld = c.ring + u * 16384;
      PCAPW(); DMA_K(c, so, ld);
      PCAPW(); DMA_K(c, so + 16384u, ld + 4096);
      PCAPW(); DMA_V(c, so, ld + 8192);
      if (u == 1 && lane == 0) lds_add(L, C_RING);
      PCAPW(); DMA_V(c, so + 16384u, ld + 8192 + 4096);
    }
    lds_wait_ge(L, C_QREADY, 1);
#pragma unroll
    for (int s = 0; s < 4; ++s) { u32x4 v = *(LAS u32x4*)(L + L_Q + (i & 3) * 256 + 64 * s + 16 * g); if (i >= 4) v = u32x4{0, 0, 0, 0}; c.qf[s] = __builtin_bit_cast(bf16x8, v); }
    AttnState st;
#pragma unroll
    for (int d = 0; d < 8; ++d) st.acc[d] = f32x4{0.f, 0.f, 0.f, 0.f};
    st.m = -INFINITY; st.l = 0.f;
    const unsigned T32 = lds_ldu(L, C_TLO);
    WAIT_VM(0);
#pragma unroll 1
    for (int t = 0; t < NT_TILES; t += 2) {
      if (aw == 0 && lane == 0) lds_st(L, C_PROG, t);
      if (t == NT_TILES - 2) { if (lane == 0) lds_st(L, C_ATTEND, 1); lds_wait_ge(L, C_WOREAD, 4); }
      attn_tile<0>(L, c, st, t, NT_TILES - 1 - t, lane, aw);
      attn_tile<1>(L, c, st, t + 1, NT_TILES - 2 - t, lane, aw);
    }
    const float lsum = xrow16_sum(st.l);
    LAS float* mr = (LAS float*)(L + L_STG + aw * 2048);
    LAS float* mlr = (LAS float*)(L + L_STG + 8192);
    if (i < 4) {
#pragma unroll
      for (int d = 0; d < 8; ++d) *(LAS f32x4*)(mr + i * 128 + 16 * d + 4 * g) = st.acc[d];
      if (g == 0) { mlr[i * 8 + aw] = st.m; mlr[i * 8 + 4 + aw] = lsum; }
    }
    WAIT_LGKM0();
    unsigned old = 0;
    if (lane == 0) old = lds_add(L, C_MRG);
    old = __builtin_amdgcn_readfirstlane(old);
    asm volatile("" ::: "memory");
    if (old == 3u) {
      const f32x4 mw = *(LAS f32x4*)(mlr + g * 8), lw = *(LAS f32x4*)(mlr + g * 8 + 4);
      f32x4 a0[4], a1[4];
#pragma unroll
      for (int ww = 0; ww < 4; ++ww) { LAS float* rec = (LAS float*)(L + L_STG + ww * 2048) + g * 128 + 4 * i; a0[ww] = *(LAS f32x4*)rec; a1[ww] = *(LAS f32x4*)(rec + 64); }
      const float M = fmaxf(fmaxf(mw[0], mw[1]), fmaxf(mw[2], mw[3]));
      float Ls = 0.f; f32x4 o0 = {0.f, 0.f, 0.f, 0.f}, o1 = {0.f, 0.f, 0.f, 0.f};
#pragma unroll
      for (int ww = 0; ww < 4; ++ww) {
        const float wt = __builtin_amdgcn_exp2f((mw[ww] - M) * 1.4426950408889634f);
        Ls = fmaf(wt, lw[ww], Ls); o0 += wt * a0[ww]; o1 += wt * a1[ww]; }
      const unsigned po = WS_PART + ((b * 32 + kvh * 4 + g) * 4 + sp) * 512 + i * 16;
      __builtin_amdgcn_raw_buffer_store_b128(u32x4{cvtpk(o0[0], o0[1]), T32, cvtpk(o0[2], o0[3]), T32}, rsW, po, 0, 16);
      __builtin_amdgcn_raw_buffer_store_b128(u32x4{cvtpk(o1[0], o1[1]), T32, cvtpk(o1[2], o1[3]), T32}, rsW, po + 256, 0, 16);
      if (i == 0) __builtin_amdgcn_raw_buffer_store_b128(u32x4{__float_as_uint(M), T32, __float_as_uint(Ls), T32}, rsW, WS_ML + (((kvh * 4 + g) * 8 + b) * 4 + sp) * 16, 0, 16);
    }
    return;
  }

  const int hw = wave - 4;
  u32x4 xv = *(const GAS u32x4*)(x + hw * DM + lane * 8);
  u64 E = 0;
  if (hw == 0) E = __hip_atomic_load((GAS u64*)(ws + WS_EPOCH), RLX_AGENT);
  {
    const u64 qa = (u64)wq;
    const u32x4 rsQ = {(unsigned)qa, (unsigned)(qa >> 32), 4u << 20, 0x00020000u};
    const unsigned n0 = xc * 512 + r * 16;
    const unsigned vo = (lane >> 1) * 8192 + (lane & 1) * 16;
    dma1(vo, rsQ, (2 * hw) * 262144u + n0 * 2, L_STG + (2 * hw) * 1024);
    dma1(vo, rsQ, (2 * hw + 1) * 262144u + n0 * 2, L_STG + (2 * hw + 1) * 1024);
  }
  if (hw == 3) { if (lane < 32) lds_st(L, lane, 0u); WAIT_LGKM0(); }
  __builtin_amdgcn_s_barrier();
  asm volatile("" : "+v"(xv), "+v"(E) :: "memory");
  {
    float f[8];
#pragma unroll
    for (int k = 0; k < 4; ++k) { f[2 * k] = lo16(xv[k]); f[2 * k + 1] = hi16(xv[k]); }
    float s = ((f[0] + f[1]) + (f[2] + f[3])) + ((f[4] + f[5]) + (f[6] + f[7]));
    s = wave_sum(s);
    const float mean = s * (1.f / DM);
    float q = 0.f;
#pragma unroll
    for (int k = 0; k < 8; ++k) { const float d = f[k] - mean; q = fmaf(d, d, q); }
    q = wave_sum(q);
    const float var = rbf(q * (1.f / DM));
    const float mb = rbf(mean);
    const float sd = rbf(sqrtf(rbf(var + 1e-5f)));
    u32x4 o;
#pragma unroll
    for (int k = 0; k < 4; ++k) o[k] = cvtpk(rbf(f[2 * k] - mb) / sd, rbf(f[2 * k + 1] - mb) / sd);
    *(LAS u32x4*)(L + L_XN + hw * XN_STRIDE + lane * 16) = o;
  }
  WAIT_VM(0);
  if (hw == 0 && lane == 0) { const u64 T = E + 1; lds_st(L, C_TLO, (unsigned)T); lds_st(L, C_THI, (unsigned)(T >> 32)); }
  WAIT_LGKM0();
  if (lane == 0) lds_add(L, C_LN);
  if (hw == 0) lds_wait_ge<1>(L, C_LN, 8); else lds_wait_ge<6>(L, C_LN, 8);
  const unsigned Tlo = lds_ldu(L, C_TLO), Thi = lds_ldu(L, C_THI);
  const unsigned tg_lo = Tlo << 16, tg_hi = (Thi << 16) | (Tlo >> 16);
#define TAG_OK(lo_, hi_) ((((lo_) ^ tg_lo) >> 16) == 0 && (hi_) == tg_hi)
  const int gam = xc + 8 * sp, f0 = gam * 64 + (r & 7) * 8;
  const int hd = 4 * xc + sp, cbk = r & 7;

  if (hw == 0) {
    const f32x4 d = proj16<1>(L, lane);
    const unsigned pk = i < 8 ? cvtpk(d[0], d[1]) : cvtpk(d[2], d[3]);
    __builtin_amdgcn_raw_buffer_store_b128(u32x4{tg_lo | (pk & 0xffffu), tg_hi, tg_lo | (pk >> 16), tg_hi}, rsW,
                                           WS_QG + ((i & 7) * 4096 + xc * 512 + r * 16 + 4 * g + 2 * (i >> 3)) * 8, 0, 16);
    if (lane == 0) lds_st(L, C_STGFREE, 1);
  } else if (hw == 1) {
    lds_wait_ge<8>(L, C_GUST, 4);
    if (gam < 28) {
      const f32x4 d = proj16<0>(L, lane);
      float hv[2];
#pragma unroll
      for (int k = 0; k < 2; ++k) {
        const float own = rbf(g < 2 ? d[k] : d[2 + k]);
        const float oth = dpp<XOR8>(own);
        hv[k] = own > 0.f ? own * oth : 0.f;
      }
      const unsigned pk = cvtpk(hv[0], hv[1]);
      const int b0 = 4 * (g & 1) + (g < 2 ? 0 : 2);
      if (i < 8) __builtin_amdgcn_raw_buffer_store_b128(u32x4{tg_lo | (pk & 0xffffu), tg_hi, tg_lo | (pk >> 16), tg_hi}, rsW, WS_HG + ((f0 + i) * 8 + b0) * 8, 0, 16);
    }
    if (lane == 0) lds_st(L, C_HDONE, 1);
  } else if (hw == 2) {
    const unsigned qo = WS_QG + (b * 4096 + kvh * 512) * 8 + lane * 16;
    bool okk[4] = {false, false, false, false};
    lds_wait_ge<4>(L, C_STGFREE, 1);
#pragma unroll 1
    for (unsigned spins = 0; spins < SPIN_CAP; ++spins) {
      asm volatile("" ::: "memory");
      u32x4 v[4];
#pragma unroll
      for (int k = 0; k < 4; ++k) if (!okk[k]) v[k] = __builtin_amdgcn_raw_buffer_load_b128(rsW, qo + k * 1024, 0, 16);
#pragma unroll
      for (int k = 0; k < 4; ++k) if (!okk[k]) {
        okk[k] = TAG_OK(v[k][0], v[k][1]) && TAG_OK(v[k][2], v[k][3]);
        *(LAS unsigned*)(L + L_Q + (lane + 64 * k) * 4) = (v[k][0] & 0xffffu) | (v[k][2] << 16);
      }
      if (__all(okk[0] && okk[1] && okk[2] && okk[3])) break;
      __builtin_amdgcn_s_sleep(1);
    }
    WAIT_LGKM0();
    if (lane == 0) lds_st(L, C_QREADY, 1);
  } else if (hw >= 4) {
    const int h4 = hw - 4;
    const __amdgpu_buffer_rsrc_t rsD = __builtin_amdgcn_make_buffer_rsrc((void*)wd, 0, FF * DM * 2, 0x00020000);
    const GAS unsigned short* sgu = (const GAS unsigned short*)((lane & 1) ? wu : wg) + (size_t)(128 * h4 + (lane >> 1)) * FF + (gam < 28 ? f0 : 0);
    const GAS unsigned short* swo = (const GAS unsigned short*)wo + (size_t)(128 * hd + 32 * h4 + (lane >> 3)) * DM + cbk * 64 + (lane & 7) * 8;
    u32x4 rg[4], ro[4], rd_[2], xk[8], xvv[8];
    {
      const __amdgpu_buffer_rsrc_t rK = __builtin_amdgcn_make_buffer_rsrc((void*)kc, 0, 128u << 20, 0x00020000), rV = __builtin_amdgcn_make_buffer_rsrc((void*)vc, 0, 128u << 20, 0x00020000);
      const unsigned so = kv_off + 2u * 32768u + h4 * 4096;
      const unsigned vk0 = g * 256 + ((i ^ g) << 4), vv0 = g * 256 + ((i ^ (2 * g)) << 4);
      lds_wait_ge<4>(L, C_RING, 4);
#pragma unroll
      for (int u = 0; u < 8; ++u) { if ((u & 3) == 0) WAIT_VMI(HCAP); xk[u] = __builtin_amdgcn_raw_buffer_load_b128(rK, (vk0 ^ ((u & 3) << 6)) + (u & 3) * 1024 + (u >> 2) * 16384, so, 2); }
#pragma unroll
      for (int u = 0; u < 8; ++u) { if ((u & 3) == 0) WAIT_VMI(HCAP); xvv[u] = __builtin_amdgcn_raw_buffer_load_b128(rV, (vv0 ^ ((u & 1) << 7)) + (u & 3) * 1024 + (u >> 2) * 16384, so, 2); }
      WAIT_VMI(HCAP);
    }
#pragma unroll
    for (int u = 0; u < 4; ++u) ro[u] = __builtin_nontemporal_load((const GAS u32x4*)(swo + (size_t)u * 8 * DM));
#pragma unroll
    for (int u = 0; u < 2; ++u) rd_[u] = __builtin_amdgcn_raw_buffer_load_b128(rsD, (unsigned)((56 * hd + 16 * h4 + 8 * u + (lane >> 3)) * 1024 + cbk * 128 + (lane & 7) * 16), 0, 2);
    lds_wait_ge<2>(L, C_XF + h4, 1);
#pragma unroll
    for (int u = 0; u < 8; ++u) *(LAS u32x4*)(L + L_RING + h4 * 32768 + u * 1024 + lane * 16) = xk[u];
    WAIT_LGKM0();
    if (lane == 0) lds_st(L, C_XR + h4, 1);
    lds_wait_ge<1>(L, C_XF + h4, 2);
#pragma unroll
    for (int u = 0; u < 8; ++u) *(LAS u32x4*)(L + L_RING + h4 * 32768 + 8192 + u * 1024 + lane * 16) = xvv[u];
    WAIT_LGKM0();
    if (lane == 0) lds_st(L, C_XR + h4, 2);
#pragma unroll
    for (int u = 0; u < 4; ++u) rg[u] = *(const GAS u32x4*)(sgu + (size_t)u * 32 * FF);
    lds_wait_ge(L, C_STGFREE, 1);
#pragma unroll
    for (int u = 0; u < 4; ++u) *(LAS u32x4*)(L + L_STG + (4 * h4 + u) * 1024 + lane * 16) = rg[u];
    WAIT_LGKM0();
    if (lane == 0) lds_add(L, C_GUST);
    lds_wait_ge<4>(L, C_HDONE, 1);
#pragma unroll
    for (int u = 0; u < 4; ++u) *(LAS u32x4*)(L + L_STG + (4 * h4 + u) * 1024 + lane * 16) = ro[u];
#pragma unroll
    for (int u = 0; u < 2; ++u) *(LAS u32x4*)(L + L_XN + (2 * h4 + u) * 1024 + lane * 16) = rd_[u];
    WAIT_LGKM0();
    if (lane == 0) lds_add(L, C_WOLD);
  }
  {
    const int fl = 7 * hw + (lane >> 3), bb = lane & 7;
    const unsigned ho = WS_HG + ((56 * hd + 7 * hw) * 8 + (lane < 56 ? lane : 0)) * 8;
    u32x2 hv = {0u, 0u};
    bool ok = false;
    lds_wait_ge<32>(L, C_PROG, GATE_H);
#pragma unroll 1
    for (unsigned spins = 0; spins < SPIN_CAP; ++spins) {
      asm volatile("" ::: "memory");
      if (!ok) { hv = __builtin_amdgcn_raw_buffer_load_b64(rsW, ho, 0, 16); ok = TAG_OK(hv[0], hv[1]); }
      if (__all(ok)) break;
      __builtin_amdgcn_s_sleep(SLP_MID);
    }
    if (lane < 56) *(LAS unsigned short*)(L + L_OA + bb * OA_STRIDE + fl * 2) = (unsigned short)(hv[0] & 0xffffu);
    if (lane < 4) *(LAS unsigned*)(L + L_OA + hw * OA_STRIDE + 112 + lane * 4) = 0u;
    WAIT_LGKM0();
    if (lane == 0) lds_add(L, C_OG);
  }
  const int bf = r >> 2, cb = 4 * xc + (r & 3), n1 = cb * 16;
  const unsigned ba = (g * 8 + (i >> 2)) * 128 + (lane & 3) * 8 + (hw & 3) * 32;
  const unsigned oa = L_OA + (i & 7) * OA_STRIDE + g * 16;
  bf16x8 wf[4];
  unsigned y2p = 0;
  float xr[2] = {0.f, 0.f};
  if (hw < 4) {
    lds_wait_ge<8>(L, C_WOLD, 4);
    lds_wait_ge<4>(L, C_OG, 8);
    f32x4 d = {0.f, 0.f, 0.f, 0.f};
#pragma unroll
    for (int s = 0; s < 2; ++s) {
      const bf16x4 a0 = __builtin_amdgcn_ds_read_tr16_b64_v4bf16((LAS bf16x4*)(L + L_XN + ba + s * 4096));
      const bf16x4 a1 = __builtin_amdgcn_ds_read_tr16_b64_v4bf16((LAS bf16x4*)(L + L_XN + ba + s * 4096 + 512));
      d = __builtin_amdgcn_mfma_f32_16x16x32_bf16(__builtin_shufflevector(a0, a1, 0, 1, 2, 3, 4, 5, 6, 7), *(LAS bf16x8*)(L + oa + s * 64), d, 0, 0, 0);
    }
    y2p = i < 8 ? cvtpk(d[0], d[1]) : cvtpk(d[2], d[3]);
#pragma unroll
    for (int s = 0; s < 4; ++s) {
      const bf16x4 a0 = __builtin_amdgcn_ds_read_tr16_b64_v4bf16((LAS bf16x4*)(L + L_STG + ba + s * 4096));
      const bf16x4 a1 = __builtin_amdgcn_ds_read_tr16_b64_v4bf16((LAS bf16x4*)(L + L_STG + ba + s * 4096 + 512));
      wf[s] = __builtin_shufflevector(a0, a1, 0, 1, 2, 3, 4, 5, 6, 7);
    }
    asm volatile("s_waitcnt lgkmcnt(0)" : "+v"(wf[0]), "+v"(wf[1]), "+v"(wf[2]), "+v"(wf[3]), "+v"(y2p) :: "memory");
    if (lane == 0) lds_add(L, C_WOREAD);
  } else if (hw == 6) {
    const unsigned xq = *(const GAS unsigned*)(x + bf * DM + n1 + (lane & 7) * 2);
    xr[0] = lo16(xq); xr[1] = hi16(xq);
  } else if (hw == 5 && w == 0) {
#pragma unroll 1
    for (unsigned spins = 0; spins < SPIN_CAP; ++spins) {
      bool ok = true;
#pragma unroll
      for (int k = 0; k < 4; ++k) { const u32x2 v = __builtin_amdgcn_raw_buffer_load_b64(rsW, WS_QG + (lane + 64 * k) * 128, 0, 16); ok &= TAG_OK(v[0], v[1]); }
      if (__all(ok)) break;
      __builtin_amdgcn_s_sleep(SLP_MID);
      asm volatile("" ::: "memory");
    }
    if (lane == 0) __hip_atomic_store((GAS u64*)(ws + WS_EPOCH), ((u64)Thi << 32) | Tlo, RLX_AGENT);
  }
  {
    lds_wait_ge<4>(L, C_WOREAD, 4);
    lds_wait_ge<16>(L, C_ATTEND, 1);
    const unsigned po = WS_PART + ((hw * 32 + hd) * 4 + g) * 512 + i * 16;
    const unsigned mo = WS_ML + ((hd * 8 + hw) * 4 + g) * 16;
    u32x4 pv0 = {0u, 0u, 0u, 0u}, pv1 = {0u, 0u, 0u, 0u}, ml = {0u, 0u, 0u, 0u};
    if (hw == 7) {
      bool ok = lane >= 32;
#pragma unroll 1
      for (unsigned spins = 0; spins < SPIN_CAP; ++spins) {
        asm volatile("" ::: "memory");
        if (!ok) {
          ml = __builtin_amdgcn_raw_buffer_load_b128(rsW, WS_ML + hd * 512 + lane * 16, 0, 16);
          ok = ml[1] == Tlo && ml[3] == Tlo;
        }
        const unsigned miss = ~(unsigned)__ballot(ok);
        if (lane == 0) lds_st(L, C_MLMASK, ~miss);
        const unsigned two = (miss & (miss >> 1) & 0x77777777u) | (miss & (miss >> 2) & 0x33333333u) | (miss & (miss >> 3) & 0x11111111u);
        if (two == 0u) break;
        __builtin_amdgcn_s_sleep(SLP_PART);
      }
      if (lane == 0) lds_st(L, C_MLMASK, 0xffffffffu);
    } else {
      while (__builtin_popcount((lds_ldu(L, C_MLMASK) >> (4 * hw)) & 15u) < 3) __builtin_amdgcn_s_sleep(1);
      asm volatile("" ::: "memory");
    }
    bool ok = false;
#pragma unroll 1
    for (unsigned spins = 0; spins < SPIN_CAP; ++spins) {
      asm volatile("" ::: "memory");
      if (!ok) {
        pv0 = __builtin_amdgcn_raw_buffer_load_b128(rsW, po, 0, 16);
        pv1 = __builtin_amdgcn_raw_buffer_load_b128(rsW, po + 256, 0, 16);
        ml = __builtin_amdgcn_raw_buffer_load_b128(rsW, mo, 0, 16);
        ok = pv0[1] == Tlo && pv0[3] == Tlo && pv1[1] == Tlo && pv1[3] == Tlo && ml[1] == Tlo && ml[3] == Tlo;
      }
      if (__all(ok)) break;
      __builtin_amdgcn_s_sleep(SLP_DIR);
    }
    const float mj = __uint_as_float(ml[0]);
    const float M = xrow16_max(mj);
    const float wt = __builtin_amdgcn_exp2f((mj - M) * 1.4426950408889634f);
    const float inv = 1.f / xrow16_sum(wt * __uint_as_float(ml[2]));
    const u32x2 oa0 = {cvtpk(xrow16_sum(wt * lo16(pv0[0])) * inv, xrow16_sum(wt * hi16(pv0[0])) * inv), cvtpk(xrow16_sum(wt * lo16(pv0[2])) * inv, xrow16_sum(wt * hi16(pv0[2])) * inv)};
    const u32x2 oa1 = {cvtpk(xrow16_sum(wt * lo16(pv1[0])) * inv, xrow16_sum(wt * hi16(pv1[0])) * inv), cvtpk(xrow16_sum(wt * lo16(pv1[2])) * inv, xrow16_sum(wt * hi16(pv1[2])) * inv)};
    if (g == 0) { *(LAS u32x2*)(L + L_OA + hw * OA_STRIDE + i * 8) = oa0; *(LAS u32x2*)(L + L_OA + hw * OA_STRIDE + 128 + i * 8) = oa1; }
    WAIT_LGKM0();
    if (lane == 0) lds_add(L, C_OG);
  }
  if (hw < 4) {
    lds_wait_ge(L, C_OG, 16);
    f32x4 d = {0.f, 0.f, 0.f, 0.f};
#pragma unroll
    for (int s = 0; s < 4; ++s) d = __builtin_amdgcn_mfma_f32_16x16x32_bf16(wf[s], *(LAS bf16x8*)(L + oa + s * 64), d, 0, 0, 0);
    const unsigned yp = i < 8 ? cvtpk(d[0], d[1]) : cvtpk(d[2], d[3]);
    const unsigned yo = ((((i & 7) * 32 + 4 * cbk + hw) * 32 + hd) * 16 + 4 * g + 2 * (i >> 3)) * 8;
    __builtin_amdgcn_raw_buffer_store_b128(u32x4{(yp & 0xffffu) | (y2p << 16), Tlo, (yp >> 16) | (y2p & 0xffff0000u), Tlo}, rsW, WS_Y + yo, 0, 16);
  } else if (hw == 6) {
    const unsigned io = WS_Y + (bf * 32 + cb) * 4096 + lane * 16;
    u32x4 y[4];
    bool okk[4] = {false, false, false, false};
#pragma unroll 1
    for (unsigned spins = 0; spins < SPIN_CAP; ++spins) {
      asm volatile("" ::: "memory");
#pragma unroll
      for (int k = 0; k < 4; ++k) if (!okk[k]) y[k] = __builtin_amdgcn_raw_buffer_load_b128(rsW, io + k * 1024, 0, 16);
#pragma unroll
      for (int k = 0; k < 4; ++k) okk[k] = y[k][1] == Tlo && y[k][3] == Tlo;
      if (__all(okk[0] && okk[1] && okk[2] && okk[3])) break;
      __builtin_amdgcn_s_sleep(SLP_Y);
    }
    float o[2];
#pragma unroll
    for (int k = 0; k < 2; ++k) {
      float t = (lo16(y[0][2 * k]) + lo16(y[1][2 * k])) + (lo16(y[2][2 * k]) + lo16(y[3][2 * k]));
      float u = (hi16(y[0][2 * k]) + hi16(y[1][2 * k])) + (hi16(y[2][2 * k]) + hi16(y[3][2 * k]));
      t += dpp<XOR8>(t); t = rbf(xrow16_sum(t));
      u += dpp<XOR8>(u); u = rbf(xrow16_sum(u));
      const float gate = rbf(u / (1.f + __expf(-u)));
      o[k] = xr[k] + rbf(t * gate);
    }
    if (lane < 8) *(GAS unsigned*)(out + bf * DM + n1 + lane * 2) = cvtpk(o[0], o[1]);
  }
}

extern "C" void kernel_launch(void* const* d_in, const int* in_sizes, int n_in, void* d_out, int out_size, void* d_ws, size_t ws_size, hipStream_t stream) {
  const unsigned short* x = (const unsigned short*)d_in[0];
  const unsigned short* kc = (const unsigned short*)d_in[1];
  const unsigned short* vc = (const unsigned short*)d_in[2];
  const unsigned short* wq = (const unsigned short*)d_in[3];
  const unsigned short* wo = (const unsigned short*)d_in[4];
  const unsigned short* wg = (const unsigned short*)d_in[6];
  const unsigned short* wu = (const unsigned short*)d_in[7];
  const unsigned short* wd = (const unsigned short*)d_in[8];
  (void)hipFuncSetAttribute((const void*)fused_decode, hipFuncAttributeMaxDynamicSharedMemorySize, L_END);
  fused_decode<<<dim3(NWG), dim3(NTHR), L_END, stream>>>(x, kc, vc, wq, wo, wg, wu, wd, (unsigned short*)d_out, (unsigned char*)d_ws);
}
```

```cpp
#include <hip/hip_runtime.h>
#include <hip/hip_bf16.h>
#include <cstdint>

typedef __bf16 bf16x2 __attribute__((ext_vector_type(2)));
typedef __bf16 bf16x4 __attribute__((ext_vector_type(4)));
typedef __bf16 bf16x8 __attribute__((ext_vector_type(8)));
typedef unsigned u32x2 __attribute__((ext_vector_type(2)));
typedef unsigned u32x4 __attribute__((ext_vector_type(4)));
typedef float f32x2 __attribute__((ext_vector_type(2)));
typedef float f32x4 __attribute__((ext_vector_type(4)));
typedef unsigned long long u64;
#define LAS __attribute__((address_space(3)))
#define GAS __attribute__((address_space(1)))
#define RLX_AGENT __ATOMIC_RELAXED, __HIP_MEMORY_SCOPE_AGENT
#define RLX_WG __ATOMIC_RELAXED, __HIP_MEMORY_SCOPE_WORKGROUP

constexpr int NB = 8, DM = 512, HKV = 8, SK = 8192, FF = 1792;
constexpr int NWG = 256, NTHR = 768;
constexpr int NT_TILES = 16;

#define CAPG 6
#define PCAP 2
#define HCAP 2
#define GATE_H 4
#define SLP_PART 4
#define SLP_DIR 2
#define SLP_Y 2
#define SLP_MID 64

constexpr unsigned WS_EPOCH = 0;
constexpr unsigned WS_ML = 8192;
constexpr unsigned WS_QG = 32768;
constexpr unsigned WS_HG = WS_QG + NB * 4096 * 8;
constexpr unsigned WS_PART = WS_HG + NB * FF * 8;
constexpr unsigned WS_Y = WS_PART + NWG * 2048;
constexpr unsigned WS_END = WS_Y + (1u << 20);

constexpr unsigned L_RING = 0;
constexpr unsigned L_STG = 131072;
constexpr unsigned L_XN = L_STG + 16384;
constexpr unsigned XN_STRIDE = 1040;
constexpr unsigned L_Q = L_XN + 8 * XN_STRIDE;
constexpr unsigned L_OA = L_Q + 1024;
constexpr unsigned OA_STRIDE = 272;
constexpr unsigned L_CTL = L_OA + 8 * OA_STRIDE;
constexpr unsigned L_END = L_CTL + 128;
static_assert(L_END <= 163840, "LDS");
enum { C_LN = 0, C_STGFREE, C_QREADY, C_HDONE, C_MRG, C_ATTEND, C_TLO, C_THI, C_WOLD, C_WOREAD, C_OG, C_PROG, C_MLMASK, C_RING, C_GUST,
       C_XF = 20  , C_XR = 24   };

__device__ __forceinline__ unsigned cvtpk(float a, float b) {
  bf16x2 t = __builtin_convertvector(f32x2{a, b}, bf16x2);
  return __builtin_bit_cast(unsigned, t);
}
__device__ __forceinline__ float lo16(unsigned u) { return __builtin_bit_cast(float, u << 16); }
__device__ __forceinline__ float hi16(unsigned u) { return __builtin_bit_cast(float, u & 0xffff0000u); }
__device__ __forceinline__ float rbf(float f) { return lo16(cvtpk(f, f)); }
template <int CTRL> __device__ __forceinline__ float dpp(float x) {
  return __builtin_bit_cast(float, __builtin_amdgcn_mov_dpp(__builtin_bit_cast(int, x), CTRL, 0xf, 0xf, true));
}
constexpr int XOR1 = 0xB1, XOR2 = 0x4E, XOR7 = 0x141, XOR8 = 0x128;
__device__ __forceinline__ float xrow16_max(float x) {
  auto s = __builtin_amdgcn_permlane16_swap(__float_as_uint(x), __float_as_uint(x), false, false);
  x = fmaxf(__uint_as_float(s[0]), __uint_as_float(s[1]));
  auto t = __builtin_amdgcn_permlane32_swap(__float_as_uint(x), __float_as_uint(x), false, false);
  return fmaxf(__uint_as_float(t[0]), __uint_as_float(t[1]));
}
__device__ __forceinline__ float xrow16_sum(float x) {
  auto s = __builtin_amdgcn_permlane16_swap(__float_as_uint(x), __float_as_uint(x), false, false);
  x = __uint_as_float(s[0]) + __uint_as_float(s[1]);
  auto t = __builtin_amdgcn_permlane32_swap(__float_as_uint(x), __float_as_uint(x), false, false);
  return __uint_as_float(t[0]) + __uint_as_float(t[1]);
}
__device__ __forceinline__ float wave_sum(float x) {
  x += dpp<XOR1>(x); x += dpp<XOR2>(x); x += dpp<XOR7>(x); x += dpp<XOR8>(x);
  return xrow16_sum(x);
}

__device__ __forceinline__ void dma4(unsigned v0, unsigned v1, unsigned v2, unsigned v3, u32x4 rs, unsigned so, unsigned ldsaddr) {
  unsigned keep;
  asm volatile(
    "s_mov_b32 %0, m0\n\t"
    "s_mov_b32 m0, %7\n\t"
    "s_nop 0\n\t"
    "buffer_load_dwordx4 %1, %5, %6 offen nt lds\n\t"
    "buffer_load_dwordx4 %2, %5, %6 offen offset:1024 nt lds\n\t"
    "buffer_load_dwordx4 %3, %5, %6 offen offset:2048 nt lds\n\t"
    "buffer_load_dwordx4 %4, %5, %6 offen offset:3072 nt lds\n\t"
    "s_mov_b32 m0, %0"
    : "=&s"(keep) : "v"(v0), "v"(v1), "v"(v2), "v"(v3), "s"(rs), "s"(so), "s"(ldsaddr) : "memory");
}
__device__ __forceinline__ void dma1(unsigned v0, u32x4 rs, unsigned so, unsigned ldsaddr) {
  unsigned keep;
  asm volatile(
    "s_mov_b32 %0, m0\n\t"
    "s_mov_b32 m0, %4\n\t"
    "s_nop 0\n\t"
    "buffer_load_dwordx4 %1, %2, %3 offen lds\n\t"
    "s_mov_b32 m0, %0"
    : "=&s"(keep) : "v"(v0), "s"(rs), "s"(so), "s"(ldsaddr) : "memory");
}
#define WAIT_VM(n) asm volatile("s_waitcnt vmcnt(" #n ")" ::: "memory")
#define WAIT_VMI(n) asm volatile("s_waitcnt vmcnt(%0)" :: "i"(n) : "memory")
#define WAIT_LGKM0() asm volatile("s_waitcnt lgkmcnt(0)" ::: "memory")

__device__ __forceinline__ unsigned lds_ld(LAS unsigned char* L, int idx) { return __hip_atomic_load((LAS unsigned*)(L + L_CTL) + idx, RLX_WG); }
__device__ __forceinline__ unsigned lds_ldu(LAS unsigned char* L, int idx) { return (unsigned)__builtin_amdgcn_readfirstlane(lds_ld(L, idx)); }
__device__ __forceinline__ void lds_st(LAS unsigned char* L, int idx, unsigned v) { __hip_atomic_store((LAS unsigned*)(L + L_CTL) + idx, v, RLX_WG); }
__device__ __forceinline__ unsigned lds_add(LAS unsigned char* L, int idx) { return __hip_atomic_fetch_add((LAS unsigned*)(L + L_CTL) + idx, 1u, RLX_WG); }
template <int SLP = 1>
__device__ __forceinline__ void lds_wait_ge(LAS unsigned char* L, int idx, unsigned want) {
  while (lds_ldu(L, idx) < want) __builtin_amdgcn_s_sleep(SLP);
  asm volatile("" ::: "memory");
}
constexpr unsigned SPIN_CAP = 1u << 15;

__device__ __forceinline__ u32x2 pack2(float a, float b, unsigned tag24) {
  const unsigned ua = (__float_as_uint(a) + 0x800u) >> 12, ub = (__float_as_uint(b) + 0x800u) >> 12;
  return u32x2{ua | (ub << 20), (ub >> 12) | (tag24 << 8)};
}
__device__ __forceinline__ float unpack_a(unsigned lo) { return __uint_as_float(lo << 12); }
__device__ __forceinline__ float unpack_b(unsigned lo, unsigned hi) { return __uint_as_float(((lo >> 20) | (hi << 12)) << 12); }

struct AttnState { f32x4 acc[8]; float m, l; };
struct AttnConst { u32x4 rsK, rsV; unsigned gbase, ring, vk0, vv0, kaddr, vaddr; bf16x8 qf[4]; };

#define DMA_K(c_, so_, ld_) dma4((c_).vk0, (c_).vk0 ^ 64, (c_).vk0 ^ 128, (c_).vk0 ^ 192, (c_).rsK, (so_), (ld_))
#define DMA_V(c_, so_, ld_) dma4((c_).vv0, (c_).vv0 ^ 128, (c_).vv0, (c_).vv0 ^ 128, (c_).rsV, (so_), (ld_))

template <int SLOT>
__device__ __forceinline__ void attn_tile(LAS unsigned char* L, const AttnConst& c, AttnState& st, const int t, const int rem, const int lane, const int aw) {
  constexpr float SCALE = 0.088388347648318440f, LOG2E = 1.4426950408889634f;
  const unsigned so2 = c.gbase + (unsigned)(t + 2) * 32768u, ld2 = c.ring + SLOT * 16384;
  if (rem >= 1) WAIT_VM(24); else WAIT_VM(8);
  if (SLOT == 0 && t == 2) lds_wait_ge(L, C_XR + aw, 1);
  f32x4 s0 = {0.f, 0.f, 0.f, 0.f}, s1 = {0.f, 0.f, 0.f, 0.f};
#pragma unroll
  for (int s = 0; s < 4; ++s) {
    const bf16x8 k0 = *(LAS bf16x8*)(L + (c.kaddr ^ (s << 6)) + SLOT * 16384);
    const bf16x8 k1 = *(LAS bf16x8*)(L + (c.kaddr ^ (s << 6)) + SLOT * 16384 + 4096);
    s0 = __builtin_amdgcn_mfma_f32_16x16x32_bf16(k0, c.qf[s], s0, 0, 0, 0);
    s1 = __builtin_amdgcn_mfma_f32_16x16x32_bf16(k1, c.qf[s], s1, 0, 0, 0);
  }
  WAIT_LGKM0();
  if (SLOT == 0 && t == 0) { if (lane == 0) lds_st(L, C_XF + aw, 1); }
  else if (rem >= 2) { WAIT_VMI(CAPG); DMA_K(c, so2, ld2); WAIT_VMI(CAPG); DMA_K(c, so2 + 16384u, ld2 + 4096); }
  float x[8];
  { unsigned u;
    u = cvtpk(s0[0], s0[1]); u = cvtpk(lo16(u) * SCALE, hi16(u) * SCALE); x[0] = lo16(u); x[1] = hi16(u);
    u = cvtpk(s0[2], s0[3]); u = cvtpk(lo16(u) * SCALE, hi16(u) * SCALE); x[2] = lo16(u); x[3] = hi16(u);
    u = cvtpk(s1[0], s1[1]); u = cvtpk(lo16(u) * SCALE, hi16(u) * SCALE); x[4] = lo16(u); x[5] = hi16(u);
    u = cvtpk(s1[2], s1[3]); u = cvtpk(lo16(u) * SCALE, hi16(u) * SCALE); x[6] = lo16(u); x[7] = hi16(u); }
  float pm = fmaxf(fmaxf(fmaxf(x[0], x[1]), fmaxf(x[2], x[3])), fmaxf(fmaxf(x[4], x[5]), fmaxf(x[6], x[7])));
  pm = xrow16_max(pm);
  if (__any(pm > st.m)) {
    const float mn = fmaxf(st.m, pm);
    const float alpha = __builtin_amdgcn_exp2f((st.m - mn) * LOG2E);
    st.l *= alpha; st.m = mn;
#pragma unroll
    for (int d = 0; d < 8; ++d) st.acc[d] *= alpha;
  }
  const float mb = st.m * LOG2E;
  float p[8];
#pragma unroll
  for (int e = 0; e < 8; ++e) p[e] = __builtin_amdgcn_exp2f(fmaf(x[e], LOG2E, -mb));
  st.l += ((p[0] + p[1]) + (p[2] + p[3])) + ((p[4] + p[5]) + (p[6] + p[7]));
  const u32x4 pw = {cvtpk(p[0], p[1]), cvtpk(p[2], p[3]), cvtpk(p[4], p[5]), cvtpk(p[6], p[7])};
  const bf16x8 pf = __builtin_bit_cast(bf16x8, pw);
  if (rem >= 2) WAIT_VM(24); else if (rem == 1) WAIT_VM(16); else WAIT_VM(0);
  if (SLOT == 0 && t == 2) lds_wait_ge(L, C_XR + aw, 2);
#pragma unroll
  for (int d = 0; d < 8; ++d) {
    const bf16x4 v0 = __builtin_amdgcn_ds_read_tr16_b64_v4bf16((LAS bf16x4*)(L + (c.vaddr ^ (d << 5)) + SLOT * 16384));
    const bf16x4 v1 = __builtin_amdgcn_ds_read_tr16_b64_v4bf16((LAS bf16x4*)(L + (c.vaddr ^ (d << 5)) + SLOT * 16384 + 4096));
    const bf16x8 vf = __builtin_shufflevector(v0, v1, 0, 1, 2, 3, 4, 5, 6, 7);
    st.acc[d] = __builtin_amdgcn_mfma_f32_16x16x32_bf16(vf, pf, st.acc[d], 0, 0, 0);
  }
  WAIT_LGKM0();
  if (SLOT == 0 && t == 0) { if (lane == 0) lds_st(L, C_XF + aw, 2); }
  else if (rem >= 2) { WAIT_VMI(CAPG); DMA_V(c, so2, ld2 + 8192); WAIT_VMI(CAPG); DMA_V(c, so2 + 16384u, ld2 + 8192 + 4096); }
}

template <int TR>
__device__ __forceinline__ f32x4 proj16(LAS unsigned char* L, int lane) {
  const int g = lane >> 4, i = lane & 15;
  unsigned aa = L_XN + (i & 7) * XN_STRIDE + g * 16;
  unsigned ba = L_STG + g * 256 + i * 8;
  f32x4 d0 = {0.f, 0.f, 0.f, 0.f}, d1 = {0.f, 0.f, 0.f, 0.f};
#pragma unroll 2
  for (int s = 0; s < 4; ++s) {
    bf16x8 a[4], bb[4];
#pragma unroll
    for (int k = 0; k < 4; ++k) {
      a[k] = *(LAS bf16x8*)(L + aa + k * 64);
      const bf16x4 b0 = __builtin_amdgcn_ds_read_tr16_b64_v4bf16((LAS bf16x4*)(L + ba + k * 1024));
      const bf16x4 b1 = __builtin_amdgcn_ds_read_tr16_b64_v4bf16((LAS bf16x4*)(L + ba + k * 1024 + 128));
      bb[k] = __builtin_shufflevector(b0, b1, 0, 1, 2, 3, 4, 5, 6, 7);
    }
    if (TR) {
      d0 = __builtin_amdgcn_mfma_f32_16x16x32_bf16(bb[0], a[0], d0, 0, 0, 0); d1 = __builtin_amdgcn_mfma_f32_16x16x32_bf16(bb[1], a[1], d1, 0, 0, 0);
      d0 = __builtin_amdgcn_mfma_f32_16x16x32_bf16(bb[2], a[2], d0, 0, 0, 0); d1 = __builtin_amdgcn_mfma_f32_16x16x32_bf16(bb[3], a[3], d1, 0, 0, 0);
    } else {
      d0 = __builtin_amdgcn_mfma_f32_16x16x32_bf16(a[0], bb[0], d0, 0, 0, 0); d1 = __builtin_amdgcn_mfma_f32_16x16x32_bf16(a[1], bb[1], d1, 0, 0, 0);
      d0 = __builtin_amdgcn_mfma_f32_16x16x32_bf16(a[2], bb[2], d0, 0, 0, 0); d1 = __builtin_amdgcn_mfma_f32_16x16x32_bf16(a[3], bb[3], d1, 0, 0, 0);
    }
    aa += 256; ba += 4096;
  }
  return d0 + d1;
}

__global__ void __launch_bounds__(NTHR, 3)
fused_decode(const unsigned short* __restrict__ x, const unsigned short* __restrict__ kc, const unsigned short* __restrict__ vc,
             const unsigned short* __restrict__ wq, const unsigned short* __restrict__ wo, const unsigned short* __restrict__ wg,
             const unsigned short* __restrict__ wu, const unsigned short* __restrict__ wd, unsigned short* __restrict__ out, unsigned char* ws) {
  extern __shared__ __attribute__((aligned(16))) unsigned char lds[];
  LAS unsigned char* L = (LAS unsigned char*)lds;
  const int w = blockIdx.x;
  const int tid = threadIdx.x, lane = tid & 63, wave = __builtin_amdgcn_readfirstlane(tid >> 6);
  const int g = lane >> 4, i = lane & 15;
  const int xc = w & 7, r = w >> 3;
  const int b = r & 7, sp = r >> 3, kvh = (xc + 2 * sp) & 7;
  const __amdgpu_buffer_rsrc_t rsW = __builtin_amdgcn_make_buffer_rsrc(ws, 0, WS_END, 0x00020000);
  const unsigned kv_off = (unsigned)(((b * HKV + kvh) * SK + sp * 2048) * 256);

  if (wave < 4) {
    const int aw = wave;
    const u64 ka = (u64)kc, va = (u64)vc;
    AttnConst c;
    c.rsK = u32x4{(unsigned)ka, (unsigned)(ka >> 32), 128u << 20, 0x00020000u};
    c.rsV = u32x4{(unsigned)va, (unsigned)(va >> 32), 128u << 20, 0x00020000u};
    c.gbase = kv_off + aw * 4096;
    c.ring = L_RING + aw * 32768;
    c.vk0 = g * 256 + ((i ^ g) << 4);
    c.vv0 = g * 256 + ((i ^ (2 * g)) << 4);
    c.kaddr = c.ring + i * 256 + ((i ^ g) << 4);
    const int q4 = i >> 2, pp = lane & 3;
    c.vaddr = c.ring + 8192 + (4 * g + q4) * 256 + ((8 * (g & 1) + 2 * q4) << 4) + 8 * pp;
    __builtin_amdgcn_s_barrier();
#define PCAPW() do { if (lds_ldu(L, C_QREADY)) WAIT_VMI(CAPG); else WAIT_VMI(PCAP); } while (0)
#pragma unroll 1
    for (int u = 0; u < 2; ++u) {
      const unsigned so = c.gbase + u * 32768u, ld = c.ring + u * 16384;
      PCAPW(); DMA_K(c, so, ld);
      PCAPW(); DMA_K(c, so + 16384u, ld + 4096);
      PCAPW(); DMA_V(c, so, ld + 8192);
      if (u == 1 && lane == 0) lds_add(L, C_RING);
      PCAPW(); DMA_V(c, so + 16384u, ld + 8192 + 4096);
    }
    lds_wait_ge(L, C_QREADY, 1);
#pragma unroll
    for (int s = 0; s < 4; ++s) { u32x4 v = *(LAS u32x4*)(L + L_Q + (i & 3) * 256 + 64 * s + 16 * g); if (i >= 4) v = u32x4{0, 0, 0, 0}; c.qf[s] = __builtin_bit_cast(bf16x8, v); }
    AttnState st;
#pragma unroll
    for (int d = 0; d < 8; ++d) st.acc[d] = f32x4{0.f, 0.f, 0.f, 0.f};
    st.m = -INFINITY; st.l = 0.f;
    const unsigned T32 = lds_ldu(L, C_TLO);
    WAIT_VM(0);
#pragma unroll 1
    for (int t = 0; t < NT_TILES; t += 2) {
      if (aw == 0 && lane == 0) lds_st(L, C_PROG, t);
      if (t == NT_TILES - 2) { if (lane == 0) lds_st(L, C_ATTEND, 1); lds_wait_ge(L, C_WOREAD, 4); }
      attn_tile<0>(L, c, st, t, NT_TILES - 1 - t, lane, aw);
      attn_tile<1>(L, c, st, t + 1, NT_TILES - 2 - t, lane, aw);
    }
    const float lsum = xrow16_sum(st.l);
    LAS float* mr = (LAS float*)(L + L_STG + aw * 2048);
    LAS float* mlr = (LAS float*)(L + L_STG + 8192);
    if (i < 4) {
#pragma unroll
      for (int d = 0; d < 8; ++d) *(LAS f32x4*)(mr + i * 128 + 16 * d + 4 * g) = st.acc[d];
      if (g == 0) { mlr[i * 8 + aw] = st.m; mlr[i * 8 + 4 + aw] = lsum; }
    }
    WAIT_LGKM0();
    unsigned old = 0;
    if (lane == 0) old = lds_add(L, C_MRG);
    old = __builtin_amdgcn_readfirstlane(old);
    asm volatile("" ::: "memory");
    if (old == 3u) {
      const f32x4 mw = *(LAS f32x4*)(mlr + g * 8), lw = *(LAS f32x4*)(mlr + g * 8 + 4);
      f32x4 a0[4], a1[4];
#pragma unroll
      for (int ww = 0; ww < 4; ++ww) { LAS float* rec = (LAS float*)(L + L_STG + ww * 2048) + g * 128 + 4 * i; a0[ww] = *(LAS f32x4*)rec; a1[ww] = *(LAS f32x4*)(rec + 64); }
      const float M = fmaxf(fmaxf(mw[0], mw[1]), fmaxf(mw[2], mw[3]));
      float Ls = 0.f; f32x4 o0 = {0.f, 0.f, 0.f, 0.f}, o1 = {0.f, 0.f, 0.f, 0.f};
#pragma unroll
      for (int ww = 0; ww < 4; ++ww) {
        const float wt = __builtin_amdgcn_exp2f((mw[ww] - M) * 1.4426950408889634f);
        Ls = fmaf(wt, lw[ww], Ls); o0 += wt * a0[ww]; o1 += wt * a1[ww]; }
      const unsigned po = WS_PART + ((b * 32 + kvh * 4 + g) * 4 + sp) * 512 + i * 16;
      const unsigned T24 = T32 & 0xffffffu;
      const u32x2 g0 = pack2(o0[0], o0[1], T24), g1 = pack2(o0[2], o0[3], T24), g2 = pack2(o1[0], o1[1], T24), g3 = pack2(o1[2], o1[3], T24);
      __builtin_amdgcn_raw_buffer_store_b128(u32x4{g0[0], g0[1], g1[0], g1[1]}, rsW, po, 0, 16);
      __builtin_amdgcn_raw_buffer_store_b128(u32x4{g2[0], g2[1], g3[0], g3[1]}, rsW, po + 256, 0, 16);
      if (i == 0) __builtin_amdgcn_raw_buffer_store_b128(u32x4{__float_as_uint(M), T32, __float_as_uint(Ls), T32}, rsW, WS_ML + (((kvh * 4 + g) * 8 + b) * 4 + sp) * 16, 0, 16);
    }
    return;
  }

  const int hw = wave - 4;
  u32x4 xv = *(const GAS u32x4*)(x + hw * DM + lane * 8);
  u64 E = 0;
  if (hw == 0) E = __hip_atomic_load((GAS u64*)(ws + WS_EPOCH), RLX_AGENT);
  {
    const u64 qa = (u64)wq;
    const u32x4 rsQ = {(unsigned)qa, (unsigned)(qa >> 32), 4u << 20, 0x00020000u};
    const unsigned n0 = xc * 512 + r * 16;
    const unsigned vo = (lane >> 1) * 8192 + (lane & 1) * 16;
    dma1(vo, rsQ, (2 * hw) * 262144u + n0 * 2, L_STG + (2 * hw) * 1024);
    dma1(vo, rsQ, (2 * hw + 1) * 262144u + n0 * 2, L_STG + (2 * hw + 1) * 1024);
  }
  if (hw == 3) { if (lane < 32) lds_st(L, lane, 0u); WAIT_LGKM0(); }
  __builtin_amdgcn_s_barrier();
  asm volatile("" : "+v"(xv), "+v"(E) :: "memory");
  {
    float f[8];
#pragma unroll
    for (int k = 0; k < 4; ++k) { f[2 * k] = lo16(xv[k]); f[2 * k + 1] = hi16(xv[k]); }
    float s = ((f[0] + f[1]) + (f[2] + f[3])) + ((f[4] + f[5]) + (f[6] + f[7]));
    s = wave_sum(s);
    const float mean = s * (1.f / DM);
    float q = 0.f;
#pragma unroll
    for (int k = 0; k < 8; ++k) { const float d = f[k] - mean; q = fmaf(d, d, q); }
    q = wave_sum(q);
    const float var = rbf(q * (1.f / DM));
    const float mb = rbf(mean);
    const float sd = rbf(sqrtf(rbf(var + 1e-5f)));
    u32x4 o;
#pragma unroll
    for (int k = 0; k < 4; ++k) o[k] = cvtpk(rbf(f[2 * k] - mb) / sd, rbf(f[2 * k + 1] - mb) / sd);
    *(LAS u32x4*)(L + L_XN + hw * XN_STRIDE + lane * 16) = o;
  }
  WAIT_VM(0);
  if (hw == 0 && lane == 0) { const u64 T = E + 1; lds_st(L, C_TLO, (unsigned)T); lds_st(L, C_THI, (unsigned)(T >> 32)); }
  WAIT_LGKM0();
  if (lane == 0) lds_add(L, C_LN);
  if (hw == 0) lds_wait_ge<1>(L, C_LN, 8); else lds_wait_ge<6>(L, C_LN, 8);
  const unsigned Tlo = lds_ldu(L, C_TLO), Thi = lds_ldu(L, C_THI);
  const unsigned tg_lo = Tlo << 16, tg_hi = (Thi << 16) | (Tlo >> 16);
  const unsigned T24 = Tlo & 0xffffffu;
#define TAG_OK(lo_, hi_) ((((lo_) ^ tg_lo) >> 16) == 0 && (hi_) == tg_hi)
  const int gam = xc + 8 * sp, f0 = gam * 64 + (r & 7) * 8;
  const int hd = 4 * xc + sp, cbk = r & 7;

  if (hw == 0) {
    const f32x4 d = proj16<1>(L, lane);
    const unsigned pk = i < 8 ? cvtpk(d[0], d[1]) : cvtpk(d[2], d[3]);
    __builtin_amdgcn_raw_buffer_store_b128(u32x4{tg_lo | (pk & 0xffffu), tg_hi, tg_lo | (pk >> 16), tg_hi}, rsW,
                                           WS_QG + ((i & 7) * 4096 + xc * 512 + r * 16 + 4 * g + 2 * (i >> 3)) * 8, 0, 16);
    if (lane == 0) lds_st(L, C_STGFREE, 1);
  } else if (hw == 1) {
    lds_wait_ge<8>(L, C_GUST, 4);
    if (gam < 28) {
      const f32x4 d = proj16<0>(L, lane);
      float hv[2];
#pragma unroll
      for (int k = 0; k < 2; ++k) {
        const float own = rbf(g < 2 ? d[k] : d[2 + k]);
        const float oth = dpp<XOR8>(own);
        hv[k] = own > 0.f ? own * oth : 0.f;
      }
      const unsigned pk = cvtpk(hv[0], hv[1]);
      const int b0 = 4 * (g & 1) + (g < 2 ? 0 : 2);
      if (i < 8) __builtin_amdgcn_raw_buffer_store_b128(u32x4{tg_lo | (pk & 0xffffu), tg_hi, tg_lo | (pk >> 16), tg_hi}, rsW, WS_HG + ((f0 + i) * 8 + b0) * 8, 0, 16);
    }
    if (lane == 0) lds_st(L, C_HDONE, 1);
  } else if (hw == 2) {
    const unsigned qo = WS_QG + (b * 4096 + kvh * 512) * 8 + lane * 16;
    bool okk[4] = {false, false, false, false};
    lds_wait_ge<4>(L, C_STGFREE, 1);
#pragma unroll 1
    for (unsigned spins = 0; spins < SPIN_CAP; ++spins) {
      asm volatile("" ::: "memory");
      u32x4 v[4];
#pragma unroll
      for (int k = 0; k < 4; ++k) if (!okk[k]) v[k] = __builtin_amdgcn_raw_buffer_load_b128(rsW, qo + k * 1024, 0, 16);
#pragma unroll
      for (int k = 0; k < 4; ++k) if (!okk[k]) {
        okk[k] = TAG_OK(v[k][0], v[k][1]) && TAG_OK(v[k][2], v[k][3]);
        *(LAS unsigned*)(L + L_Q + (lane + 64 * k) * 4) = (v[k][0] & 0xffffu) | (v[k][2] << 16);
      }
      if (__all(okk[0] && okk[1] && okk[2] && okk[3])) break;
      __builtin_amdgcn_s_sleep(1);
    }
    WAIT_LGKM0();
    if (lane == 0) lds_st(L, C_QREADY, 1);
  } else if (hw >= 4) {
    const int h4 = hw - 4;
    const __amdgpu_buffer_rsrc_t rsD = __builtin_amdgcn_make_buffer_rsrc((void*)wd, 0, FF * DM * 2, 0x00020000);
    const GAS unsigned short* sgu = (const GAS unsigned short*)((lane & 1) ? wu : wg) + (size_t)(128 * h4 + (lane >> 1)) * FF + (gam < 28 ? f0 : 0);
    const GAS unsigned short* swo = (const GAS unsigned short*)wo + (size_t)(128 * hd + 32 * h4 + (lane >> 3)) * DM + cbk * 64 + (lane & 7) * 8;
    u32x4 rg[4], ro[4], rd_[2], xk[8], xvv[8];
    {
      const __amdgpu_buffer_rsrc_t rK = __builtin_amdgcn_make_buffer_rsrc((void*)kc, 0, 128u << 20, 0x00020000), rV = __builtin_amdgcn_make_buffer_rsrc((void*)vc, 0, 128u << 20, 0x00020000);
      const unsigned so = kv_off + 2u * 32768u + h4 * 4096;
      const unsigned vk0 = g * 256 + ((i ^ g) << 4), vv0 = g * 256 + ((i ^ (2 * g)) << 4);
      lds_wait_ge<4>(L, C_RING, 4);
#pragma unroll
      for (int u = 0; u < 8; ++u) { if ((u & 3) == 0) WAIT_VMI(HCAP); xk[u] = __builtin_amdgcn_raw_buffer_load_b128(rK, (vk0 ^ ((u & 3) << 6)) + (u & 3) * 1024 + (u >> 2) * 16384, so, 2); }
#pragma unroll
      for (int u = 0; u < 8; ++u) { if ((u & 3) == 0) WAIT_VMI(HCAP); xvv[u] = __builtin_amdgcn_raw_buffer_load_b128(rV, (vv0 ^ ((u & 1) << 7)) + (u & 3) * 1024 + (u >> 2) * 16384, so, 2); }
      WAIT_VMI(HCAP);
    }
#pragma unroll
    for (int u = 0; u < 4; ++u) ro[u] = __builtin_nontemporal_load((const GAS u32x4*)(swo + (size_t)u * 8 * DM));
#pragma unroll
    for (int u = 0; u < 2; ++u) rd_[u] = __builtin_amdgcn_raw_buffer_load_b128(rsD, (unsigned)((56 * hd + 16 * h4 + 8 * u + (lane >> 3)) * 1024 + cbk * 128 + (lane & 7) * 16), 0, 2);
    lds_wait_ge<2>(L, C_XF + h4, 1);
#pragma unroll
    for (int u = 0; u < 8; ++u) *(LAS u32x4*)(L + L_RING + h4 * 32768 + u * 1024 + lane * 16) = xk[u];
    WAIT_LGKM0();
    if (lane == 0) lds_st(L, C_XR + h4, 1);
    lds_wait_ge<1>(L, C_XF + h4, 2);
#pragma unroll
    for (int u = 0; u < 8; ++u) *(LAS u32x4*)(L + L_RING + h4 * 32768 + 8192 + u * 1024 + lane * 16) = xvv[u];
    WAIT_LGKM0();
    if (lane == 0) lds_st(L, C_XR + h4, 2);
#pragma unroll
    for (int u = 0; u < 4; ++u) rg[u] = *(const GAS u32x4*)(sgu + (size_t)u * 32 * FF);
    lds_wait_ge(L, C_STGFREE, 1);
#pragma unroll
    for (int u = 0; u < 4; ++u) *(LAS u32x4*)(L + L_STG + (4 * h4 + u) * 1024 + lane * 16) = rg[u];
    WAIT_LGKM0();
    if (lane == 0) lds_add(L, C_GUST);
    lds_wait_ge<4>(L, C_HDONE, 1);
#pragma unroll
    for (int u = 0; u < 4; ++u) *(LAS u32x4*)(L + L_STG + (4 * h4 + u) * 1024 + lane * 16) = ro[u];
#pragma unroll
    for (int u = 0; u < 2; ++u) *(LAS u32x4*)(L + L_XN + (2 * h4 + u) * 1024 + lane * 16) = rd_[u];
    WAIT_LGKM0();
    if (lane == 0) lds_add(L, C_WOLD);
  }
  {
    const int fl = 7 * hw + (lane >> 3), bb = lane & 7;
    const unsigned ho = WS_HG + ((56 * hd + 7 * hw) * 8 + (lane < 56 ? lane : 0)) * 8;
    u32x2 hv = {0u, 0u};
    bool ok = false;
    lds_wait_ge<32>(L, C_PROG, GATE_H);
#pragma unroll 1
    for (unsigned spins = 0; spins < SPIN_CAP; ++spins) {
      asm volatile("" ::: "memory");
      if (!ok) { hv = __builtin_amdgcn_raw_buffer_load_b64(rsW, ho, 0, 16); ok = TAG_OK(hv[0], hv[1]); }
      if (__all(ok)) break;
      __builtin_amdgcn_s_sleep(SLP_MID);
    }
    if (lane < 56) *(LAS unsigned short*)(L + L_OA + bb * OA_STRIDE + fl * 2) = (unsigned short)(hv[0] & 0xffffu);
    if (lane < 4) *(LAS unsigned*)(L + L_OA + hw * OA_STRIDE + 112 + lane * 4) = 0u;
    WAIT_LGKM0();
    if (lane == 0) lds_add(L, C_OG);
  }
  const int bf = r >> 2, cb = 4 * xc + (r & 3), n1 = cb * 16;
  const unsigned ba = (g * 8 + (i >> 2)) * 128 + (lane & 3) * 8 + (hw & 3) * 32;
  const unsigned oa = L_OA + (i & 7) * OA_STRIDE + g * 16;
  bf16x8 wf[4];
  float y2a = 0.f, y2b = 0.f;
  float xr[2] = {0.f, 0.f};
  if (hw < 4) {
    lds_wait_ge<8>(L, C_WOLD, 4);
    lds_wait_ge<4>(L, C_OG, 8);
    f32x4 d = {0.f, 0.f, 0.f, 0.f};
#pragma unroll
    for (int s = 0; s < 2; ++s) {
      const bf16x4 a0 = __builtin_amdgcn_ds_read_tr16_b64_v4bf16((LAS bf16x4*)(L + L_XN + ba + s * 4096));
      const bf16x4 a1 = __builtin_amdgcn_ds_read_tr16_b64_v4bf16((LAS bf16x4*)(L + L_XN + ba + s * 4096 + 512));
      d = __builtin_amdgcn_mfma_f32_16x16x32_bf16(__builtin_shufflevector(a0, a1, 0, 1, 2, 3, 4, 5, 6, 7), *(LAS bf16x8*)(L + oa + s * 64), d, 0, 0, 0);
    }
    y2a = i < 8 ? d[0] : d[2]; y2b = i < 8 ? d[1] : d[3];
#pragma unroll
    for (int s = 0; s < 4; ++s) {
      const bf16x4 a0 = __builtin_amdgcn_ds_read_tr16_b64_v4bf16((LAS bf16x4*)(L + L_STG + ba + s * 4096));
      const bf16x4 a1 = __builtin_amdgcn_ds_read_tr16_b64_v4bf16((LAS bf16x4*)(L + L_STG + ba + s * 4096 + 512));
      wf[s] = __builtin_shufflevector(a0, a1, 0, 1, 2, 3, 4, 5, 6, 7);
    }
    asm volatile("s_waitcnt lgkmcnt(0)" : "+v"(wf[0]), "+v"(wf[1]), "+v"(wf[2]), "+v"(wf[3]), "+v"(y2a), "+v"(y2b) :: "memory");
    if (lane == 0) lds_add(L, C_WOREAD);
  } else if (hw == 6) {
    const unsigned xq = *(const GAS unsigned*)(x + bf * DM + n1 + (lane & 7) * 2);
    xr[0] = lo16(xq); xr[1] = hi16(xq);
  } else if (hw == 5 && w == 0) {
#pragma unroll 1
    for (unsigned spins = 0; spins < SPIN_CAP; ++spins) {
      bool ok = true;
#pragma unroll
      for (int k = 0; k < 4; ++k) { const u32x2 v = __builtin_amdgcn_raw_buffer_load_b64(rsW, WS_QG + (lane + 64 * k) * 128, 0, 16); ok &= TAG_OK(v[0], v[1]); }
      if (__all(ok)) break;
      __builtin_amdgcn_s_sleep(SLP_MID);
      asm volatile("" ::: "memory");
    }
    if (lane == 0) __hip_atomic_store((GAS u64*)(ws + WS_EPOCH), ((u64)Thi << 32) | Tlo, RLX_AGENT);
  }
  {
    lds_wait_ge<4>(L, C_WOREAD, 4);
    lds_wait_ge<16>(L, C_ATTEND, 1);
    const unsigned po = WS_PART + ((hw * 32 + hd) * 4 + g) * 512 + i * 16;
    const unsigned mo = WS_ML + ((hd * 8 + hw) * 4 + g) * 16;
    u32x4 pv0 = {0u, 0u, 0u, 0u}, pv1 = {0u, 0u, 0u, 0u}, ml = {0u, 0u, 0u, 0u};
    if (hw == 7) {
      bool ok = lane >= 32;
#pragma unroll 1
      for (unsigned spins = 0; spins < SPIN_CAP; ++spins) {
        asm volatile("" ::: "memory");
        if (!ok) {
          ml = __builtin_amdgcn_raw_buffer_load_b128(rsW, WS_ML + hd * 512 + lane * 16, 0, 16);
          ok = ml[1] == Tlo && ml[3] == Tlo;
        }
        const unsigned miss = ~(unsigned)__ballot(ok);
        if (lane == 0) lds_st(L, C_MLMASK, ~miss);
        const unsigned two = (miss & (miss >> 1) & 0x77777777u) | (miss & (miss >> 2) & 0x33333333u) | (miss & (miss >> 3) & 0x11111111u);
        if (two == 0u) break;
        __builtin_amdgcn_s_sleep(SLP_PART);
      }
      if (lane == 0) lds_st(L, C_MLMASK, 0xffffffffu);
    } else {
      while (__builtin_popcount((lds_ldu(L, C_MLMASK) >> (4 * hw)) & 15u) < 3) __builtin_amdgcn_s_sleep(1);
      asm volatile("" ::: "memory");
    }
    bool ok = false;
#pragma unroll 1
    for (unsigned spins = 0; spins < SPIN_CAP; ++spins) {
      asm volatile("" ::: "memory");
      if (!ok) {
        pv0 = __builtin_amdgcn_raw_buffer_load_b128(rsW, po, 0, 16);
        pv1 = __builtin_amdgcn_raw_buffer_load_b128(rsW, po + 256, 0, 16);
        ml = __builtin_amdgcn_raw_buffer_load_b128(rsW, mo, 0, 16);
        ok = (pv0[1] >> 8) == T24 && (pv0[3] >> 8) == T24 && (pv1[1] >> 8) == T24 && (pv1[3] >> 8) == T24 && ml[1] == Tlo && ml[3] == Tlo;
      }
      if (__all(ok)) break;
      __builtin_amdgcn_s_sleep(SLP_DIR);
    }
    const float mj = __uint_as_float(ml[0]);
    const float M = xrow16_max(mj);
    const float wt = __builtin_amdgcn_exp2f((mj - M) * 1.4426950408889634f);
    const float inv = 1.f / xrow16_sum(wt * __uint_as_float(ml[2]));
    const u32x2 oa0 = {cvtpk(xrow16_sum(wt * unpack_a(pv0[0])) * inv, xrow16_sum(wt * unpack_b(pv0[0], pv0[1])) * inv), cvtpk(xrow16_sum(wt * unpack_a(pv0[2])) * inv, xrow16_sum(wt * unpack_b(pv0[2], pv0[3])) * inv)};
    const u32x2 oa1 = {cvtpk(xrow16_sum(wt * unpack_a(pv1[0])) * inv, xrow16_sum(wt * unpack_b(pv1[0], pv1[1])) * inv), cvtpk(xrow16_sum(wt * unpack_a(pv1[2])) * inv, xrow16_sum(wt * unpack_b(pv1[2], pv1[3])) * inv)};
    if (g == 0) { *(LAS u32x2*)(L + L_OA + hw * OA_STRIDE + i * 8) = oa0; *(LAS u32x2*)(L + L_OA + hw * OA_STRIDE + 128 + i * 8) = oa1; }
    WAIT_LGKM0();
    if (lane == 0) lds_add(L, C_OG);
  }
  if (hw < 4) {
    lds_wait_ge(L, C_OG, 16);
    f32x4 d = {0.f, 0.f, 0.f, 0.f};
#pragma unroll
    for (int s = 0; s < 4; ++s) d = __builtin_amdgcn_mfma_f32_16x16x32_bf16(wf[s], *(LAS bf16x8*)(L + oa + s * 64), d, 0, 0, 0);
    const u32x2 g0 = pack2(i < 8 ? d[0] : d[2], y2a, T24), g1 = pack2(i < 8 ? d[1] : d[3], y2b, T24);
    const unsigned yo = ((((i & 7) * 32 + 4 * cbk + hw) * 32 + hd) * 16 + 4 * g + 2 * (i >> 3)) * 8;
    __builtin_amdgcn_raw_buffer_store_b128(u32x4{g0[0], g0[1], g1[0], g1[1]}, rsW, WS_Y + yo, 0, 16);
  } else if (hw == 6) {
    const unsigned io = WS_Y + (bf * 32 + cb) * 4096 + lane * 16;
    u32x4 y[4];
    bool okk[4] = {false, false, false, false};
#pragma unroll 1
    for (unsigned spins = 0; spins < SPIN_CAP; ++spins) {
      asm volatile("" ::: "memory");
#pragma unroll
      for (int k = 0; k < 4; ++k) if (!okk[k]) y[k] = __builtin_amdgcn_raw_buffer_load_b128(rsW, io + k * 1024, 0, 16);
#pragma unroll
      for (int k = 0; k < 4; ++k) okk[k] = (y[k][1] >> 8) == T24 && (y[k][3] >> 8) == T24;
      if (__all(okk[0] && okk[1] && okk[2] && okk[3])) break;
      __builtin_amdgcn_s_sleep(SLP_Y);
    }
    float o[2];
#pragma unroll
    for (int k = 0; k < 2; ++k) {
      float t = (unpack_a(y[0][2 * k]) + unpack_a(y[1][2 * k])) + (unpack_a(y[2][2 * k]) + unpack_a(y[3][2 * k]));
      float u = (unpack_b(y[0][2 * k], y[0][2 * k + 1]) + unpack_b(y[1][2 * k], y[1][2 * k + 1])) + (unpack_b(y[2][2 * k], y[2][2 * k + 1]) + unpack_b(y[3][2 * k], y[3][2 * k + 1]));
      t += dpp<XOR8>(t); t = rbf(xrow16_sum(t));
      u += dpp<XOR8>(u); u = rbf(xrow16_sum(u));
      const float gate = rbf(u / (1.f + __expf(-u)));
      o[k] = xr[k] + rbf(t * gate);
    }
    if (lane < 8) *(GAS unsigned*)(out + bf * DM + n1 + lane * 2) = cvtpk(o[0], o[1]);
  }
}

extern "C" void kernel_launch(void* const* d_in, const int* in_sizes, int n_in, void* d_out, int out_size, void* d_ws, size_t ws_size, hipStream_t stream) {
  const unsigned short* x = (const unsigned short*)d_in[0];
  const unsigned short* kc = (const unsigned short*)d_in[1];
  const unsigned short* vc = (const unsigned short*)d_in[2];
  const unsigned short* wq = (const unsigned short*)d_in[3];
  const unsigned short* wo = (const unsigned short*)d_in[4];
  const unsigned short* wg = (const unsigned short*)d_in[6];
  const unsigned short* wu = (const unsigned short*)d_in[7];
  const unsigned short* wd = (const unsigned short*)d_in[8];
  (void)hipFuncSetAttribute((const void*)fused_decode, hipFuncAttributeMaxDynamicSharedMemorySize, L_END);
  fused_decode<<<dim3(NWG), dim3(NTHR), L_END, stream>>>(x, kc, vc, wq, wo, wg, wu, wd, (unsigned short*)d_out, (unsigned char*)d_ws);
}
```

```cpp
#include <hip/hip_runtime.h>
#include <hip/hip_bf16.h>
#include <cstdint>

typedef __bf16 bf16x2 __attribute__((ext_vector_type(2)));
typedef __bf16 bf16x4 __attribute__((ext_vector_type(4)));
typedef __bf16 bf16x8 __attribute__((ext_vector_type(8)));
typedef unsigned u32x2 __attribute__((ext_vector_type(2)));
typedef unsigned u32x4 __attribute__((ext_vector_type(4)));
typedef float f32x2 __attribute__((ext_vector_type(2)));
typedef float f32x4 __attribute__((ext_vector_type(4)));
typedef unsigned long long u64;
#define LAS __attribute__((address_space(3)))
#define GAS __attribute__((address_space(1)))
#define RLX_AGENT __ATOMIC_RELAXED, __HIP_MEMORY_SCOPE_AGENT
#define RLX_WG __ATOMIC_RELAXED, __HIP_MEMORY_SCOPE_WORKGROUP

constexpr int NB = 8, DM = 512, HKV = 8, SK = 8192, FF = 1792;
constexpr int NWG = 256, NTHR = 768;
constexpr int NT_TILES = 16;

#define CAPG 4
#define HCAP 2
#define GATE_H 8
#define SLP_PART 4
#define SLP_DIR 2
#define SLP_Y 2
#define SLP_MID 64

constexpr unsigned WS_EPOCH = 0;
constexpr unsigned WS_ML = 8192;
constexpr unsigned WS_QG = 32768;
constexpr unsigned WS_HG = WS_QG + NB * 4096 * 8;
constexpr unsigned WS_PART = WS_HG + NB * FF * 8;
constexpr unsigned WS_Y = WS_PART + NWG * 2048;
constexpr unsigned WS_END = WS_Y + (1u << 20);

constexpr unsigned L_RING = 0;
constexpr unsigned L_STG = 131072;
constexpr unsigned L_XN = L_STG + 16384;
constexpr unsigned XN_STRIDE = 1040;
constexpr unsigned L_Q = L_XN + 8 * XN_STRIDE;
constexpr unsigned L_OA = L_Q + 1024;
constexpr unsigned OA_STRIDE = 272;
constexpr unsigned L_CTL = L_OA + 8 * OA_STRIDE;
constexpr unsigned L_END = L_CTL + 128;
static_assert(L_END <= 163840, "LDS");
enum { C_LN = 0, C_STGFREE, C_QREADY, C_HDONE, C_MRG, C_ATTEND, C_TLO, C_THI, C_WOLD, C_WOREAD, C_OG, C_PROG, C_MLMASK, C_RING, C_GUST,
       C_XF = 20  , C_XR = 24   };

__device__ __forceinline__ unsigned cvtpk(float a, float b) {
  bf16x2 t = __builtin_convertvector(f32x2{a, b}, bf16x2);
  return __builtin_bit_cast(unsigned, t);
}
__device__ __forceinline__ float lo16(unsigned u) { return __builtin_bit_cast(float, u << 16); }
__device__ __forceinline__ float hi16(unsigned u) { return __builtin_bit_cast(float, u & 0xffff0000u); }
__device__ __forceinline__ float rbf(float f) { return lo16(cvtpk(f, f)); }
template <int CTRL> __device__ __forceinline__ float dpp(float x) {
  return __builtin_bit_cast(float, __builtin_amdgcn_mov_dpp(__builtin_bit_cast(int, x), CTRL, 0xf, 0xf, true));
}
constexpr int XOR1 = 0xB1, XOR2 = 0x4E, XOR7 = 0x141, XOR8 = 0x128;
__device__ __forceinline__ float xrow16_max(float x) {
  auto s = __builtin_amdgcn_permlane16_swap(__float_as_uint(x), __float_as_uint(x), false, false);
  x = fmaxf(__uint_as_float(s[0]), __uint_as_float(s[1]));
  auto t = __builtin_amdgcn_permlane32_swap(__float_as_uint(x), __float_as_uint(x), false, false);
  return fmaxf(__uint_as_float(t[0]), __uint_as_float(t[1]));
}
__device__ __forceinline__ float xrow16_sum(float x) {
  auto s = __builtin_amdgcn_permlane16_swap(__float_as_uint(x), __float_as_uint(x), false, false);
  x = __uint_as_float(s[0]) + __uint_as_float(s[1]);
  auto t = __builtin_amdgcn_permlane32_swap(__float_as_uint(x), __float_as_uint(x), false, false);
  return __uint_as_float(t[0]) + __uint_as_float(t[1]);
}
__device__ __forceinline__ float wave_sum(float x) {
  x += dpp<XOR1>(x); x += dpp<XOR2>(x); x += dpp<XOR7>(x); x += dpp<XOR8>(x);
  return xrow16_sum(x);
}

__device__ __forceinline__ void dma4(unsigned v0, unsigned v1, unsigned v2, unsigned v3, u32x4 rs, unsigned so, unsigned ldsaddr) {
  unsigned keep;
  asm volatile(
    "s_mov_b32 %0, m0\n\t"
    "s_mov_b32 m0, %7\n\t"
    "s_nop 0\n\t"
    "buffer_load_dwordx4 %1, %5, %6 offen nt lds\n\t"
    "buffer_load_dwordx4 %2, %5, %6 offen offset:1024 nt lds\n\t"
    "buffer_load_dwordx4 %3, %5, %6 offen offset:2048 nt lds\n\t"
    "buffer_load_dwordx4 %4, %5, %6 offen offset:3072 nt lds\n\t"
    "s_mov_b32 m0, %0"
    : "=&s"(keep) : "v"(v0), "v"(v1), "v"(v2), "v"(v3), "s"(rs), "s"(so), "s"(ldsaddr) : "memory");
}
__device__ __forceinline__ void dma1(unsigned v0, u32x4 rs, unsigned so, unsigned ldsaddr) {
  unsigned keep;
  asm volatile(
    "s_mov_b32 %0, m0\n\t"
    "s_mov_b32 m0, %4\n\t"
    "s_nop 0\n\t"
    "buffer_load_dwordx4 %1, %2, %3 offen lds\n\t"
    "s_mov_b32 m0, %0"
    : "=&s"(keep) : "v"(v0), "s"(rs), "s"(so), "s"(ldsaddr) : "memory");
}
#define WAIT_VM(n) asm volatile("s_waitcnt vmcnt(" #n ")" ::: "memory")
#define WAIT_VMI(n) asm volatile("s_waitcnt vmcnt(%0)" :: "i"(n) : "memory")
#define WAIT_LGKM0() asm volatile("s_waitcnt lgkmcnt(0)" ::: "memory")

__device__ __forceinline__ unsigned lds_ld(LAS unsigned char* L, int idx) { return __hip_atomic_load((LAS unsigned*)(L + L_CTL) + idx, RLX_WG); }
__device__ __forceinline__ unsigned lds_ldu(LAS unsigned char* L, int idx) { return (unsigned)__builtin_amdgcn_readfirstlane(lds_ld(L, idx)); }
__device__ __forceinline__ void lds_st(LAS unsigned char* L, int idx, unsigned v) { __hip_atomic_store((LAS unsigned*)(L + L_CTL) + idx, v, RLX_WG); }
__device__ __forceinline__ unsigned lds_add(LAS unsigned char* L, int idx) { return __hip_atomic_fetch_add((LAS unsigned*)(L + L_CTL) + idx, 1u, RLX_WG); }
template <int SLP = 1>
__device__ __forceinline__ void lds_wait_ge(LAS unsigned char* L, int idx, unsigned want) {
  while (lds_ldu(L, idx) < want) __builtin_amdgcn_s_sleep(SLP);
  asm volatile("" ::: "memory");
}
constexpr unsigned SPIN_CAP = 1u << 15;

__device__ __forceinline__ u32x2 pack2(float a, float b, unsigned tag24) {
  const unsigned ua = (__float_as_uint(a) + 0x800u) >> 12, ub = (__float_as_uint(b) + 0x800u) >> 12;
  return u32x2{ua | (ub << 20), (ub >> 12) | (tag24 << 8)};
}
__device__ __forceinline__ float unpack_a(unsigned lo) { return __uint_as_float(lo << 12); }
__device__ __forceinline__ float unpack_b(unsigned lo, unsigned hi) { return __uint_as_float(((lo >> 20) | (hi << 12)) << 12); }

struct AttnState { f32x4 acc[8]; float m, l; };
struct AttnConst { u32x4 rsK, rsV; unsigned gbase, ring, vk0, vv0, kaddr, vaddr; bf16x8 qf[4]; };

#define DMA_K(c_, so_, ld_) dma4((c_).vk0, (c_).vk0 ^ 64, (c_).vk0 ^ 128, (c_).vk0 ^ 192, (c_).rsK, (so_), (ld_))
#define DMA_V(c_, so_, ld_) dma4((c_).vv0, (c_).vv0 ^ 128, (c_).vv0, (c_).vv0 ^ 128, (c_).rsV, (so_), (ld_))

template <int SLOT>
__device__ __forceinline__ void attn_tile(LAS unsigned char* L, const AttnConst& c, AttnState& st, const int t, const int rem, const int lane, const int aw) {
  constexpr float SCALE = 0.088388347648318440f, LOG2E = 1.4426950408889634f;
  const unsigned so2 = c.gbase + (unsigned)(t + 2) * 32768u, ld2 = c.ring + SLOT * 16384;
  if (rem >= 1) WAIT_VM(24); else WAIT_VM(8);
  if (SLOT == 0 && t == 2) lds_wait_ge(L, C_XR + aw, 1);
  f32x4 s0 = {0.f, 0.f, 0.f, 0.f}, s1 = {0.f, 0.f, 0.f, 0.f};
#pragma unroll
  for (int s = 0; s < 4; ++s) {
    const bf16x8 k0 = *(LAS bf16x8*)(L + (c.kaddr ^ (s << 6)) + SLOT * 16384);
    const bf16x8 k1 = *(LAS bf16x8*)(L + (c.kaddr ^ (s << 6)) + SLOT * 16384 + 4096);
    s0 = __builtin_amdgcn_mfma_f32_16x16x32_bf16(k0, c.qf[s], s0, 0, 0, 0);
    s1 = __builtin_amdgcn_mfma_f32_16x16x32_bf16(k1, c.qf[s], s1, 0, 0, 0);
  }
  WAIT_LGKM0();
  if (SLOT == 0 && t == 0) { if (lane == 0) lds_st(L, C_XF + aw, 1); }
  else if (rem >= 2) { WAIT_VMI(CAPG); DMA_K(c, so2, ld2); WAIT_VMI(CAPG); DMA_K(c, so2 + 16384u, ld2 + 4096); }
  float x[8];
  { unsigned u;
    u = cvtpk(s0[0], s0[1]); u = cvtpk(lo16(u) * SCALE, hi16(u) * SCALE); x[0] = lo16(u); x[1] = hi16(u);
    u = cvtpk(s0[2], s0[3]); u = cvtpk(lo16(u) * SCALE, hi16(u) * SCALE); x[2] = lo16(u); x[3] = hi16(u);
    u = cvtpk(s1[0], s1[1]); u = cvtpk(lo16(u) * SCALE, hi16(u) * SCALE); x[4] = lo16(u); x[5] = hi16(u);
    u = cvtpk(s1[2], s1[3]); u = cvtpk(lo16(u) * SCALE, hi16(u) * SCALE); x[6] = lo16(u); x[7] = hi16(u); }
  float pm = fmaxf(fmaxf(fmaxf(x[0], x[1]), fmaxf(x[2], x[3])), fmaxf(fmaxf(x[4], x[5]), fmaxf(x[6], x[7])));
  pm = xrow16_max(pm);
  if (__any(pm > st.m)) {
    const float mn = fmaxf(st.m, pm);
    const float alpha = __builtin_amdgcn_exp2f((st.m - mn) * LOG2E);
    st.l *= alpha; st.m = mn;
#pragma unroll
    for (int d = 0; d < 8; ++d) st.acc[d] *= alpha;
  }
  const float mb = st.m * LOG2E;
  float p[8];
#pragma unroll
  for (int e = 0; e < 8; ++e) p[e] = __builtin_amdgcn_exp2f(fmaf(x[e], LOG2E, -mb));
  st.l += ((p[0] + p[1]) + (p[2] + p[3])) + ((p[4] + p[5]) + (p[6] + p[7]));
  const u32x4 pw = {cvtpk(p[0], p[1]), cvtpk(p[2], p[3]), cvtpk(p[4], p[5]), cvtpk(p[6], p[7])};
  const bf16x8 pf = __builtin_bit_cast(bf16x8, pw);
  if (rem >= 2) WAIT_VM(24); else if (rem == 1) WAIT_VM(16); else WAIT_VM(0);
  if (SLOT == 0 && t == 2) lds_wait_ge(L, C_XR + aw, 2);
#pragma unroll
  for (int d = 0; d < 8; ++d) {
    const bf16x4 v0 = __builtin_amdgcn_ds_read_tr16_b64_v4bf16((LAS bf16x4*)(L + (c.vaddr ^ (d << 5)) + SLOT * 16384));
    const bf16x4 v1 = __builtin_amdgcn_ds_read_tr16_b64_v4bf16((LAS bf16x4*)(L + (c.vaddr ^ (d << 5)) + SLOT * 16384 + 4096));
    const bf16x8 vf = __builtin_shufflevector(v0, v1, 0, 1, 2, 3, 4, 5, 6, 7);
    st.acc[d] = __builtin_amdgcn_mfma_f32_16x16x32_bf16(vf, pf, st.acc[d], 0, 0, 0);
  }
  WAIT_LGKM0();
  if (SLOT == 0 && t == 0) { if (lane == 0) lds_st(L, C_XF + aw, 2); }
  else if (rem >= 2) { WAIT_VMI(CAPG); DMA_V(c, so2, ld2 + 8192); WAIT_VMI(CAPG); DMA_V(c, so2 + 16384u, ld2 + 8192 + 4096); }
}

template <int TR>
__device__ __forceinline__ f32x4 proj16(LAS unsigned char* L, int lane) {
  const int g = lane >> 4, i = lane & 15;
  unsigned aa = L_XN + (i & 7) * XN_STRIDE + g * 16;
  unsigned ba = L_STG + g * 256 + i * 8;
  f32x4 d0 = {0.f, 0.f, 0.f, 0.f}, d1 = {0.f, 0.f, 0.f, 0.f};
#pragma unroll 2
  for (int s = 0; s < 4; ++s) {
    bf16x8 a[4], bb[4];
#pragma unroll
    for (int k = 0; k < 4; ++k) {
      a[k] = *(LAS bf16x8*)(L + aa + k * 64);
      const bf16x4 b0 = __builtin_amdgcn_ds_read_tr16_b64_v4bf16((LAS bf16x4*)(L + ba + k * 1024));
      const bf16x4 b1 = __builtin_amdgcn_ds_read_tr16_b64_v4bf16((LAS bf16x4*)(L + ba + k * 1024 + 128));
      bb[k] = __builtin_shufflevector(b0, b1, 0, 1, 2, 3, 4, 5, 6, 7);
    }
    if (TR) {
      d0 = __builtin_amdgcn_mfma_f32_16x16x32_bf16(bb[0], a[0], d0, 0, 0, 0); d1 = __builtin_amdgcn_mfma_f32_16x16x32_bf16(bb[1], a[1], d1, 0, 0, 0);
      d0 = __builtin_amdgcn_mfma_f32_16x16x32_bf16(bb[2], a[2], d0, 0, 0, 0); d1 = __builtin_amdgcn_mfma_f32_16x16x32_bf16(bb[3], a[3], d1, 0, 0, 0);
    } else {
      d0 = __builtin_amdgcn_mfma_f32_16x16x32_bf16(a[0], bb[0], d0, 0, 0, 0); d1 = __builtin_amdgcn_mfma_f32_16x16x32_bf16(a[1], bb[1], d1, 0, 0, 0);
      d0 = __builtin_amdgcn_mfma_f32_16x16x32_bf16(a[2], bb[2], d0, 0, 0, 0); d1 = __builtin_amdgcn_mfma_f32_16x16x32_bf16(a[3], bb[3], d1, 0, 0, 0);
    }
    aa += 256; ba += 4096;
  }
  return d0 + d1;
}

__global__ void __launch_bounds__(NTHR, 3)
fused_decode(const unsigned short* __restrict__ x, const unsigned short* __restrict__ kc, const unsigned short* __restrict__ vc,
             const unsigned short* __restrict__ wq, const unsigned short* __restrict__ wo, const unsigned short* __restrict__ wg,
             const unsigned short* __restrict__ wu, const unsigned short* __restrict__ wd, unsigned short* __restrict__ out, unsigned char* ws) {
  extern __shared__ __attribute__((aligned(16))) unsigned char lds[];
  LAS unsigned char* L = (LAS unsigned char*)lds;
  const int w = blockIdx.x;
  const int tid = threadIdx.x, lane = tid & 63, wave = __builtin_amdgcn_readfirstlane(tid >> 6);
  const int g = lane >> 4, i = lane & 15;
  const int xc = w & 7, r = w >> 3;
  const int b = r & 7, sp = r >> 3, kvh = (xc + 2 * sp) & 7;
  const __amdgpu_buffer_rsrc_t rsW = __builtin_amdgcn_make_buffer_rsrc(ws, 0, WS_END, 0x00020000);
  const unsigned kv_off = (unsigned)(((b * HKV + kvh) * SK + sp * 2048) * 256);

  if (wave < 4) {
    const int aw = wave;
    const u64 ka = (u64)kc, va = (u64)vc;
    AttnConst c;
    c.rsK = u32x4{(unsigned)ka, (unsigned)(ka >> 32), 128u << 20, 0x00020000u};
    c.rsV = u32x4{(unsigned)va, (unsigned)(va >> 32), 128u << 20, 0x00020000u};
    c.gbase = kv_off + aw * 4096;
    c.ring = L_RING + aw * 32768;
    c.vk0 = g * 256 + ((i ^ g) << 4);
    c.vv0 = g * 256 + ((i ^ (2 * g)) << 4);
    c.kaddr = c.ring + i * 256 + ((i ^ g) << 4);
    const int q4 = i >> 2, pp = lane & 3;
    c.vaddr = c.ring + 8192 + (4 * g + q4) * 256 + ((8 * (g & 1) + 2 * q4) << 4) + 8 * pp;
    __builtin_amdgcn_s_barrier();
#pragma unroll 1
    for (int u = 0; u < 2; ++u) {
      const unsigned so = c.gbase + u * 32768u, ld = c.ring + u * 16384;
      WAIT_VMI(CAPG); DMA_K(c, so, ld);
      WAIT_VMI(CAPG); DMA_K(c, so + 16384u, ld + 4096);
      WAIT_VMI(CAPG); DMA_V(c, so, ld + 8192);
      if (u == 1 && lane == 0) lds_add(L, C_RING);
      WAIT_VMI(CAPG); DMA_V(c, so + 16384u, ld + 8192 + 4096);
    }
    lds_wait_ge(L, C_QREADY, 1);
#pragma unroll
    for (int s = 0; s < 4; ++s) { u32x4 v = *(LAS u32x4*)(L + L_Q + (i & 3) * 256 + 64 * s + 16 * g); if (i >= 4) v = u32x4{0, 0, 0, 0}; c.qf[s] = __builtin_bit_cast(bf16x8, v); }
    AttnState st;
#pragma unroll
    for (int d = 0; d < 8; ++d) st.acc[d] = f32x4{0.f, 0.f, 0.f, 0.f};
    st.m = -INFINITY; st.l = 0.f;
    const unsigned T32 = lds_ldu(L, C_TLO);
    WAIT_VM(0);
#pragma unroll 1
    for (int t = 0; t < NT_TILES; t += 2) {
      if (aw == 0 && lane == 0) lds_st(L, C_PROG, t);
      if (t == NT_TILES - 2) { if (lane == 0) lds_st(L, C_ATTEND, 1); lds_wait_ge(L, C_WOREAD, 4); }
      attn_tile<0>(L, c, st, t, NT_TILES - 1 - t, lane, aw);
      attn_tile<1>(L, c, st, t + 1, NT_TILES - 2 - t, lane, aw);
    }
    const float lsum = xrow16_sum(st.l);
    LAS float* mr = (LAS float*)(L + L_STG + aw * 2048);
    LAS float* mlr = (LAS float*)(L + L_STG + 8192);
    if (i < 4) {
#pragma unroll
      for (int d = 0; d < 8; ++d) *(LAS f32x4*)(mr + i * 128 + 16 * d + 4 * g) = st.acc[d];
      if (g == 0) { mlr[i * 8 + aw] = st.m; mlr[i * 8 + 4 + aw] = lsum; }
    }
    WAIT_LGKM0();
    unsigned old = 0;
    if (lane == 0) old = lds_add(L, C_MRG);
    old = __builtin_amdgcn_readfirstlane(old);
    asm volatile("" ::: "memory");
    if (old == 3u) {
      const f32x4 mw = *(LAS f32x4*)(mlr + g * 8), lw = *(LAS f32x4*)(mlr + g * 8 + 4);
      f32x4 a0[4], a1[4];
#pragma unroll
      for (int ww = 0; ww < 4; ++ww) { LAS float* rec = (LAS float*)(L + L_STG + ww * 2048) + g * 128 + 4 * i; a0[ww] = *(LAS f32x4*)rec; a1[ww] = *(LAS f32x4*)(rec + 64); }
      const float M = fmaxf(fmaxf(mw[0], mw[1]), fmaxf(mw[2], mw[3]));
      float Ls = 0.f; f32x4 o0 = {0.f, 0.f, 0.f, 0.f}, o1 = {0.f, 0.f, 0.f, 0.f};
#pragma unroll
      for (int ww = 0; ww < 4; ++ww) {
        const float wt = __builtin_amdgcn_exp2f((mw[ww] - M) * 1.4426950408889634f);
        Ls = fmaf(wt, lw[ww], Ls); o0 += wt * a0[ww]; o1 += wt * a1[ww]; }
      const unsigned po = WS_PART + ((b * 32 + kvh * 4 + g) * 4 + sp) * 512 + i * 16;
      const unsigned T24 = T32 & 0xffffffu;
      const u32x2 g0 = pack2(o0[0], o0[1], T24), g1 = pack2(o0[2], o0[3], T24), g2 = pack2(o1[0], o1[1], T24), g3 = pack2(o1[2], o1[3], T24);
      __builtin_amdgcn_raw_buffer_store_b128(u32x4{g0[0], g0[1], g1[0], g1[1]}, rsW, po, 0, 16);
      __builtin_amdgcn_raw_buffer_store_b128(u32x4{g2[0], g2[1], g3[0], g3[1]}, rsW, po + 256, 0, 16);
      if (i == 0) __builtin_amdgcn_raw_buffer_store_b128(u32x4{__float_as_uint(M), T32, __float_as_uint(Ls), T32}, rsW, WS_ML + (((kvh * 4 + g) * 8 + b) * 4 + sp) * 16, 0, 16);
    }
    return;
  }

  const int hw = wave - 4;
  u32x4 xv = *(const GAS u32x4*)(x + hw * DM + lane * 8);
  u64 E = 0;
  if (hw == 0) E = __hip_atomic_load((GAS u64*)(ws + WS_EPOCH), RLX_AGENT);
  {
    const u64 qa = (u64)wq;
    const u32x4 rsQ = {(unsigned)qa, (unsigned)(qa >> 32), 4u << 20, 0x00020000u};
    const unsigned n0 = xc * 512 + r * 16;
    const unsigned vo = (lane >> 1) * 8192 + (lane & 1) * 16;
    dma1(vo, rsQ, (2 * hw) * 262144u + n0 * 2, L_STG + (2 * hw) * 1024);
    dma1(vo, rsQ, (2 * hw + 1) * 262144u + n0 * 2, L_STG + (2 * hw + 1) * 1024);
  }
  if (hw == 3) { if (lane < 32) lds_st(L, lane, 0u); WAIT_LGKM0(); }
  __builtin_amdgcn_s_barrier();
  asm volatile("" : "+v"(xv), "+v"(E) :: "memory");
  {
    float f[8];
#pragma unroll
    for (int k = 0; k < 4; ++k) { f[2 * k] = lo16(xv[k]); f[2 * k + 1] = hi16(xv[k]); }
    float s = ((f[0] + f[1]) + (f[2] + f[3])) + ((f[4] + f[5]) + (f[6] + f[7]));
    s = wave_sum(s);
    const float mean = s * (1.f / DM);
    float q = 0.f;
#pragma unroll
    for (int k = 0; k < 8; ++k) { const float d = f[k] - mean; q = fmaf(d, d, q); }
    q = wave_sum(q);
    const float var = rbf(q * (1.f / DM));
    const float mb = rbf(mean);
    const float sd = rbf(sqrtf(rbf(var + 1e-5f)));
    u32x4 o;
#pragma unroll
    for (int k = 0; k < 4; ++k) o[k] = cvtpk(rbf(f[2 * k] - mb) / sd, rbf(f[2 * k + 1] - mb) / sd);
    *(LAS u32x4*)(L + L_XN + hw * XN_STRIDE + lane * 16) = o;
  }
  WAIT_VM(0);
  if (hw == 0 && lane == 0) { const u64 T = E + 1; lds_st(L, C_TLO, (unsigned)T); lds_st(L, C_THI, (unsigned)(T >> 32)); }
  WAIT_LGKM0();
  if (lane == 0) lds_add(L, C_LN);
  if (hw == 0) lds_wait_ge<1>(L, C_LN, 8); else lds_wait_ge<6>(L, C_LN, 8);
  const unsigned Tlo = lds_ldu(L, C_TLO), Thi = lds_ldu(L, C_THI);
  const unsigned tg_lo = Tlo << 16, tg_hi = (Thi << 16) | (Tlo >> 16);
  const unsigned T24 = Tlo & 0xffffffu;
#define TAG_OK(lo_, hi_) ((((lo_) ^ tg_lo) >> 16) == 0 && (hi_) == tg_hi)
  const int gam = xc + 8 * sp, f0 = gam * 64 + (r & 7) * 8;
  const int hd = 4 * xc + sp, cbk = r & 7;

  if (hw == 0) {
    const f32x4 d = proj16<1>(L, lane);
    const unsigned pk = i < 8 ? cvtpk(d[0], d[1]) : cvtpk(d[2], d[3]);
    __builtin_amdgcn_raw_buffer_store_b128(u32x4{tg_lo | (pk & 0xffffu), tg_hi, tg_lo | (pk >> 16), tg_hi}, rsW,
                                           WS_QG + ((i & 7) * 4096 + xc * 512 + r * 16 + 4 * g + 2 * (i >> 3)) * 8, 0, 16);
    if (lane == 0) lds_st(L, C_STGFREE, 1);
  } else if (hw == 1) {
    lds_wait_ge<8>(L, C_GUST, 4);
    if (gam < 28) {
      const f32x4 d = proj16<0>(L, lane);
      float hv[2];
#pragma unroll
      for (int k = 0; k < 2; ++k) {
        const float own = rbf(g < 2 ? d[k] : d[2 + k]);
        const float oth = dpp<XOR8>(own);
        hv[k] = own > 0.f ? own * oth : 0.f;
      }
      const unsigned pk = cvtpk(hv[0], hv[1]);
      const int b0 = 4 * (g & 1) + (g < 2 ? 0 : 2);
      if (i < 8) __builtin_amdgcn_raw_buffer_store_b128(u32x4{tg_lo | (pk & 0xffffu), tg_hi, tg_lo | (pk >> 16), tg_hi}, rsW, WS_HG + ((f0 + i) * 8 + b0) * 8, 0, 16);
    }
    if (lane == 0) lds_st(L, C_HDONE, 1);
  } else if (hw == 2) {
    const unsigned qo = WS_QG + (b * 4096 + kvh * 512) * 8 + lane * 16;
    bool okk[4] = {false, false, false, false};
    lds_wait_ge<4>(L, C_STGFREE, 1);
#pragma unroll 1
    for (unsigned spins = 0; spins < SPIN_CAP; ++spins) {
      asm volatile("" ::: "memory");
      u32x4 v[4];
#pragma unroll
      for (int k = 0; k < 4; ++k) if (!okk[k]) v[k] = __builtin_amdgcn_raw_buffer_load_b128(rsW, qo + k * 1024, 0, 16);
#pragma unroll
      for (int k = 0; k < 4; ++k) if (!okk[k]) {
        okk[k] = TAG_OK(v[k][0], v[k][1]) && TAG_OK(v[k][2], v[k][3]);
        *(LAS unsigned*)(L + L_Q + (lane + 64 * k) * 4) = (v[k][0] & 0xffffu) | (v[k][2] << 16);
      }
      if (__all(okk[0] && okk[1] && okk[2] && okk[3])) break;
      __builtin_amdgcn_s_sleep(1);
    }
    WAIT_LGKM0();
    if (lane == 0) lds_st(L, C_QREADY, 1);
  } else if (hw >= 4) {
    const int h4 = hw - 4;
    const __amdgpu_buffer_rsrc_t rsD = __builtin_amdgcn_make_buffer_rsrc((void*)wd, 0, FF * DM * 2, 0x00020000);
    const GAS unsigned short* sgu = (const GAS unsigned short*)((lane & 1) ? wu : wg) + (size_t)(128 * h4 + (lane >> 1)) * FF + (gam < 28 ? f0 : 0);
    const GAS unsigned short* swo = (const GAS unsigned short*)wo + (size_t)(128 * hd + 32 * h4 + (lane >> 3)) * DM + cbk * 64 + (lane & 7) * 8;
    u32x4 rg[4], ro[4], rd_[2], xk[8], xvv[8];
    {
      const __amdgpu_buffer_rsrc_t rK = __builtin_amdgcn_make_buffer_rsrc((void*)kc, 0, 128u << 20, 0x00020000), rV = __builtin_amdgcn_make_buffer_rsrc((void*)vc, 0, 128u << 20, 0x00020000);
      const unsigned so = kv_off + 2u * 32768u + h4 * 4096;
      const unsigned vk0 = g * 256 + ((i ^ g) << 4), vv0 = g * 256 + ((i ^ (2 * g)) << 4);
      lds_wait_ge<4>(L, C_RING, 4);
#pragma unroll
      for (int u = 0; u < 8; ++u) { if ((u & 3) == 0) WAIT_VMI(HCAP); xk[u] = __builtin_amdgcn_raw_buffer_load_b128(rK, (vk0 ^ ((u & 3) << 6)) + (u & 3) * 1024 + (u >> 2) * 16384, so, 2); }
#pragma unroll
      for (int u = 0; u < 8; ++u) { if ((u & 3) == 0) WAIT_VMI(HCAP); xvv[u] = __builtin_amdgcn_raw_buffer_load_b128(rV, (vv0 ^ ((u & 1) << 7)) + (u & 3) * 1024 + (u >> 2) * 16384, so, 2); }
      WAIT_VMI(HCAP);
    }
#pragma unroll
    for (int u = 0; u < 4; ++u) ro[u] = __builtin_nontemporal_load((const GAS u32x4*)(swo + (size_t)u * 8 * DM));
#pragma unroll
    for (int u = 0; u < 2; ++u) rd_[u] = __builtin_amdgcn_raw_buffer_load_b128(rsD, (unsigned)((56 * hd + 16 * h4 + 8 * u + (lane >> 3)) * 1024 + cbk * 128 + (lane & 7) * 16), 0, 2);
    lds_wait_ge<2>(L, C_XF + h4, 1);
#pragma unroll
    for (int u = 0; u < 8; ++u) *(LAS u32x4*)(L + L_RING + h4 * 32768 + u * 1024 + lane * 16) = xk[u];
    WAIT_LGKM0();
    if (lane == 0) lds_st(L, C_XR + h4, 1);
    lds_wait_ge<1>(L, C_XF + h4, 2);
#pragma unroll
    for (int u = 0; u < 8; ++u) *(LAS u32x4*)(L + L_RING + h4 * 32768 + 8192 + u * 1024 + lane * 16) = xvv[u];
    WAIT_LGKM0();
    if (lane == 0) lds_st(L, C_XR + h4, 2);
#pragma unroll
    for (int u = 0; u < 4; ++u) rg[u] = *(const GAS u32x4*)(sgu + (size_t)u * 32 * FF);
    lds_wait_ge(L, C_STGFREE, 1);
#pragma unroll
    for (int u = 0; u < 4; ++u) *(LAS u32x4*)(L + L_STG + (4 * h4 + u) * 1024 + lane * 16) = rg[u];
    WAIT_LGKM0();
    if (lane == 0) lds_add(L, C_GUST);
    lds_wait_ge<4>(L, C_HDONE, 1);
#pragma unroll
    for (int u = 0; u < 4; ++u) *(LAS u32x4*)(L + L_STG + (4 * h4 + u) * 1024 + lane * 16) = ro[u];
#pragma unroll
    for (int u = 0; u < 2; ++u) *(LAS u32x4*)(L + L_XN + (2 * h4 + u) * 1024 + lane * 16) = rd_[u];
    WAIT_LGKM0();
    if (lane == 0) lds_add(L, C_WOLD);
  }
  {
    const int fl = 7 * hw + (lane >> 3), bb = lane & 7;
    const unsigned ho = WS_HG + ((56 * hd + 7 * hw) * 8 + (lane < 56 ? lane : 0)) * 8;
    u32x2 hv = {0u, 0u};
    bool ok = false;
    lds_wait_ge<32>(L, C_PROG, GATE_H);
#pragma unroll 1
    for (unsigned spins = 0; spins < SPIN_CAP; ++spins) {
      asm volatile("" ::: "memory");
      if (!ok) { hv = __builtin_amdgcn_raw_buffer_load_b64(rsW, ho, 0, 16); ok = TAG_OK(hv[0], hv[1]); }
      if (__all(ok)) break;
      __builtin_amdgcn_s_sleep(SLP_MID);
    }
    if (lane < 56) *(LAS unsigned short*)(L + L_OA + bb * OA_STRIDE + fl * 2) = (unsigned short)(hv[0] & 0xffffu);
    if (lane < 4) *(LAS unsigned*)(L + L_OA + hw * OA_STRIDE + 112 + lane * 4) = 0u;
    WAIT_LGKM0();
    if (lane == 0) lds_add(L, C_OG);
  }
  const int bf = r >> 2, cb = 4 * xc + (r & 3), n1 = cb * 16;
  const unsigned ba = (g * 8 + (i >> 2)) * 128 + (lane & 3) * 8 + (hw & 3) * 32;
  const unsigned oa = L_OA + (i & 7) * OA_STRIDE + g * 16;
  bf16x8 wf[4];
  float y2a = 0.f, y2b = 0.f;
  float xr[2] = {0.f, 0.f};
  if (hw < 4) {
    lds_wait_ge<8>(L, C_WOLD, 4);
    lds_wait_ge<4>(L, C_OG, 8);
    f32x4 d = {0.f, 0.f, 0.f, 0.f};
#pragma unroll
    for (int s = 0; s < 2; ++s) {
      const bf16x4 a0 = __builtin_amdgcn_ds_read_tr16_b64_v4bf16((LAS bf16x4*)(L + L_XN + ba + s * 4096));
      const bf16x4 a1 = __builtin_amdgcn_ds_read_tr16_b64_v4bf16((LAS bf16x4*)(L + L_XN + ba + s * 4096 + 512));
      d = __builtin_amdgcn_mfma_f32_16x16x32_bf16(__builtin_shufflevector(a0, a1, 0, 1, 2, 3, 4, 5, 6, 7), *(LAS bf16x8*)(L + oa + s * 64), d, 0, 0, 0);
    }
    y2a = i < 8 ? d[0] : d[2]; y2b = i < 8 ? d[1] : d[3];
#pragma unroll
    for (int s = 0; s < 4; ++s) {
      const bf16x4 a0 = __builtin_amdgcn_ds_read_tr16_b64_v4bf16((LAS bf16x4*)(L + L_STG + ba + s * 4096));
      const bf16x4 a1 = __builtin_amdgcn_ds_read_tr16_b64_v4bf16((LAS bf16x4*)(L + L_STG + ba + s * 4096 + 512));
      wf[s] = __builtin_shufflevector(a0, a1, 0, 1, 2, 3, 4, 5, 6, 7);
    }
    asm volatile("s_waitcnt lgkmcnt(0)" : "+v"(wf[0]), "+v"(wf[1]), "+v"(wf[2]), "+v"(wf[3]), "+v"(y2a), "+v"(y2b) :: "memory");
    if (lane == 0) lds_add(L, C_WOREAD);
  } else if (hw == 6) {
    const unsigned xq = *(const GAS unsigned*)(x + bf * DM + n1 + (lane & 7) * 2);
    xr[0] = lo16(xq); xr[1] = hi16(xq);
  } else if (hw == 5 && w == 0) {
#pragma unroll 1
    for (unsigned spins = 0; spins < SPIN_CAP; ++spins) {
      bool ok = true;
#pragma unroll
      for (int k = 0; k < 4; ++k) { const u32x2 v = __builtin_amdgcn_raw_buffer_load_b64(rsW, WS_QG + (lane + 64 * k) * 128, 0, 16); ok &= TAG_OK(v[0], v[1]); }
      if (__all(ok)) break;
      __builtin_amdgcn_s_sleep(SLP_MID);
      asm volatile("" ::: "memory");
    }
    if (lane == 0) __hip_atomic_store((GAS u64*)(ws + WS_EPOCH), ((u64)Thi << 32) | Tlo, RLX_AGENT);
  }
  {
    lds_wait_ge<4>(L, C_WOREAD, 4);
    lds_wait_ge<16>(L, C_ATTEND, 1);
    const unsigned po = WS_PART + ((hw * 32 + hd) * 4 + g) * 512 + i * 16;
    const unsigned mo = WS_ML + ((hd * 8 + hw) * 4 + g) * 16;
    u32x4 pv0 = {0u, 0u, 0u, 0u}, pv1 = {0u, 0u, 0u, 0u}, ml = {0u, 0u, 0u, 0u};
    if (hw == 7) {
      bool ok = lane >= 32;
#pragma unroll 1
      for (unsigned spins = 0; spins < SPIN_CAP; ++spins) {
        asm volatile("" ::: "memory");
        if (!ok) {
          ml = __builtin_amdgcn_raw_buffer_load_b128(rsW, WS_ML + hd * 512 + lane * 16, 0, 16);
          ok = ml[1] == Tlo && ml[3] == Tlo;
        }
        const unsigned miss = ~(unsigned)__ballot(ok);
        if (lane == 0) lds_st(L, C_MLMASK, ~miss);
        const unsigned two = (miss & (miss >> 1) & 0x77777777u) | (miss & (miss >> 2) & 0x33333333u) | (miss & (miss >> 3) & 0x11111111u);
        if (two == 0u) break;
        __builtin_amdgcn_s_sleep(SLP_PART);
      }
      if (lane == 0) lds_st(L, C_MLMASK, 0xffffffffu);
    } else {
      while (__builtin_popcount((lds_ldu(L, C_MLMASK) >> (4 * hw)) & 15u) < 3) __builtin_amdgcn_s_sleep(1);
      asm volatile("" ::: "memory");
    }
    bool ok = false;
#pragma unroll 1
    for (unsigned spins = 0; spins < SPIN_CAP; ++spins) {
      asm volatile("" ::: "memory");
      if (!ok) {
        pv0 = __builtin_amdgcn_raw_buffer_load_b128(rsW, po, 0, 16);
        pv1 = __builtin_amdgcn_raw_buffer_load_b128(rsW, po + 256, 0, 16);
        ml = __builtin_amdgcn_raw_buffer_load_b128(rsW, mo, 0, 16);
        ok = (pv0[1] >> 8) == T24 && (pv0[3] >> 8) == T24 && (pv1[1] >> 8) == T24 && (pv1[3] >> 8) == T24 && ml[1] == Tlo && ml[3] == Tlo;
      }
      if (__all(ok)) break;
      __builtin_amdgcn_s_sleep(SLP_DIR);
    }
    const float mj = __uint_as_float(ml[0]);
    const float M = xrow16_max(mj);
    const float wt = __builtin_amdgcn_exp2f((mj - M) * 1.4426950408889634f);
    const float inv = 1.f / xrow16_sum(wt * __uint_as_float(ml[2]));
    const u32x2 oa0 = {cvtpk(xrow16_sum(wt * unpack_a(pv0[0])) * inv, xrow16_sum(wt * unpack_b(pv0[0], pv0[1])) * inv), cvtpk(xrow16_sum(wt * unpack_a(pv0[2])) * inv, xrow16_sum(wt * unpack_b(pv0[2], pv0[3])) * inv)};
    const u32x2 oa1 = {cvtpk(xrow16_sum(wt * unpack_a(pv1[0])) * inv, xrow16_sum(wt * unpack_b(pv1[0], pv1[1])) * inv), cvtpk(xrow16_sum(wt * unpack_a(pv1[2])) * inv, xrow16_sum(wt * unpack_b(pv1[2], pv1[3])) * inv)};
    if (g == 0) { *(LAS u32x2*)(L + L_OA + hw * OA_STRIDE + i * 8) = oa0; *(LAS u32x2*)(L + L_OA + hw * OA_STRIDE + 128 + i * 8) = oa1; }
    WAIT_LGKM0();
    if (lane == 0) lds_add(L, C_OG);
  }
  if (hw < 4) {
    lds_wait_ge(L, C_OG, 16);
    f32x4 d = {0.f, 0.f, 0.f, 0.f};
#pragma unroll
    for (int s = 0; s < 4; ++s) d = __builtin_amdgcn_mfma_f32_16x16x32_bf16(wf[s], *(LAS bf16x8*)(L + oa + s * 64), d, 0, 0, 0);
    const u32x2 g0 = pack2(i < 8 ? d[0] : d[2], y2a, T24), g1 = pack2(i < 8 ? d[1] : d[3], y2b, T24);
    const unsigned yo = ((((i & 7) * 32 + 4 * cbk + hw) * 32 + hd) * 16 + 4 * g + 2 * (i >> 3)) * 8;
    __builtin_amdgcn_raw_buffer_store_b128(u32x4{g0[0], g0[1], g1[0], g1[1]}, rsW, WS_Y + yo, 0, 16);
  } else if (hw == 6) {
    const unsigned io = WS_Y + (bf * 32 + cb) * 4096 + lane * 16;
    u32x4 y[4];
    bool okk[4] = {false, false, false, false};
#pragma unroll 1
    for (unsigned spins = 0; spins < SPIN_CAP; ++spins) {
      asm volatile("" ::: "memory");
#pragma unroll
      for (int k = 0; k < 4; ++k) if (!okk[k]) y[k] = __builtin_amdgcn_raw_buffer_load_b128(rsW, io + k * 1024, 0, 16);
#pragma unroll
      for (int k = 0; k < 4; ++k) okk[k] = (y[k][1] >> 8) == T24 && (y[k][3] >> 8) == T24;
      if (__all(okk[0] && okk[1] && okk[2] && okk[3])) break;
      __builtin_amdgcn_s_sleep(SLP_Y);
    }
    float o[2];
#pragma unroll
    for (int k = 0; k < 2; ++k) {
      float t = (unpack_a(y[0][2 * k]) + unpack_a(y[1][2 * k])) + (unpack_a(y[2][2 * k]) + unpack_a(y[3][2 * k]));
      float u = (unpack_b(y[0][2 * k], y[0][2 * k + 1]) + unpack_b(y[1][2 * k], y[1][2 * k + 1])) + (unpack_b(y[2][2 * k], y[2][2 * k + 1]) + unpack_b(y[3][2 * k], y[3][2 * k + 1]));
      t += dpp<XOR8>(t); t = rbf(xrow16_sum(t));
      u += dpp<XOR8>(u); u = rbf(xrow16_sum(u));
      const float gate = rbf(u / (1.f + __expf(-u)));
      o[k] = xr[k] + rbf(t * gate);
    }
    if (lane < 8) *(GAS unsigned*)(out + bf * DM + n1 + lane * 2) = cvtpk(o[0], o[1]);
  }
}

extern "C" void kernel_launch(void* const* d_in, const int* in_sizes, int n_in, void* d_out, int out_size, void* d_ws, size_t ws_size, hipStream_t stream) {
  const unsigned short* x = (const unsigned short*)d_in[0];
  const unsigned short* kc = (const unsigned short*)d_in[1];
  const unsigned short* vc = (const unsigned short*)d_in[2];
  const unsigned short* wq = (const unsigned short*)d_in[3];
  const unsigned short* wo = (const unsigned short*)d_in[4];
  const unsigned short* wg = (const unsigned short*)d_in[6];
  const unsigned short* wu = (const unsigned short*)d_in[7];
  const unsigned short* wd = (const unsigned short*)d_in[8];
  (void)hipFuncSetAttribute((const void*)fused_decode, hipFuncAttributeMaxDynamicSharedMemorySize, L_END);
  fused_decode<<<dim3(NWG), dim3(NTHR), L_END, stream>>>(x, kc, vc, wq, wo, wg, wu, wd, (unsigned short*)d_out, (unsigned char*)d_ws);
}
```

```cpp
#include <hip/hip_runtime.h>
#include <hip/hip_bf16.h>
#include <cstdint>

typedef __bf16 bf16x2 __attribute__((ext_vector_type(2)));
typedef __bf16 bf16x4 __attribute__((ext_vector_type(4)));
typedef __bf16 bf16x8 __attribute__((ext_vector_type(8)));
typedef unsigned u32x2 __attribute__((ext_vector_type(2)));
typedef unsigned u32x4 __attribute__((ext_vector_type(4)));
typedef float f32x2 __attribute__((ext_vector_type(2)));
typedef float f32x4 __attribute__((ext_vector_type(4)));
typedef unsigned long long u64;
#define LAS __attribute__((address_space(3)))
#define GAS __attribute__((address_space(1)))
#define RLX_AGENT __ATOMIC_RELAXED, __HIP_MEMORY_SCOPE_AGENT
#define RLX_WG __ATOMIC_RELAXED, __HIP_MEMORY_SCOPE_WORKGROUP

constexpr int NB = 8, DM = 512, HKV = 8, SK = 8192, FF = 1792;
constexpr int NWG = 256, NTHR = 768;
constexpr int NT_TILES = 16;

#define PRET 12
#define CAPG 5
#define HCAP 2
#define GATE_H 8
#define SLP_PART 4
#define SLP_DIR 2
#define SLP_Y 2
#define SLP_MID 64

constexpr unsigned WS_EPOCH = 0;
constexpr unsigned WS_ML = 8192;
constexpr unsigned WS_QG = 32768;
constexpr unsigned WS_HG = WS_QG + NB * 4096 * 8;
constexpr unsigned WS_PART = WS_HG + NB * FF * 8;
constexpr unsigned WS_Y = WS_PART + NWG * 2048;
constexpr unsigned WS_END = WS_Y + (1u << 20);

constexpr unsigned L_RING = 0;
constexpr unsigned L_STG = 131072;
constexpr unsigned L_XN = L_STG + 16384;
constexpr unsigned XN_STRIDE = 1040;
constexpr unsigned L_Q = L_XN + 8 * XN_STRIDE;
constexpr unsigned L_OA = L_Q + 1024;
constexpr unsigned OA_STRIDE = 272;
constexpr unsigned L_CTL = L_OA + 8 * OA_STRIDE;
constexpr unsigned L_END = L_CTL + 128;
static_assert(L_END <= 163840, "LDS");
enum { C_LN = 0, C_STGFREE, C_QREADY, C_HDONE, C_MRG, C_ATTEND, C_TLO, C_THI, C_WOLD, C_WOREAD, C_OG, C_PROG, C_MLMASK, C_RING, C_GUST,
       C_XF = 20  , C_XR = 24   };

__device__ __forceinline__ unsigned cvtpk(float a, float b) {
  bf16x2 t = __builtin_convertvector(f32x2{a, b}, bf16x2);
  return __builtin_bit_cast(unsigned, t);
}
__device__ __forceinline__ float lo16(unsigned u) { return __builtin_bit_cast(float, u << 16); }
__device__ __forceinline__ float hi16(unsigned u) { return __builtin_bit_cast(float, u & 0xffff0000u); }
__device__ __forceinline__ float rbf(float f) { return lo16(cvtpk(f, f)); }
template <int CTRL> __device__ __forceinline__ float dpp(float x) {
  return __builtin_bit_cast(float, __builtin_amdgcn_mov_dpp(__builtin_bit_cast(int, x), CTRL, 0xf, 0xf, true));
}
constexpr int XOR1 = 0xB1, XOR2 = 0x4E, XOR7 = 0x141, XOR8 = 0x128;
__device__ __forceinline__ float xrow16_max(float x) {
  auto s = __builtin_amdgcn_permlane16_swap(__float_as_uint(x), __float_as_uint(x), false, false);
  x = fmaxf(__uint_as_float(s[0]), __uint_as_float(s[1]));
  auto t = __builtin_amdgcn_permlane32_swap(__float_as_uint(x), __float_as_uint(x), false, false);
  return fmaxf(__uint_as_float(t[0]), __uint_as_float(t[1]));
}
__device__ __forceinline__ float xrow16_sum(float x) {
  auto s = __builtin_amdgcn_permlane16_swap(__float_as_uint(x), __float_as_uint(x), false, false);
  x = __uint_as_float(s[0]) + __uint_as_float(s[1]);
  auto t = __builtin_amdgcn_permlane32_swap(__float_as_uint(x), __float_as_uint(x), false, false);
  return __uint_as_float(t[0]) + __uint_as_float(t[1]);
}
__device__ __forceinline__ float wave_sum(float x) {
  x += dpp<XOR1>(x); x += dpp<XOR2>(x); x += dpp<XOR7>(x); x += dpp<XOR8>(x);
  return xrow16_sum(x);
}

__device__ __forceinline__ void dma4(unsigned v0, unsigned v1, unsigned v2, unsigned v3, u32x4 rs, unsigned so, unsigned ldsaddr) {
  unsigned keep;
  asm volatile(
    "s_mov_b32 %0, m0\n\t"
    "s_mov_b32 m0, %7\n\t"
    "s_nop 0\n\t"
    "buffer_load_dwordx4 %1, %5, %6 offen nt lds\n\t"
    "buffer_load_dwordx4 %2, %5, %6 offen offset:1024 nt lds\n\t"
    "buffer_load_dwordx4 %3, %5, %6 offen offset:2048 nt lds\n\t"
    "buffer_load_dwordx4 %4, %5, %6 offen offset:3072 nt lds\n\t"
    "s_mov_b32 m0, %0"
    : "=&s"(keep) : "v"(v0), "v"(v1), "v"(v2), "v"(v3), "s"(rs), "s"(so), "s"(ldsaddr) : "memory");
}
__device__ __forceinline__ void dma1(unsigned v0, u32x4 rs, unsigned so, unsigned ldsaddr) {
  unsigned keep;
  asm volatile(
    "s_mov_b32 %0, m0\n\t"
    "s_mov_b32 m0, %4\n\t"
    "s_nop 0\n\t"
    "buffer_load_dwordx4 %1, %2, %3 offen lds\n\t"
    "s_mov_b32 m0, %0"
    : "=&s"(keep) : "v"(v0), "s"(rs), "s"(so), "s"(ldsaddr) : "memory");
}
#define WAIT_VM(n) asm volatile("s_waitcnt vmcnt(" #n ")" ::: "memory")
#define WAIT_VMI(n) asm volatile("s_waitcnt vmcnt(%0)" :: "i"(n) : "memory")
#define WAIT_LGKM0() asm volatile("s_waitcnt lgkmcnt(0)" ::: "memory")

__device__ __forceinline__ unsigned lds_ld(LAS unsigned char* L, int idx) { return __hip_atomic_load((LAS unsigned*)(L + L_CTL) + idx, RLX_WG); }
__device__ __forceinline__ unsigned lds_ldu(LAS unsigned char* L, int idx) { return (unsigned)__builtin_amdgcn_readfirstlane(lds_ld(L, idx)); }
__device__ __forceinline__ void lds_st(LAS unsigned char* L, int idx, unsigned v) { __hip_atomic_store((LAS unsigned*)(L + L_CTL) + idx, v, RLX_WG); }
__device__ __forceinline__ unsigned lds_add(LAS unsigned char* L, int idx) { return __hip_atomic_fetch_add((LAS unsigned*)(L + L_CTL) + idx, 1u, RLX_WG); }
template <int SLP = 1>
__device__ __forceinline__ void lds_wait_ge(LAS unsigned char* L, int idx, unsigned want) {
  while (lds_ldu(L, idx) < want) __builtin_amdgcn_s_sleep(SLP);
  asm volatile("" ::: "memory");
}
constexpr unsigned SPIN_CAP = 1u << 15;

__device__ __forceinline__ u32x2 pack2(float a, float b, unsigned tag24) {
  const unsigned ua = (__float_as_uint(a) + 0x800u) >> 12, ub = (__float_as_uint(b) + 0x800u) >> 12;
  return u32x2{ua | (ub << 20), (ub >> 12) | (tag24 << 8)};
}
__device__ __forceinline__ float unpack_a(unsigned lo) { return __uint_as_float(lo << 12); }
__device__ __forceinline__ float unpack_b(unsigned lo, unsigned hi) { return __uint_as_float(((lo >> 20) | (hi << 12)) << 12); }

struct AttnState { f32x4 acc[8]; float m, l; };
struct AttnConst { u32x4 rsK, rsV; unsigned gbase, ring, vk0, vv0, kaddr, vaddr; bf16x8 qf[4]; };

#define DMA_K(c_, so_, ld_) dma4((c_).vk0, (c_).vk0 ^ 64, (c_).vk0 ^ 128, (c_).vk0 ^ 192, (c_).rsK, (so_), (ld_))
#define DMA_V(c_, so_, ld_) dma4((c_).vv0, (c_).vv0 ^ 128, (c_).vv0, (c_).vv0 ^ 128, (c_).rsV, (so_), (ld_))

template <int SLOT>
__device__ __forceinline__ void attn_tile(LAS unsigned char* L, const AttnConst& c, AttnState& st, const int t, const int rem, const int lane, const int aw) {
  constexpr float SCALE = 0.088388347648318440f, LOG2E = 1.4426950408889634f;
  const unsigned so2 = c.gbase + (unsigned)(t + 2) * 32768u, ld2 = c.ring + SLOT * 16384;
  if (rem >= 1) WAIT_VM(24); else WAIT_VM(8);
  if (SLOT == 0 && t == 2) lds_wait_ge(L, C_XR + aw, 1);
  f32x4 s0 = {0.f, 0.f, 0.f, 0.f}, s1 = {0.f, 0.f, 0.f, 0.f};
#pragma unroll
  for (int s = 0; s < 4; ++s) {
    const bf16x8 k0 = *(LAS bf16x8*)(L + (c.kaddr ^ (s << 6)) + SLOT * 16384);
    const bf16x8 k1 = *(LAS bf16x8*)(L + (c.kaddr ^ (s << 6)) + SLOT * 16384 + 4096);
    s0 = __builtin_amdgcn_mfma_f32_16x16x32_bf16(k0, c.qf[s], s0, 0, 0, 0);
    s1 = __builtin_amdgcn_mfma_f32_16x16x32_bf16(k1, c.qf[s], s1, 0, 0, 0);
  }
  WAIT_LGKM0();
  if (SLOT == 0 && t == 0) { if (lane == 0) lds_st(L, C_XF + aw, 1); }
  else if (rem >= 2) { WAIT_VMI(CAPG); DMA_K(c, so2, ld2); WAIT_VMI(CAPG); DMA_K(c, so2 + 16384u, ld2 + 4096); }
  float x[8];
  { unsigned u;
    u = cvtpk(s0[0], s0[1]); u = cvtpk(lo16(u) * SCALE, hi16(u) * SCALE); x[0] = lo16(u); x[1] = hi16(u);
    u = cvtpk(s0[2], s0[3]); u = cvtpk(lo16(u) * SCALE, hi16(u) * SCALE); x[2] = lo16(u); x[3] = hi16(u);
    u = cvtpk(s1[0], s1[1]); u = cvtpk(lo16(u) * SCALE, hi16(u) * SCALE); x[4] = lo16(u); x[5] = hi16(u);
    u = cvtpk(s1[2], s1[3]); u = cvtpk(lo16(u) * SCALE, hi16(u) * SCALE); x[6] = lo16(u); x[7] = hi16(u); }
  float pm = fmaxf(fmaxf(fmaxf(x[0], x[1]), fmaxf(x[2], x[3])), fmaxf(fmaxf(x[4], x[5]), fmaxf(x[6], x[7])));
  pm = xrow16_max(pm);
  if (__any(pm > st.m)) {
    const float mn = fmaxf(st.m, pm);
    const float alpha = __builtin_amdgcn_exp2f((st.m - mn) * LOG2E);
    st.l *= alpha; st.m = mn;
#pragma unroll
    for (int d = 0; d < 8; ++d) st.acc[d] *= alpha;
  }
  const float mb = st.m * LOG2E;
  float p[8];
#pragma unroll
  for (int e = 0; e < 8; ++e) p[e] = __builtin_amdgcn_exp2f(fmaf(x[e], LOG2E, -mb));
  st.l += ((p[0] + p[1]) + (p[2] + p[3])) + ((p[4] + p[5]) + (p[6] + p[7]));
  const u32x4 pw = {cvtpk(p[0], p[1]), cvtpk(p[2], p[3]), cvtpk(p[4], p[5]), cvtpk(p[6], p[7])};
  const bf16x8 pf = __builtin_bit_cast(bf16x8, pw);
  if (rem >= 2) WAIT_VM(24); else if (rem == 1) WAIT_VM(16); else WAIT_VM(0);
  if (SLOT == 0 && t == 2) lds_wait_ge(L, C_XR + aw, 2);
#pragma unroll
  for (int d = 0; d < 8; ++d) {
    const bf16x4 v0 = __builtin_amdgcn_ds_read_tr16_b64_v4bf16((LAS bf16x4*)(L + (c.vaddr ^ (d << 5)) + SLOT * 16384));
    const bf16x4 v1 = __builtin_amdgcn_ds_read_tr16_b64_v4bf16((LAS bf16x4*)(L + (c.vaddr ^ (d << 5)) + SLOT * 16384 + 4096));
    const bf16x8 vf = __builtin_shufflevector(v0, v1, 0, 1, 2, 3, 4, 5, 6, 7);
    st.acc[d] = __builtin_amdgcn_mfma_f32_16x16x32_bf16(vf, pf, st.acc[d], 0, 0, 0);
  }
  WAIT_LGKM0();
  if (SLOT == 0 && t == 0) { if (lane == 0) lds_st(L, C_XF + aw, 2); }
  else if (rem >= 2) { WAIT_VMI(CAPG); DMA_V(c, so2, ld2 + 8192); WAIT_VMI(CAPG); DMA_V(c, so2 + 16384u, ld2 + 8192 + 4096); }
}

template <int TR>
__device__ __forceinline__ f32x4 proj16(LAS unsigned char* L, int lane) {
  const int g = lane >> 4, i = lane & 15;
  unsigned aa = L_XN + (i & 7) * XN_STRIDE + g * 16;
  unsigned ba = L_STG + g * 256 + i * 8;
  f32x4 d0 = {0.f, 0.f, 0.f, 0.f}, d1 = {0.f, 0.f, 0.f, 0.f};
#pragma unroll 2
  for (int s = 0; s < 4; ++s) {
    bf16x8 a[4], bb[4];
#pragma unroll
    for (int k = 0; k < 4; ++k) {
      a[k] = *(LAS bf16x8*)(L + aa + k * 64);
      const bf16x4 b0 = __builtin_amdgcn_ds_read_tr16_b64_v4bf16((LAS bf16x4*)(L + ba + k * 1024));
      const bf16x4 b1 = __builtin_amdgcn_ds_read_tr16_b64_v4bf16((LAS bf16x4*)(L + ba + k * 1024 + 128));
      bb[k] = __builtin_shufflevector(b0, b1, 0, 1, 2, 3, 4, 5, 6, 7);
    }
    if (TR) {
      d0 = __builtin_amdgcn_mfma_f32_16x16x32_bf16(bb[0], a[0], d0, 0, 0, 0); d1 = __builtin_amdgcn_mfma_f32_16x16x32_bf16(bb[1], a[1], d1, 0, 0, 0);
      d0 = __builtin_amdgcn_mfma_f32_16x16x32_bf16(bb[2], a[2], d0, 0, 0, 0); d1 = __builtin_amdgcn_mfma_f32_16x16x32_bf16(bb[3], a[3], d1, 0, 0, 0);
    } else {
      d0 = __builtin_amdgcn_mfma_f32_16x16x32_bf16(a[0], bb[0], d0, 0, 0, 0); d1 = __builtin_amdgcn_mfma_f32_16x16x32_bf16(a[1], bb[1], d1, 0, 0, 0);
      d0 = __builtin_amdgcn_mfma_f32_16x16x32_bf16(a[2], bb[2], d0, 0, 0, 0); d1 = __builtin_amdgcn_mfma_f32_16x16x32_bf16(a[3], bb[3], d1, 0, 0, 0);
    }
    aa += 256; ba += 4096;
  }
  return d0 + d1;
}

__global__ void __launch_bounds__(NTHR, 3)
fused_decode(const unsigned short* __restrict__ x, const unsigned short* __restrict__ kc, const unsigned short* __restrict__ vc,
             const unsigned short* __restrict__ wq, const unsigned short* __restrict__ wo, const unsigned short* __restrict__ wg,
             const unsigned short* __restrict__ wu, const unsigned short* __restrict__ wd, unsigned short* __restrict__ out, unsigned char* ws) {
  extern __shared__ __attribute__((aligned(16))) unsigned char lds[];
  LAS unsigned char* L = (LAS unsigned char*)lds;
  const int w = blockIdx.x;
  const int tid = threadIdx.x, lane = tid & 63, wave = __builtin_amdgcn_readfirstlane(tid >> 6);
  const int g = lane >> 4, i = lane & 15;
  const int xc = w & 7, r = w >> 3;
  const int b = r & 7, sp = r >> 3, kvh = (xc + 2 * sp) & 7;
  const __amdgpu_buffer_rsrc_t rsW = __builtin_amdgcn_make_buffer_rsrc(ws, 0, WS_END, 0x00020000);
  const unsigned kv_off = (unsigned)(((b * HKV + kvh) * SK + sp * 2048) * 256);

  if (wave < 4) {
    const int aw = wave;
    const u64 ka = (u64)kc, va = (u64)vc;
    AttnConst c;
    c.rsK = u32x4{(unsigned)ka, (unsigned)(ka >> 32), 128u << 20, 0x00020000u};
    c.rsV = u32x4{(unsigned)va, (unsigned)(va >> 32), 128u << 20, 0x00020000u};
    c.gbase = kv_off + aw * 4096;
    c.ring = L_RING + aw * 32768;
    c.vk0 = g * 256 + ((i ^ g) << 4);
    c.vv0 = g * 256 + ((i ^ (2 * g)) << 4);
    c.kaddr = c.ring + i * 256 + ((i ^ g) << 4);
    const int q4 = i >> 2, pp = lane & 3;
    c.vaddr = c.ring + 8192 + (4 * g + q4) * 256 + ((8 * (g & 1) + 2 * q4) << 4) + 8 * pp;
    __builtin_amdgcn_s_barrier();
#pragma unroll 1
    for (int u = 0; u < 2; ++u) {
      const unsigned so = c.gbase + u * 32768u, ld = c.ring + u * 16384;
      WAIT_VMI(CAPG); DMA_K(c, so, ld);
      WAIT_VMI(CAPG); DMA_K(c, so + 16384u, ld + 4096);
      WAIT_VMI(CAPG); DMA_V(c, so, ld + 8192);
      if (u == 1 && lane == 0) lds_add(L, C_RING);
      WAIT_VMI(CAPG); DMA_V(c, so + 16384u, ld + 8192 + 4096);
    }
    lds_wait_ge(L, C_QREADY, 1);
#pragma unroll
    for (int s = 0; s < 4; ++s) { u32x4 v = *(LAS u32x4*)(L + L_Q + (i & 3) * 256 + 64 * s + 16 * g); if (i >= 4) v = u32x4{0, 0, 0, 0}; c.qf[s] = __builtin_bit_cast(bf16x8, v); }
    AttnState st;
#pragma unroll
    for (int d = 0; d < 8; ++d) st.acc[d] = f32x4{0.f, 0.f, 0.f, 0.f};
    st.m = -INFINITY; st.l = 0.f;
    const unsigned T32 = lds_ldu(L, C_TLO);
    WAIT_VM(0);
#pragma unroll 1
    for (int t = 0; t < NT_TILES; t += 2) {
      if (aw == 0 && lane == 0) lds_st(L, C_PROG, t);
      if (t == NT_TILES - 2) { if (lane == 0) lds_st(L, C_ATTEND, 1); lds_wait_ge(L, C_WOREAD, 4); }
      attn_tile<0>(L, c, st, t, NT_TILES - 1 - t, lane, aw);
      attn_tile<1>(L, c, st, t + 1, NT_TILES - 2 - t, lane, aw);
    }
    const float lsum = xrow16_sum(st.l);
    LAS float* mr = (LAS float*)(L + L_STG + aw * 2048);
    LAS float* mlr = (LAS float*)(L + L_STG + 8192);
    if (i < 4) {
#pragma unroll
      for (int d = 0; d < 8; ++d) *(LAS f32x4*)(mr + i * 128 + 16 * d + 4 * g) = st.acc[d];
      if (g == 0) { mlr[i * 8 + aw] = st.m; mlr[i * 8 + 4 + aw] = lsum; }
    }
    WAIT_LGKM0();
    unsigned old = 0;
    if (lane == 0) old = lds_add(L, C_MRG);
    old = __builtin_amdgcn_readfirstlane(old);
    asm volatile("" ::: "memory");
    if (old == 3u) {
      const f32x4 mw = *(LAS f32x4*)(mlr + g * 8), lw = *(LAS f32x4*)(mlr + g * 8 + 4);
      f32x4 a0[4], a1[4];
#pragma unroll
      for (int ww = 0; ww < 4; ++ww) { LAS float* rec = (LAS float*)(L + L_STG + ww * 2048) + g * 128 + 4 * i; a0[ww] = *(LAS f32x4*)rec; a1[ww] = *(LAS f32x4*)(rec + 64); }
      const float M = fmaxf(fmaxf(mw[0], mw[1]), fmaxf(mw[2], mw[3]));
      float Ls = 0.f; f32x4 o0 = {0.f, 0.f, 0.f, 0.f}, o1 = {0.f, 0.f, 0.f, 0.f};
#pragma unroll
      for (int ww = 0; ww < 4; ++ww) {
        const float wt = __builtin_amdgcn_exp2f((mw[ww] - M) * 1.4426950408889634f);
        Ls = fmaf(wt, lw[ww], Ls); o0 += wt * a0[ww]; o1 += wt * a1[ww]; }
      const unsigned po = WS_PART + ((b * 32 + kvh * 4 + g) * 4 + sp) * 512 + i * 16;
      const unsigned T24 = T32 & 0xffffffu;
      const u32x2 g0 = pack2(o0[0], o0[1], T24), g1 = pack2(o0[2], o0[3], T24), g2 = pack2(o1[0], o1[1], T24), g3 = pack2(o1[2], o1[3], T24);
      __builtin_amdgcn_raw_buffer_store_b128(u32x4{g0[0], g0[1], g1[0], g1[1]}, rsW, po, 0, 16);
      __builtin_amdgcn_raw_buffer_store_b128(u32x4{g2[0], g2[1], g3[0], g3[1]}, rsW, po + 256, 0, 16);
      if (i == 0) __builtin_amdgcn_raw_buffer_store_b128(u32x4{__float_as_uint(M), T32, __float_as_uint(Ls), T32}, rsW, WS_ML + (((kvh * 4 + g) * 8 + b) * 4 + sp) * 16, 0, 16);
    }
    return;
  }

  const int hw = wave - 4;
  u32x4 xv = *(const GAS u32x4*)(x + hw * DM + lane * 8);
  u64 E = 0;
  if (hw == 0) E = __hip_atomic_load((GAS u64*)(ws + WS_EPOCH), RLX_AGENT);
  {
    const u64 qa = (u64)wq;
    const u32x4 rsQ = {(unsigned)qa, (unsigned)(qa >> 32), 4u << 20, 0x00020000u};
    const unsigned n0 = xc * 512 + r * 16;
    const unsigned vo = (lane >> 1) * 8192 + (lane & 1) * 16;
    dma1(vo, rsQ, (2 * hw) * 262144u + n0 * 2, L_STG + (2 * hw) * 1024);
    dma1(vo, rsQ, (2 * hw + 1) * 262144u + n0 * 2, L_STG + (2 * hw + 1) * 1024);
  }
  if (hw == 3) { if (lane < 32) lds_st(L, lane, 0u); WAIT_LGKM0(); }
  __builtin_amdgcn_s_barrier();
  asm volatile("" : "+v"(xv), "+v"(E) :: "memory");
  {
    float f[8];
#pragma unroll
    for (int k = 0; k < 4; ++k) { f[2 * k] = lo16(xv[k]); f[2 * k + 1] = hi16(xv[k]); }
    float s = ((f[0] + f[1]) + (f[2] + f[3])) + ((f[4] + f[5]) + (f[6] + f[7]));
    s = wave_sum(s);
    const float mean = s * (1.f / DM);
    float q = 0.f;
#pragma unroll
    for (int k = 0; k < 8; ++k) { const float d = f[k] - mean; q = fmaf(d, d, q); }
    q = wave_sum(q);
    const float var = rbf(q * (1.f / DM));
    const float mb = rbf(mean);
    const float sd = rbf(sqrtf(rbf(var + 1e-5f)));
    u32x4 o;
#pragma unroll
    for (int k = 0; k < 4; ++k) o[k] = cvtpk(rbf(f[2 * k] - mb) / sd, rbf(f[2 * k + 1] - mb) / sd);
    *(LAS u32x4*)(L + L_XN + hw * XN_STRIDE + lane * 16) = o;
  }
  WAIT_VM(0);
  if (hw == 0 && lane == 0) { const u64 T = E + 1; lds_st(L, C_TLO, (unsigned)T); lds_st(L, C_THI, (unsigned)(T >> 32)); }
  WAIT_LGKM0();
  if (lane == 0) lds_add(L, C_LN);
  if (hw == 0) lds_wait_ge<1>(L, C_LN, 8); else lds_wait_ge<6>(L, C_LN, 8);
  const unsigned Tlo = lds_ldu(L, C_TLO), Thi = lds_ldu(L, C_THI);
  const unsigned tg_lo = Tlo << 16, tg_hi = (Thi << 16) | (Tlo >> 16);
  const unsigned T24 = Tlo & 0xffffffu;
#define TAG_OK(lo_, hi_) ((((lo_) ^ tg_lo) >> 16) == 0 && (hi_) == tg_hi)
  const int gam = xc + 8 * sp, f0 = gam * 64 + (r & 7) * 8;
  const int hd = 4 * xc + sp, cbk = r & 7;

  if (hw == 0) {
    const f32x4 d = proj16<1>(L, lane);
    const unsigned pk = i < 8 ? cvtpk(d[0], d[1]) : cvtpk(d[2], d[3]);
    __builtin_amdgcn_raw_buffer_store_b128(u32x4{tg_lo | (pk & 0xffffu), tg_hi, tg_lo | (pk >> 16), tg_hi}, rsW,
                                           WS_QG + ((i & 7) * 4096 + xc * 512 + r * 16 + 4 * g + 2 * (i >> 3)) * 8, 0, 16);
    if (lane == 0) lds_st(L, C_STGFREE, 1);
  } else if (hw == 1) {
    lds_wait_ge<8>(L, C_GUST, 4);
    if (gam < 28) {
      const f32x4 d = proj16<0>(L, lane);
      float hv[2];
#pragma unroll
      for (int k = 0; k < 2; ++k) {
        const float own = rbf(g < 2 ? d[k] : d[2 + k]);
        const float oth = dpp<XOR8>(own);
        hv[k] = own > 0.f ? own * oth : 0.f;
      }
      const unsigned pk = cvtpk(hv[0], hv[1]);
      const int b0 = 4 * (g & 1) + (g < 2 ? 0 : 2);
      if (i < 8) __builtin_amdgcn_raw_buffer_store_b128(u32x4{tg_lo | (pk & 0xffffu), tg_hi, tg_lo | (pk >> 16), tg_hi}, rsW, WS_HG + ((f0 + i) * 8 + b0) * 8, 0, 16);
    }
    if (lane == 0) lds_st(L, C_HDONE, 1);
  } else if (hw == 2) {
    const unsigned qo = WS_QG + (b * 4096 + kvh * 512) * 8 + lane * 16;
    bool okk[4] = {false, false, false, false};
    lds_wait_ge<4>(L, C_STGFREE, 1);
#pragma unroll 1
    for (unsigned spins = 0; spins < SPIN_CAP; ++spins) {
      asm volatile("" ::: "memory");
      u32x4 v[4];
#pragma unroll
      for (int k = 0; k < 4; ++k) if (!okk[k]) v[k] = __builtin_amdgcn_raw_buffer_load_b128(rsW, qo + k * 1024, 0, 16);
#pragma unroll
      for (int k = 0; k < 4; ++k) if (!okk[k]) {
        okk[k] = TAG_OK(v[k][0], v[k][1]) && TAG_OK(v[k][2], v[k][3]);
        *(LAS unsigned*)(L + L_Q + (lane + 64 * k) * 4) = (v[k][0] & 0xffffu) | (v[k][2] << 16);
      }
      if (__all(okk[0] && okk[1] && okk[2] && okk[3])) break;
      __builtin_amdgcn_s_sleep(1);
    }
    WAIT_LGKM0();
    if (lane == 0) lds_st(L, C_QREADY, 1);
  } else if (hw >= 4) {
    const int h4 = hw - 4;
    const __amdgpu_buffer_rsrc_t rsD = __builtin_amdgcn_make_buffer_rsrc((void*)wd, 0, FF * DM * 2, 0x00020000);
    const GAS unsigned short* sgu = (const GAS unsigned short*)((lane & 1) ? wu : wg) + (size_t)(128 * h4 + (lane >> 1)) * FF + (gam < 28 ? f0 : 0);
    const GAS unsigned short* swo = (const GAS unsigned short*)wo + (size_t)(128 * hd + 32 * h4 + (lane >> 3)) * DM + cbk * 64 + (lane & 7) * 8;
    u32x4 rg[4], ro[4], rd_[2], xk[8], xvv[8];
    {
      const __amdgpu_buffer_rsrc_t rK = __builtin_amdgcn_make_buffer_rsrc((void*)kc, 0, 128u << 20, 0x00020000), rV = __builtin_amdgcn_make_buffer_rsrc((void*)vc, 0, 128u << 20, 0x00020000);
      const unsigned so = kv_off + 2u * 32768u + h4 * 4096;
      const unsigned vk0 = g * 256 + ((i ^ g) << 4), vv0 = g * 256 + ((i ^ (2 * g)) << 4);
      lds_wait_ge<4>(L, C_RING, 4);
#pragma unroll
      for (int u = 0; u < 8; ++u) { if ((u & 3) == 0) WAIT_VMI(HCAP); xk[u] = __builtin_amdgcn_raw_buffer_load_b128(rK, (vk0 ^ ((u & 3) << 6)) + (u & 3) * 1024 + (u >> 2) * 16384, so, 2); }
#pragma unroll
      for (int u = 0; u < 8; ++u) { if ((u & 3) == 0) WAIT_VMI(HCAP); xvv[u] = __builtin_amdgcn_raw_buffer_load_b128(rV, (vv0 ^ ((u & 1) << 7)) + (u & 3) * 1024 + (u >> 2) * 16384, so, 2); }
      WAIT_VMI(HCAP);
    }
#pragma unroll
    for (int u = 0; u < 4; ++u) ro[u] = __builtin_nontemporal_load((const GAS u32x4*)(swo + (size_t)u * 8 * DM));
#pragma unroll
    for (int u = 0; u < 2; ++u) rd_[u] = __builtin_amdgcn_raw_buffer_load_b128(rsD, (unsigned)((56 * hd + 16 * h4 + 8 * u + (lane >> 3)) * 1024 + cbk * 128 + (lane & 7) * 16), 0, 2);
    lds_wait_ge<2>(L, C_XF + h4, 1);
#pragma unroll
    for (int u = 0; u < 8; ++u) *(LAS u32x4*)(L + L_RING + h4 * 32768 + u * 1024 + lane * 16) = xk[u];
    WAIT_LGKM0();
    if (lane == 0) lds_st(L, C_XR + h4, 1);
    lds_wait_ge<1>(L, C_XF + h4, 2);
#pragma unroll
    for (int u = 0; u < 8; ++u) *(LAS u32x4*)(L + L_RING + h4 * 32768 + 8192 + u * 1024 + lane * 16) = xvv[u];
    WAIT_LGKM0();
    if (lane == 0) lds_st(L, C_XR + h4, 2);
#pragma unroll
    for (int u = 0; u < 4; ++u) { rg[u] = *(const GAS u32x4*)(sgu + (size_t)u * 32 * FF); WAIT_VM(0); }
    lds_wait_ge(L, C_STGFREE, 1);
#pragma unroll
    for (int u = 0; u < 4; ++u) *(LAS u32x4*)(L + L_STG + (4 * h4 + u) * 1024 + lane * 16) = rg[u];
    WAIT_LGKM0();
    if (lane == 0) lds_add(L, C_GUST);
    lds_wait_ge<4>(L, C_HDONE, 1);
#pragma unroll
    for (int u = 0; u < 4; ++u) *(LAS u32x4*)(L + L_STG + (4 * h4 + u) * 1024 + lane * 16) = ro[u];
#pragma unroll
    for (int u = 0; u < 2; ++u) *(LAS u32x4*)(L + L_XN + (2 * h4 + u) * 1024 + lane * 16) = rd_[u];
    WAIT_LGKM0();
    if (lane == 0) lds_add(L, C_WOLD);
  }
  {
    const int fl = 7 * hw + (lane >> 3), bb = lane & 7;
    const unsigned ho = WS_HG + ((56 * hd + 7 * hw) * 8 + (lane < 56 ? lane : 0)) * 8;
    u32x2 hv = {0u, 0u};
    bool ok = false;
    lds_wait_ge<32>(L, C_PROG, GATE_H);
#pragma unroll 1
    for (unsigned spins = 0; spins < SPIN_CAP; ++spins) {
      asm volatile("" ::: "memory");
      if (!ok) { hv = __builtin_amdgcn_raw_buffer_load_b64(rsW, ho, 0, 16); ok = TAG_OK(hv[0], hv[1]); }
      if (__all(ok)) break;
      __builtin_amdgcn_s_sleep(SLP_MID);
    }
    if (lane < 56) *(LAS unsigned short*)(L + L_OA + bb * OA_STRIDE + fl * 2) = (unsigned short)(hv[0] & 0xffffu);
    if (lane < 4) *(LAS unsigned*)(L + L_OA + hw * OA_STRIDE + 112 + lane * 4) = 0u;
    WAIT_LGKM0();
    if (lane == 0) lds_add(L, C_OG);
  }
  const int bf = r >> 2, cb = 4 * xc + (r & 3), n1 = cb * 16;
  const unsigned ba = (g * 8 + (i >> 2)) * 128 + (lane & 3) * 8 + (hw & 3) * 32;
  const unsigned oa = L_OA + (i & 7) * OA_STRIDE + g * 16;
  bf16x8 wf[4];
  float y2a = 0.f, y2b = 0.f;
  float xr[2] = {0.f, 0.f};
  if (hw < 4) {
    lds_wait_ge<8>(L, C_WOLD, 4);
    lds_wait_ge<4>(L, C_OG, 8);
    f32x4 d = {0.f, 0.f, 0.f, 0.f};
#pragma unroll
    for (int s = 0; s < 2; ++s) {
      const bf16x4 a0 = __builtin_amdgcn_ds_read_tr16_b64_v4bf16((LAS bf16x4*)(L + L_XN + ba + s * 4096));
      const bf16x4 a1 = __builtin_amdgcn_ds_read_tr16_b64_v4bf16((LAS bf16x4*)(L + L_XN + ba + s * 4096 + 512));
      d = __builtin_amdgcn_mfma_f32_16x16x32_bf16(__builtin_shufflevector(a0, a1, 0, 1, 2, 3, 4, 5, 6, 7), *(LAS bf16x8*)(L + oa + s * 64), d, 0, 0, 0);
    }
    y2a = i < 8 ? d[0] : d[2]; y2b = i < 8 ? d[1] : d[3];
#pragma unroll
    for (int s = 0; s < 4; ++s) {
      const bf16x4 a0 = __builtin_amdgcn_ds_read_tr16_b64_v4bf16((LAS bf16x4*)(L + L_STG + ba + s * 4096));
      const bf16x4 a1 = __builtin_amdgcn_ds_read_tr16_b64_v4bf16((LAS bf16x4*)(L + L_STG + ba + s * 4096 + 512));
      wf[s] = __builtin_shufflevector(a0, a1, 0, 1, 2, 3, 4, 5, 6, 7);
    }
    asm volatile("s_waitcnt lgkmcnt(0)" : "+v"(wf[0]), "+v"(wf[1]), "+v"(wf[2]), "+v"(wf[3]), "+v"(y2a), "+v"(y2b) :: "memory");
    if (lane == 0) lds_add(L, C_WOREAD);
  } else if (hw == 7) {
    lds_wait_ge<32>(L, C_PROG, PRET);
    const unsigned a0 = WS_Y + ((((lane & 7) * 32 + 4 * cbk + ((lane >> 3) & 3)) * 32 + hd) * 16) * 8;
    const unsigned a1 = WS_Y + (bf * 32 + cb) * 4096 + (lane & 31) * 128;
    const unsigned a2 = WS_PART + (((lane & 7) * 32 + hd) * 4 + ((lane >> 3) & 3)) * 512;
    const unsigned a3 = WS_PART + ((b * 32 + kvh * 4 + (lane & 3)) * 4 + sp) * 512;
    const unsigned a4 = WS_ML + (lane < 32 ? hd * 512 + lane * 16 : (((kvh * 4 + (lane & 3)) * 8 + b) * 4 + sp) * 16);
    unsigned t = __builtin_amdgcn_raw_buffer_load_b32(rsW, a0, 0, 16) ^ __builtin_amdgcn_raw_buffer_load_b32(rsW, lane < 32 ? a1 : a3, 0, 16) ^
                 __builtin_amdgcn_raw_buffer_load_b32(rsW, a2, 0, 16) ^ __builtin_amdgcn_raw_buffer_load_b32(rsW, a4, 0, 16);
    t ^= *(const GAS unsigned*)(out + bf * DM + n1);
    asm volatile("" :: "v"(t));
  } else if (hw == 6) {
    const unsigned xq = *(const GAS unsigned*)(x + bf * DM + n1 + (lane & 7) * 2);
    xr[0] = lo16(xq); xr[1] = hi16(xq);
  } else if (hw == 5 && w == 0) {
#pragma unroll 1
    for (unsigned spins = 0; spins < SPIN_CAP; ++spins) {
      bool ok = true;
#pragma unroll
      for (int k = 0; k < 4; ++k) { const u32x2 v = __builtin_amdgcn_raw_buffer_load_b64(rsW, WS_QG + (lane + 64 * k) * 128, 0, 16); ok &= TAG_OK(v[0], v[1]); }
      if (__all(ok)) break;
      __builtin_amdgcn_s_sleep(SLP_MID);
      asm volatile("" ::: "memory");
    }
    if (lane == 0) __hip_atomic_store((GAS u64*)(ws + WS_EPOCH), ((u64)Thi << 32) | Tlo, RLX_AGENT);
  }
  {
    lds_wait_ge<4>(L, C_WOREAD, 4);
    lds_wait_ge<16>(L, C_ATTEND, 1);
    const unsigned po = WS_PART + ((hw * 32 + hd) * 4 + g) * 512 + i * 16;
    const unsigned mo = WS_ML + ((hd * 8 + hw) * 4 + g) * 16;
    u32x4 pv0 = {0u, 0u, 0u, 0u}, pv1 = {0u, 0u, 0u, 0u}, ml = {0u, 0u, 0u, 0u};
    if (hw == 7) {
      bool ok = lane >= 32;
#pragma unroll 1
      for (unsigned spins = 0; spins < SPIN_CAP; ++spins) {
        asm volatile("" ::: "memory");
        if (!ok) {
          ml = __builtin_amdgcn_raw_buffer_load_b128(rsW, WS_ML + hd * 512 + lane * 16, 0, 16);
          ok = ml[1] == Tlo && ml[3] == Tlo;
        }
        const unsigned miss = ~(unsigned)__ballot(ok);
        if (lane == 0) lds_st(L, C_MLMASK, ~miss);
        const unsigned two = (miss & (miss >> 1) & 0x77777777u) | (miss & (miss >> 2) & 0x33333333u) | (miss & (miss >> 3) & 0x11111111u);
        if (two == 0u) break;
        __builtin_amdgcn_s_sleep(SLP_PART);
      }
      if (lane == 0) lds_st(L, C_MLMASK, 0xffffffffu);
    } else {
      while (__builtin_popcount((lds_ldu(L, C_MLMASK) >> (4 * hw)) & 15u) < 3) __builtin_amdgcn_s_sleep(1);
      asm volatile("" ::: "memory");
    }
    bool ok = false;
#pragma unroll 1
    for (unsigned spins = 0; spins < SPIN_CAP; ++spins) {
      asm volatile("" ::: "memory");
      if (!ok) {
        pv0 = __builtin_amdgcn_raw_buffer_load_b128(rsW, po, 0, 16);
        pv1 = __builtin_amdgcn_raw_buffer_load_b128(rsW, po + 256, 0, 16);
        ml = __builtin_amdgcn_raw_buffer_load_b128(rsW, mo, 0, 16);
        ok = (pv0[1] >> 8) == T24 && (pv0[3] >> 8) == T24 && (pv1[1] >> 8) == T24 && (pv1[3] >> 8) == T24 && ml[1] == Tlo && ml[3] == Tlo;
      }
      if (__all(ok)) break;
      __builtin_amdgcn_s_sleep(SLP_DIR);
    }
    const float mj = __uint_as_float(ml[0]);
    const float M = xrow16_max(mj);
    const float wt = __builtin_amdgcn_exp2f((mj - M) * 1.4426950408889634f);
    const float inv = 1.f / xrow16_sum(wt * __uint_as_float(ml[2]));
    const u32x2 oa0 = {cvtpk(xrow16_sum(wt * unpack_a(pv0[0])) * inv, xrow16_sum(wt * unpack_b(pv0[0], pv0[1])) * inv), cvtpk(xrow16_sum(wt * unpack_a(pv0[2])) * inv, xrow16_sum(wt * unpack_b(pv0[2], pv0[3])) * inv)};
    const u32x2 oa1 = {cvtpk(xrow16_sum(wt * unpack_a(pv1[0])) * inv, xrow16_sum(wt * unpack_b(pv1[0], pv1[1])) * inv), cvtpk(xrow16_sum(wt * unpack_a(pv1[2])) * inv, xrow16_sum(wt * unpack_b(pv1[2], pv1[3])) * inv)};
    if (g == 0) { *(LAS u32x2*)(L + L_OA + hw * OA_STRIDE + i * 8) = oa0; *(LAS u32x2*)(L + L_OA + hw * OA_STRIDE + 128 + i * 8) = oa1; }
    WAIT_LGKM0();
    if (lane == 0) lds_add(L, C_OG);
  }
  if (hw < 4) {
    lds_wait_ge(L, C_OG, 16);
    f32x4 d = {0.f, 0.f, 0.f, 0.f};
#pragma unroll
    for (int s = 0; s < 4; ++s) d = __builtin_amdgcn_mfma_f32_16x16x32_bf16(wf[s], *(LAS bf16x8*)(L + oa + s * 64), d, 0, 0, 0);
    const u32x2 g0 = pack2(i < 8 ? d[0] : d[2], y2a, T24), g1 = pack2(i < 8 ? d[1] : d[3], y2b, T24);
    const unsigned yo = ((((i & 7) * 32 + 4 * cbk + hw) * 32 + hd) * 16 + 4 * g + 2 * (i >> 3)) * 8;
    __builtin_amdgcn_raw_buffer_store_b128(u32x4{g0[0], g0[1], g1[0], g1[1]}, rsW, WS_Y + yo, 0, 16);
  } else if (hw == 6) {
    const unsigned io = WS_Y + (bf * 32 + cb) * 4096 + lane * 16;
    u32x4 y[4];
    bool okk[4] = {false, false, false, false};
#pragma unroll 1
    for (unsigned spins = 0; spins < SPIN_CAP; ++spins) {
      asm volatile("" ::: "memory");
#pragma unroll
      for (int k = 0; k < 4; ++k) if (!okk[k]) y[k] = __builtin_amdgcn_raw_buffer_load_b128(rsW, io + k * 1024, 0, 16);
#pragma unroll
      for (int k = 0; k < 4; ++k) okk[k] = (y[k][1] >> 8) == T24 && (y[k][3] >> 8) == T24;
      if (__all(okk[0] && okk[1] && okk[2] && okk[3])) break;
      __builtin_amdgcn_s_sleep(SLP_Y);
    }
    float o[2];
#pragma unroll
    for (int k = 0; k < 2; ++k) {
      float t = (unpack_a(y[0][2 * k]) + unpack_a(y[1][2 * k])) + (unpack_a(y[2][2 * k]) + unpack_a(y[3][2 * k]));
      float u = (unpack_b(y[0][2 * k], y[0][2 * k + 1]) + unpack_b(y[1][2 * k], y[1][2 * k + 1])) + (unpack_b(y[2][2 * k], y[2][2 * k + 1]) + unpack_b(y[3][2 * k], y[3][2 * k + 1]));
      t += dpp<XOR8>(t); t = rbf(xrow16_sum(t));
      u += dpp<XOR8>(u); u = rbf(xrow16_sum(u));
      const float gate = rbf(u / (1.f + __expf(-u)));
      o[k] = xr[k] + rbf(t * gate);
    }
    if (lane < 8) *(GAS unsigned*)(out + bf * DM + n1 + lane * 2) = cvtpk(o[0], o[1]);
  }
}

extern "C" void kernel_launch(void* const* d_in, const int* in_sizes, int n_in, void* d_out, int out_size, void* d_ws, size_t ws_size, hipStream_t stream) {
  const unsigned short* x = (const unsigned short*)d_in[0];
  const unsigned short* kc = (const unsigned short*)d_in[1];
  const unsigned short* vc = (const unsigned short*)d_in[2];
  const unsigned short* wq = (const unsigned short*)d_in[3];
  const unsigned short* wo = (const unsigned short*)d_in[4];
  const unsigned short* wg = (const unsigned short*)d_in[6];
  const unsigned short* wu = (const unsigned short*)d_in[7];
  const unsigned short* wd = (const unsigned short*)d_in[8];
  (void)hipFuncSetAttribute((const void*)fused_decode, hipFuncAttributeMaxDynamicSharedMemorySize, L_END);
  fused_decode<<<dim3(NWG), dim3(NTHR), L_END, stream>>>(x, kc, vc, wq, wo, wg, wu, wd, (unsigned short*)d_out, (unsigned char*)d_ws);
}
```

```cpp
#include <hip/hip_runtime.h>
#include <hip/hip_bf16.h>
#include <cstdint>

typedef __bf16 bf16x2 __attribute__((ext_vector_type(2)));
typedef __bf16 bf16x4 __attribute__((ext_vector_type(4)));
typedef __bf16 bf16x8 __attribute__((ext_vector_type(8)));
typedef unsigned u32x2 __attribute__((ext_vector_type(2)));
typedef unsigned u32x4 __attribute__((ext_vector_type(4)));
typedef float f32x2 __attribute__((ext_vector_type(2)));
typedef float f32x4 __attribute__((ext_vector_type(4)));
typedef unsigned long long u64;
#define LAS __attribute__((address_space(3)))
#define GAS __attribute__((address_space(1)))
#define RLX_AGENT __ATOMIC_RELAXED, __HIP_MEMORY_SCOPE_AGENT
#define RLX_WG __ATOMIC_RELAXED, __HIP_MEMORY_SCOPE_WORKGROUP

constexpr int NB = 8, DM = 512, HKV = 8, SK = 8192, FF = 1792;
constexpr int NWG = 256, NTHR = 768;
constexpr int NT_TILES = 16;

#define CAPG 5
#define HCAP 2
#define GATE_H 8
#define SLP_PART 4
#define SLP_DIR 2
#define SLP_Y 2
#define SLP_MID 64

constexpr unsigned WS_EPOCH = 0;
constexpr unsigned WS_ML = 8192;
constexpr unsigned WS_QG = 32768;
constexpr unsigned WS_HG = WS_QG + NB * 4096 * 8;
constexpr unsigned WS_PART = WS_HG + NB * FF * 8;
constexpr unsigned WS_Y = WS_PART + NWG * 2048;
constexpr unsigned WS_END = WS_Y + (1u << 20);

constexpr unsigned L_RING = 0;
constexpr unsigned L_STG = 131072;
constexpr unsigned L_XN = L_STG + 16384;
constexpr unsigned XN_STRIDE = 1040;
constexpr unsigned L_Q = L_XN + 8 * XN_STRIDE;
constexpr unsigned L_OA = L_Q + 1024;
constexpr unsigned OA_STRIDE = 272;
constexpr unsigned L_CTL = L_OA + 8 * OA_STRIDE;
constexpr unsigned L_END = L_CTL + 128;
static_assert(L_END <= 163840, "LDS");
enum { C_LN = 0, C_STGFREE, C_QREADY, C_HDONE, C_MRG, C_ATTEND, C_TLO, C_THI, C_WOLD, C_WOREAD, C_OG, C_PROG, C_MLMASK, C_RING, C_GUST,
       C_XF = 20  , C_XR = 24   };

__device__ __forceinline__ unsigned cvtpk(float a, float b) {
  bf16x2 t = __builtin_convertvector(f32x2{a, b}, bf16x2);
  return __builtin_bit_cast(unsigned, t);
}
__device__ __forceinline__ float lo16(unsigned u) { return __builtin_bit_cast(float, u << 16); }
__device__ __forceinline__ float hi16(unsigned u) { return __builtin_bit_cast(float, u & 0xffff0000u); }
__device__ __forceinline__ float rbf(float f) { return lo16(cvtpk(f, f)); }
template <int CTRL> __device__ __forceinline__ float dpp(float x) {
  return __builtin_bit_cast(float, __builtin_amdgcn_mov_dpp(__builtin_bit_cast(int, x), CTRL, 0xf, 0xf, true));
}
constexpr int XOR1 = 0xB1, XOR2 = 0x4E, XOR7 = 0x141, XOR8 = 0x128;
__device__ __forceinline__ float xrow16_max(float x) {
  auto s = __builtin_amdgcn_permlane16_swap(__float_as_uint(x), __float_as_uint(x), false, false);
  x = fmaxf(__uint_as_float(s[0]), __uint_as_float(s[1]));
  auto t = __builtin_amdgcn_permlane32_swap(__float_as_uint(x), __float_as_uint(x), false, false);
  return fmaxf(__uint_as_float(t[0]), __uint_as_float(t[1]));
}
__device__ __forceinline__ float xrow16_sum(float x) {
  auto s = __builtin_amdgcn_permlane16_swap(__float_as_uint(x), __float_as_uint(x), false, false);
  x = __uint_as_float(s[0]) + __uint_as_float(s[1]);
  auto t = __builtin_amdgcn_permlane32_swap(__float_as_uint(x), __float_as_uint(x), false, false);
  return __uint_as_float(t[0]) + __uint_as_float(t[1]);
}
__device__ __forceinline__ float wave_sum(float x) {
  x += dpp<XOR1>(x); x += dpp<XOR2>(x); x += dpp<XOR7>(x); x += dpp<XOR8>(x);
  return xrow16_sum(x);
}

__device__ __forceinline__ void dma4(unsigned v0, unsigned v1, unsigned v2, unsigned v3, u32x4 rs, unsigned so, unsigned ldsaddr) {
  unsigned keep;
  asm volatile(
    "s_mov_b32 %0, m0\n\t"
    "s_mov_b32 m0, %7\n\t"
    "s_nop 0\n\t"
    "buffer_load_dwordx4 %1, %5, %6 offen nt lds\n\t"
    "buffer_load_dwordx4 %2, %5, %6 offen offset:1024 nt lds\n\t"
    "buffer_load_dwordx4 %3, %5, %6 offen offset:2048 nt lds\n\t"
    "buffer_load_dwordx4 %4, %5, %6 offen offset:3072 nt lds\n\t"
    "s_mov_b32 m0, %0"
    : "=&s"(keep) : "v"(v0), "v"(v1), "v"(v2), "v"(v3), "s"(rs), "s"(so), "s"(ldsaddr) : "memory");
}
__device__ __forceinline__ void dma1(unsigned v0, u32x4 rs, unsigned so, unsigned ldsaddr) {
  unsigned keep;
  asm volatile(
    "s_mov_b32 %0, m0\n\t"
    "s_mov_b32 m0, %4\n\t"
    "s_nop 0\n\t"
    "buffer_load_dwordx4 %1, %2, %3 offen lds\n\t"
    "s_mov_b32 m0, %0"
    : "=&s"(keep) : "v"(v0), "s"(rs), "s"(so), "s"(ldsaddr) : "memory");
}
#define WAIT_VM(n) asm volatile("s_waitcnt vmcnt(" #n ")" ::: "memory")
#define WAIT_VMI(n) asm volatile("s_waitcnt vmcnt(%0)" :: "i"(n) : "memory")
#define WAIT_LGKM0() asm volatile("s_waitcnt lgkmcnt(0)" ::: "memory")

__device__ __forceinline__ unsigned lds_ld(LAS unsigned char* L, int idx) { return __hip_atomic_load((LAS unsigned*)(L + L_CTL) + idx, RLX_WG); }
__device__ __forceinline__ unsigned lds_ldu(LAS unsigned char* L, int idx) { return (unsigned)__builtin_amdgcn_readfirstlane(lds_ld(L, idx)); }
__device__ __forceinline__ void lds_st(LAS unsigned char* L, int idx, unsigned v) { __hip_atomic_store((LAS unsigned*)(L + L_CTL) + idx, v, RLX_WG); }
__device__ __forceinline__ unsigned lds_add(LAS unsigned char* L, int idx) { return __hip_atomic_fetch_add((LAS unsigned*)(L + L_CTL) + idx, 1u, RLX_WG); }
template <int SLP = 1>
__device__ __forceinline__ void lds_wait_ge(LAS unsigned char* L, int idx, unsigned want) {
  while (lds_ldu(L, idx) < want) __builtin_amdgcn_s_sleep(SLP);
  asm volatile("" ::: "memory");
}
constexpr unsigned SPIN_CAP = 1u << 15;
__device__ __forceinline__ int opaque(int v) { asm volatile("" : "+s"(v)); return v; }

__device__ __forceinline__ u32x2 pack2(float a, float b, unsigned tag24) {
  const unsigned ua = (__float_as_uint(a) + 0x800u) >> 12, ub = (__float_as_uint(b) + 0x800u) >> 12;
  return u32x2{ua | (ub << 20), (ub >> 12) | (tag24 << 8)};
}
__device__ __forceinline__ float unpack_a(unsigned lo) { return __uint_as_float(lo << 12); }
__device__ __forceinline__ float unpack_b(unsigned lo, unsigned hi) { return __uint_as_float(((lo >> 20) | (hi << 12)) << 12); }

struct AttnState { f32x4 acc[8]; float m, l; };
struct AttnConst { u32x4 rsK, rsV; unsigned gbase, ring, vk0, vv0, kaddr, vaddr; bf16x8 qf[4]; };

#define DMA_K(c_, so_, ld_) dma4((c_).vk0, (c_).vk0 ^ 64, (c_).vk0 ^ 128, (c_).vk0 ^ 192, (c_).rsK, (so_), (ld_))
#define DMA_V(c_, so_, ld_) dma4((c_).vv0, (c_).vv0 ^ 128, (c_).vv0, (c_).vv0 ^ 128, (c_).rsV, (so_), (ld_))

template <int SLOT>
__device__ __forceinline__ void attn_tile(LAS unsigned char* L, const AttnConst& c, AttnState& st, const int t, const int rem, const int lane, const int aw) {
  constexpr float SCALE = 0.088388347648318440f, LOG2E = 1.4426950408889634f;
  const unsigned so2 = c.gbase + (unsigned)(t + 2) * 32768u, ld2 = c.ring + SLOT * 16384;
  if (rem >= 1) WAIT_VM(24); else WAIT_VM(8);
  if (SLOT == 0 && t == 2) lds_wait_ge(L, C_XR + aw, 1);
  f32x4 s0 = {0.f, 0.f, 0.f, 0.f}, s1 = {0.f, 0.f, 0.f, 0.f};
#pragma unroll
  for (int s = 0; s < 4; ++s) {
    const bf16x8 k0 = *(LAS bf16x8*)(L + (c.kaddr ^ (s << 6)) + SLOT * 16384);
    const bf16x8 k1 = *(LAS bf16x8*)(L + (c.kaddr ^ (s << 6)) + SLOT * 16384 + 4096);
    s0 = __builtin_amdgcn_mfma_f32_16x16x32_bf16(k0, c.qf[s], s0, 0, 0, 0);
    s1 = __builtin_amdgcn_mfma_f32_16x16x32_bf16(k1, c.qf[s], s1, 0, 0, 0);
  }
  WAIT_LGKM0();
  if (SLOT == 0 && t == 0) { if (lane == 0) lds_st(L, C_XF + aw, 1); }
  else if (rem >= 2) { WAIT_VMI(CAPG); DMA_K(c, so2, ld2); WAIT_VMI(CAPG); DMA_K(c, so2 + 16384u, ld2 + 4096); }
  float x[8];
  { unsigned u;
    u = cvtpk(s0[0], s0[1]); u = cvtpk(lo16(u) * SCALE, hi16(u) * SCALE); x[0] = lo16(u); x[1] = hi16(u);
    u = cvtpk(s0[2], s0[3]); u = cvtpk(lo16(u) * SCALE, hi16(u) * SCALE); x[2] = lo16(u); x[3] = hi16(u);
    u = cvtpk(s1[0], s1[1]); u = cvtpk(lo16(u) * SCALE, hi16(u) * SCALE); x[4] = lo16(u); x[5] = hi16(u);
    u = cvtpk(s1[2], s1[3]); u = cvtpk(lo16(u) * SCALE, hi16(u) * SCALE); x[6] = lo16(u); x[7] = hi16(u); }
  float pm = fmaxf(fmaxf(fmaxf(x[0], x[1]), fmaxf(x[2], x[3])), fmaxf(fmaxf(x[4], x[5]), fmaxf(x[6], x[7])));
  pm = xrow16_max(pm);
  if (__any(pm > st.m)) {
    const float mn = fmaxf(st.m, pm);
    const float alpha = __builtin_amdgcn_exp2f((st.m - mn) * LOG2E);
    st.l *= alpha; st.m = mn;
#pragma unroll
    for (int d = 0; d < 8; ++d) st.acc[d] *= alpha;
  }
  const float mb = st.m * LOG2E;
  float p[8];
#pragma unroll
  for (int e = 0; e < 8; ++e) p[e] = __builtin_amdgcn_exp2f(fmaf(x[e], LOG2E, -mb));
  st.l += ((p[0] + p[1]) + (p[2] + p[3])) + ((p[4] + p[5]) + (p[6] + p[7]));
  const u32x4 pw = {cvtpk(p[0], p[1]), cvtpk(p[2], p[3]), cvtpk(p[4], p[5]), cvtpk(p[6], p[7])};
  const bf16x8 pf = __builtin_bit_cast(bf16x8, pw);
  if (rem >= 2) WAIT_VM(24); else if (rem == 1) WAIT_VM(16); else WAIT_VM(0);
  if (SLOT == 0 && t == 2) lds_wait_ge(L, C_XR + aw, 2);
#pragma unroll
  for (int d = 0; d < 8; ++d) {
    const bf16x4 v0 = __builtin_amdgcn_ds_read_tr16_b64_v4bf16((LAS bf16x4*)(L + (c.vaddr ^ (d << 5)) + SLOT * 16384));
    const bf16x4 v1 = __builtin_amdgcn_ds_read_tr16_b64_v4bf16((LAS bf16x4*)(L + (c.vaddr ^ (d << 5)) + SLOT * 16384 + 4096));
    const bf16x8 vf = __builtin_shufflevector(v0, v1, 0, 1, 2, 3, 4, 5, 6, 7);
    st.acc[d] = __builtin_amdgcn_mfma_f32_16x16x32_bf16(vf, pf, st.acc[d], 0, 0, 0);
  }
  WAIT_LGKM0();
  if (SLOT == 0 && t == 0) { if (lane == 0) lds_st(L, C_XF + aw, 2); }
  else if (rem >= 2) { WAIT_VMI(CAPG); DMA_V(c, so2, ld2 + 8192); WAIT_VMI(CAPG); DMA_V(c, so2 + 16384u, ld2 + 8192 + 4096); }
}

template <int TR>
__device__ __forceinline__ f32x4 proj16(LAS unsigned char* L, int lane) {
  const int g = lane >> 4, i = lane & 15;
  unsigned aa = L_XN + (i & 7) * XN_STRIDE + g * 16;
  unsigned ba = L_STG + g * 256 + i * 8;
  f32x4 d0 = {0.f, 0.f, 0.f, 0.f}, d1 = {0.f, 0.f, 0.f, 0.f};
#pragma unroll 2
  for (int s = 0; s < 4; ++s) {
    bf16x8 a[4], bb[4];
#pragma unroll
    for (int k = 0; k < 4; ++k) {
      a[k] = *(LAS bf16x8*)(L + aa + k * 64);
      const bf16x4 b0 = __builtin_amdgcn_ds_read_tr16_b64_v4bf16((LAS bf16x4*)(L + ba + k * 1024));
      const bf16x4 b1 = __builtin_amdgcn_ds_read_tr16_b64_v4bf16((LAS bf16x4*)(L + ba + k * 1024 + 128));
      bb[k] = __builtin_shufflevector(b0, b1, 0, 1, 2, 3, 4, 5, 6, 7);
    }
    if (TR) {
      d0 = __builtin_amdgcn_mfma_f32_16x16x32_bf16(bb[0], a[0], d0, 0, 0, 0); d1 = __builtin_amdgcn_mfma_f32_16x16x32_bf16(bb[1], a[1], d1, 0, 0, 0);
      d0 = __builtin_amdgcn_mfma_f32_16x16x32_bf16(bb[2], a[2], d0, 0, 0, 0); d1 = __builtin_amdgcn_mfma_f32_16x16x32_bf16(bb[3], a[3], d1, 0, 0, 0);
    } else {
      d0 = __builtin_amdgcn_mfma_f32_16x16x32_bf16(a[0], bb[0], d0, 0, 0, 0); d1 = __builtin_amdgcn_mfma_f32_16x16x32_bf16(a[1], bb[1], d1, 0, 0, 0);
      d0 = __builtin_amdgcn_mfma_f32_16x16x32_bf16(a[2], bb[2], d0, 0, 0, 0); d1 = __builtin_amdgcn_mfma_f32_16x16x32_bf16(a[3], bb[3], d1, 0, 0, 0);
    }
    aa += 256; ba += 4096;
  }
  return d0 + d1;
}

__global__ void __launch_bounds__(NTHR, 3)
fused_decode(const unsigned short* __restrict__ x, const unsigned short* __restrict__ kc, const unsigned short* __restrict__ vc,
             const unsigned short* __restrict__ wq, const unsigned short* __restrict__ wo, const unsigned short* __restrict__ wg,
             const unsigned short* __restrict__ wu, const unsigned short* __restrict__ wd, unsigned short* __restrict__ out, unsigned char* ws) {
  extern __shared__ __attribute__((aligned(16))) unsigned char lds[];
  LAS unsigned char* L = (LAS unsigned char*)lds;
  const int w = blockIdx.x;
  const int tid = threadIdx.x, lane = tid & 63, wave = __builtin_amdgcn_readfirstlane(tid >> 6);
  const int g = lane >> 4, i = lane & 15;
  const int xc = w & 7, r = w >> 3;
  const int b = r & 7, sp = r >> 3, kvh = (xc + 2 * sp) & 7;
  const __amdgpu_buffer_rsrc_t rsW = __builtin_amdgcn_make_buffer_rsrc(ws, 0, WS_END, 0x00020000);
  const unsigned kv_off = (unsigned)(((b * HKV + kvh) * SK + sp * 2048) * 256);

  if (wave < 4) {
    const int aw = wave;
    const u64 ka = (u64)kc, va = (u64)vc;
    AttnConst c;
    c.rsK = u32x4{(unsigned)ka, (unsigned)(ka >> 32), 128u << 20, 0x00020000u};
    c.rsV = u32x4{(unsigned)va, (unsigned)(va >> 32), 128u << 20, 0x00020000u};
    c.gbase = kv_off + aw * 4096;
    c.ring = L_RING + aw * 32768;
    c.vk0 = g * 256 + ((i ^ g) << 4);
    c.vv0 = g * 256 + ((i ^ (2 * g)) << 4);
    c.kaddr = c.ring + i * 256 + ((i ^ g) << 4);
    const int q4 = i >> 2, pp = lane & 3;
    c.vaddr = c.ring + 8192 + (4 * g + q4) * 256 + ((8 * (g & 1) + 2 * q4) << 4) + 8 * pp;
    __builtin_amdgcn_s_barrier();
#pragma unroll 1
    for (int u = 0; u < 2; ++u) {
      const unsigned so = c.gbase + u * 32768u, ld = c.ring + u * 16384;
      WAIT_VMI(CAPG); DMA_K(c, so, ld);
      WAIT_VMI(CAPG); DMA_K(c, so + 16384u, ld + 4096);
      WAIT_VMI(CAPG); DMA_V(c, so, ld + 8192);
      if (u == 1 && lane == 0) lds_add(L, C_RING);
      WAIT_VMI(CAPG); DMA_V(c, so + 16384u, ld + 8192 + 4096);
    }
    AttnState st;
#pragma unroll
    for (int d = 0; d < 8; ++d) st.acc[d] = f32x4{0.f, 0.f, 0.f, 0.f};
    st.m = -INFINITY; st.l = 0.f;
#pragma unroll 1
    for (int ps = opaque(0); ps < 2; ++ps) {
      unsigned T32 = 0;
      if (ps) {
        lds_wait_ge(L, C_QREADY, 1);
#pragma unroll
        for (int s = 0; s < 4; ++s) { u32x4 v = *(LAS u32x4*)(L + L_Q + (i & 3) * 256 + 64 * s + 16 * g); if (i >= 4) v = u32x4{0, 0, 0, 0}; c.qf[s] = __builtin_bit_cast(bf16x8, v); }
        T32 = lds_ldu(L, C_TLO);
        WAIT_VM(0);
#pragma unroll 1
        for (int t = 0; t < NT_TILES; t += 2) {
          if (aw == 0 && lane == 0) lds_st(L, C_PROG, t);
          if (t == NT_TILES - 2) { if (lane == 0) lds_st(L, C_ATTEND, 1); lds_wait_ge(L, C_WOREAD, 4); }
          attn_tile<0>(L, c, st, t, NT_TILES - 1 - t, lane, aw);
          attn_tile<1>(L, c, st, t + 1, NT_TILES - 2 - t, lane, aw);
        }
      }
      const float lsum = xrow16_sum(st.l);
      const unsigned mb_ = ps ? L_STG : L_OA - 8192 + 0u;
      LAS float* mr = (LAS float*)(L + L_STG + aw * 2048);
      LAS float* mlr = (LAS float*)(L + L_STG + 8192);
      (void)mb_;
      if (i < (ps ? 4 : 0)) {
#pragma unroll
        for (int d = 0; d < 8; ++d) *(LAS f32x4*)(mr + i * 128 + 16 * d + 4 * g) = st.acc[d];
        if (g == 0) { mlr[i * 8 + aw] = st.m; mlr[i * 8 + 4 + aw] = lsum; }
      }
      WAIT_LGKM0();
      unsigned old = 3u;
      if (lane == 0 && ps) old = lds_add(L, C_MRG);
      old = __builtin_amdgcn_readfirstlane(old);
      asm volatile("" ::: "memory");
      if (old == 3u) {
        const f32x4 mw = *(LAS f32x4*)(mlr + g * 8), lw = *(LAS f32x4*)(mlr + g * 8 + 4);
        f32x4 a0[4], a1[4];
#pragma unroll
        for (int ww = 0; ww < 4; ++ww) { LAS float* rec = (LAS float*)(L + L_STG + ww * 2048) + g * 128 + 4 * i; a0[ww] = *(LAS f32x4*)rec; a1[ww] = *(LAS f32x4*)(rec + 64); }
        const float M = fmaxf(fmaxf(mw[0], mw[1]), fmaxf(mw[2], mw[3]));
        float Ls = 0.f; f32x4 o0 = {0.f, 0.f, 0.f, 0.f}, o1 = {0.f, 0.f, 0.f, 0.f};
#pragma unroll
        for (int ww = 0; ww < 4; ++ww) {
          const float wt = __builtin_amdgcn_exp2f((mw[ww] - M) * 1.4426950408889634f);
          Ls = fmaf(wt, lw[ww], Ls); o0 += wt * a0[ww]; o1 += wt * a1[ww]; }
        const unsigned po = WS_PART + ((b * 32 + kvh * 4 + g) * 4 + sp) * 512 + i * 16;
        const unsigned T24 = T32 & 0xffffffu;
        const u32x2 g0 = pack2(o0[0], o0[1], T24), g1 = pack2(o0[2], o0[3], T24), g2 = pack2(o1[0], o1[1], T24), g3 = pack2(o1[2], o1[3], T24);
        const unsigned oob = ps ? 0u : 0x40000000u;
        __builtin_amdgcn_raw_buffer_store_b128(u32x4{g0[0], g0[1], g1[0], g1[1]}, rsW, po | oob, 0, 16);
        __builtin_amdgcn_raw_buffer_store_b128(u32x4{g2[0], g2[1], g3[0], g3[1]}, rsW, (po + 256) | oob, 0, 16);
        if (i == 0) __builtin_amdgcn_raw_buffer_store_b128(u32x4{__float_as_uint(M), T32, __float_as_uint(Ls), T32}, rsW, (WS_ML + (((kvh * 4 + g) * 8 + b) * 4 + sp) * 16) | oob, 0, 16);
      }
    }
    return;
  }

  const int hw = wave - 4;
  u32x4 xv = *(const GAS u32x4*)(x + hw * DM + lane * 8);
  u64 E = 0;
  if (hw == 0) E = __hip_atomic_load((GAS u64*)(ws + WS_EPOCH), RLX_AGENT);
  {
    const u64 qa = (u64)wq;
    const u32x4 rsQ = {(unsigned)qa, (unsigned)(qa >> 32), 4u << 20, 0x00020000u};
    const unsigned n0 = xc * 512 + r * 16;
    const unsigned vo = (lane >> 1) * 8192 + (lane & 1) * 16;
    dma1(vo, rsQ, (2 * hw) * 262144u + n0 * 2, L_STG + (2 * hw) * 1024);
    dma1(vo, rsQ, (2 * hw + 1) * 262144u + n0 * 2, L_STG + (2 * hw + 1) * 1024);
  }
  if (hw == 3) { if (lane < 32) lds_st(L, lane, 0u); WAIT_LGKM0(); }
  __builtin_amdgcn_s_barrier();
  asm volatile("" : "+v"(xv), "+v"(E) :: "memory");
  {
    float f[8];
#pragma unroll
    for (int k = 0; k < 4; ++k) { f[2 * k] = lo16(xv[k]); f[2 * k + 1] = hi16(xv[k]); }
    float s = ((f[0] + f[1]) + (f[2] + f[3])) + ((f[4] + f[5]) + (f[6] + f[7]));
    s = wave_sum(s);
    const float mean = s * (1.f / DM);
    float q = 0.f;
#pragma unroll
    for (int k = 0; k < 8; ++k) { const float d = f[k] - mean; q = fmaf(d, d, q); }
    q = wave_sum(q);
    const float var = rbf(q * (1.f / DM));
    const float mb = rbf(mean);
    const float sd = rbf(sqrtf(rbf(var + 1e-5f)));
    u32x4 o;
#pragma unroll
    for (int k = 0; k < 4; ++k) o[k] = cvtpk(rbf(f[2 * k] - mb) / sd, rbf(f[2 * k + 1] - mb) / sd);
    *(LAS u32x4*)(L + L_XN + hw * XN_STRIDE + lane * 16) = o;
  }
  WAIT_VM(0);
  if (hw == 0 && lane == 0) { const u64 T = E + 1; lds_st(L, C_TLO, (unsigned)T); lds_st(L, C_THI, (unsigned)(T >> 32)); }
  WAIT_LGKM0();
  if (lane == 0) lds_add(L, C_LN);
  if (hw == 0) lds_wait_ge<1>(L, C_LN, 8); else lds_wait_ge<6>(L, C_LN, 8);
  const unsigned Tlo = lds_ldu(L, C_TLO), Thi = lds_ldu(L, C_THI);
  const unsigned tg_lo = Tlo << 16, tg_hi = (Thi << 16) | (Tlo >> 16);
  const unsigned T24 = Tlo & 0xffffffu;
#define TAG_OK(lo_, hi_) ((((lo_) ^ tg_lo) >> 16) == 0 && (hi_) == tg_hi)
  const int gam = xc + 8 * sp, f0 = gam * 64 + (r & 7) * 8;
  const int hd = 4 * xc + sp, cbk = r & 7;

  if (hw == 0) {
    const f32x4 d = proj16<1>(L, lane);
    const unsigned pk = i < 8 ? cvtpk(d[0], d[1]) : cvtpk(d[2], d[3]);
    __builtin_amdgcn_raw_buffer_store_b128(u32x4{tg_lo | (pk & 0xffffu), tg_hi, tg_lo | (pk >> 16), tg_hi}, rsW,
                                           WS_QG + ((i & 7) * 4096 + xc * 512 + r * 16 + 4 * g + 2 * (i >> 3)) * 8, 0, 16);
    if (lane == 0) lds_st(L, C_STGFREE, 1);
  } else if (hw == 1) {
    lds_wait_ge<8>(L, C_GUST, 4);
    if (gam < 28) {
      const f32x4 d = proj16<0>(L, lane);
      float hv[2];
#pragma unroll
      for (int k = 0; k < 2; ++k) {
        const float own = rbf(g < 2 ? d[k] : d[2 + k]);
        const float oth = dpp<XOR8>(own);
        hv[k] = own > 0.f ? own * oth : 0.f;
      }
      const unsigned pk = cvtpk(hv[0], hv[1]);
      const int b0 = 4 * (g & 1) + (g < 2 ? 0 : 2);
      if (i < 8) __builtin_amdgcn_raw_buffer_store_b128(u32x4{tg_lo | (pk & 0xffffu), tg_hi, tg_lo | (pk >> 16), tg_hi}, rsW, WS_HG + ((f0 + i) * 8 + b0) * 8, 0, 16);
    }
    if (lane == 0) lds_st(L, C_HDONE, 1);
  } else if (hw == 2) {
    const unsigned qo = WS_QG + (b * 4096 + kvh * 512) * 8 + lane * 16;
    bool okk[4] = {false, false, false, false};
    lds_wait_ge<4>(L, C_STGFREE, 1);
#pragma unroll 1
    for (unsigned spins = 0; spins < SPIN_CAP; ++spins) {
      asm volatile("" ::: "memory");
      u32x4 v[4];
#pragma unroll
      for (int k = 0; k < 4; ++k) if (!okk[k]) v[k] = __builtin_amdgcn_raw_buffer_load_b128(rsW, qo + k * 1024, 0, 16);
#pragma unroll
      for (int k = 0; k < 4; ++k) if (!okk[k]) {
        okk[k] = TAG_OK(v[k][0], v[k][1]) && TAG_OK(v[k][2], v[k][3]);
        *(LAS unsigned*)(L + L_Q + (lane + 64 * k) * 4) = (v[k][0] & 0xffffu) | (v[k][2] << 16);
      }
      if (__all(okk[0] && okk[1] && okk[2] && okk[3])) break;
      __builtin_amdgcn_s_sleep(1);
    }
    WAIT_LGKM0();
    if (lane == 0) lds_st(L, C_QREADY, 1);
  } else if (hw >= 4) {
    const int h4 = hw - 4;
    const __amdgpu_buffer_rsrc_t rsD = __builtin_amdgcn_make_buffer_rsrc((void*)wd, 0, FF * DM * 2, 0x00020000);
    const GAS unsigned short* sgu = (const GAS unsigned short*)((lane & 1) ? wu : wg) + (size_t)(128 * h4 + (lane >> 1)) * FF + (gam < 28 ? f0 : 0);
    const GAS unsigned short* swo = (const GAS unsigned short*)wo + (size_t)(128 * hd + 32 * h4 + (lane >> 3)) * DM + cbk * 64 + (lane & 7) * 8;
    u32x4 rg[4], ro[4], rd_[2], xk[8], xvv[8];
    {
      const __amdgpu_buffer_rsrc_t rK = __builtin_amdgcn_make_buffer_rsrc((void*)kc, 0, 128u << 20, 0x00020000), rV = __builtin_amdgcn_make_buffer_rsrc((void*)vc, 0, 128u << 20, 0x00020000);
      const unsigned so = kv_off + 2u * 32768u + h4 * 4096;
      const unsigned vk0 = g * 256 + ((i ^ g) << 4), vv0 = g * 256 + ((i ^ (2 * g)) << 4);
      lds_wait_ge<4>(L, C_RING, 4);
#pragma unroll
      for (int u = 0; u < 8; ++u) { if ((u & 3) == 0) WAIT_VMI(HCAP); xk[u] = __builtin_amdgcn_raw_buffer_load_b128(rK, (vk0 ^ ((u & 3) << 6)) + (u & 3) * 1024 + (u >> 2) * 16384, so, 2); }
#pragma unroll
      for (int u = 0; u < 8; ++u) { if ((u & 3) == 0) WAIT_VMI(HCAP); xvv[u] = __builtin_amdgcn_raw_buffer_load_b128(rV, (vv0 ^ ((u & 1) << 7)) + (u & 3) * 1024 + (u >> 2) * 16384, so, 2); }
      WAIT_VMI(HCAP);
    }
#pragma unroll
    for (int u = 0; u < 4; ++u) ro[u] = __builtin_nontemporal_load((const GAS u32x4*)(swo + (size_t)u * 8 * DM));
#pragma unroll
    for (int u = 0; u < 2; ++u) rd_[u] = __builtin_amdgcn_raw_buffer_load_b128(rsD, (unsigned)((56 * hd + 16 * h4 + 8 * u + (lane >> 3)) * 1024 + cbk * 128 + (lane & 7) * 16), 0, 2);
    lds_wait_ge<2>(L, C_XF + h4, 1);
#pragma unroll
    for (int u = 0; u < 8; ++u) *(LAS u32x4*)(L + L_RING + h4 * 32768 + u * 1024 + lane * 16) = xk[u];
    WAIT_LGKM0();
    if (lane == 0) lds_st(L, C_XR + h4, 1);
    lds_wait_ge<1>(L, C_XF + h4, 2);
#pragma unroll
    for (int u = 0; u < 8; ++u) *(LAS u32x4*)(L + L_RING + h4 * 32768 + 8192 + u * 1024 + lane * 16) = xvv[u];
    WAIT_LGKM0();
    if (lane == 0) lds_st(L, C_XR + h4, 2);
#pragma unroll
    for (int u = 0; u < 4; ++u) { rg[u] = *(const GAS u32x4*)(sgu + (size_t)u * 32 * FF); WAIT_VM(0); }
    lds_wait_ge(L, C_STGFREE, 1);
#pragma unroll
    for (int u = 0; u < 4; ++u) *(LAS u32x4*)(L + L_STG + (4 * h4 + u) * 1024 + lane * 16) = rg[u];
    WAIT_LGKM0();
    if (lane == 0) lds_add(L, C_GUST);
    lds_wait_ge<4>(L, C_HDONE, 1);
#pragma unroll
    for (int u = 0; u < 4; ++u) *(LAS u32x4*)(L + L_STG + (4 * h4 + u) * 1024 + lane * 16) = ro[u];
#pragma unroll
    for (int u = 0; u < 2; ++u) *(LAS u32x4*)(L + L_XN + (2 * h4 + u) * 1024 + lane * 16) = rd_[u];
    WAIT_LGKM0();
    if (lane == 0) lds_add(L, C_WOLD);
  }
  {
    const int fl = 7 * hw + (lane >> 3), bb = lane & 7;
    const unsigned ho = WS_HG + ((56 * hd + 7 * hw) * 8 + (lane < 56 ? lane : 0)) * 8;
    u32x2 hv = {0u, 0u};
    bool ok = false;
    lds_wait_ge<32>(L, C_PROG, GATE_H);
#pragma unroll 1
    for (unsigned spins = 0; spins < SPIN_CAP; ++spins) {
      asm volatile("" ::: "memory");
      if (!ok) { hv = __builtin_amdgcn_raw_buffer_load_b64(rsW, ho, 0, 16); ok = TAG_OK(hv[0], hv[1]); }
      if (__all(ok)) break;
      __builtin_amdgcn_s_sleep(SLP_MID);
    }
    if (lane < 56) *(LAS unsigned short*)(L + L_OA + bb * OA_STRIDE + fl * 2) = (unsigned short)(hv[0] & 0xffffu);
    if (lane < 4) *(LAS unsigned*)(L + L_OA + hw * OA_STRIDE + 112 + lane * 4) = 0u;
    WAIT_LGKM0();
    if (lane == 0) lds_add(L, C_OG);
  }
  const int bf = r >> 2, cb = 4 * xc + (r & 3), n1 = cb * 16;
  const unsigned ba = (g * 8 + (i >> 2)) * 128 + (lane & 3) * 8 + (hw & 3) * 32;
  const unsigned oa = L_OA + (i & 7) * OA_STRIDE + g * 16;
  bf16x8 wf[4];
  float y2a = 0.f, y2b = 0.f;
  float xr[2] = {0.f, 0.f};
  if (hw < 4) {
    lds_wait_ge<8>(L, C_WOLD, 4);
    lds_wait_ge<4>(L, C_OG, 8);
    f32x4 d = {0.f, 0.f, 0.f, 0.f};
#pragma unroll
    for (int s = 0; s < 2; ++s) {
      const bf16x4 a0 = __builtin_amdgcn_ds_read_tr16_b64_v4bf16((LAS bf16x4*)(L + L_XN + ba + s * 4096));
      const bf16x4 a1 = __builtin_amdgcn_ds_read_tr16_b64_v4bf16((LAS bf16x4*)(L + L_XN + ba + s * 4096 + 512));
      d = __builtin_amdgcn_mfma_f32_16x16x32_bf16(__builtin_shufflevector(a0, a1, 0, 1, 2, 3, 4, 5, 6, 7), *(LAS bf16x8*)(L + oa + s * 64), d, 0, 0, 0);
    }
    y2a = i < 8 ? d[0] : d[2]; y2b = i < 8 ? d[1] : d[3];
#pragma unroll
    for (int s = 0; s < 4; ++s) {
      const bf16x4 a0 = __builtin_amdgcn_ds_read_tr16_b64_v4bf16((LAS bf16x4*)(L + L_STG + ba + s * 4096));
      const bf16x4 a1 = __builtin_amdgcn_ds_read_tr16_b64_v4bf16((LAS bf16x4*)(L + L_STG + ba + s * 4096 + 512));
      wf[s] = __builtin_shufflevector(a0, a1, 0, 1, 2, 3, 4, 5, 6, 7);
    }
    asm volatile("s_waitcnt lgkmcnt(0)" : "+v"(wf[0]), "+v"(wf[1]), "+v"(wf[2]), "+v"(wf[3]), "+v"(y2a), "+v"(y2b) :: "memory");
    if (lane == 0) lds_add(L, C_WOREAD);
  } else if (hw == 6) {
    const unsigned xq = *(const GAS unsigned*)(x + bf * DM + n1 + (lane & 7) * 2);
    xr[0] = lo16(xq); xr[1] = hi16(xq);
  } else if (hw == 5 && w == 0) {
#pragma unroll 1
    for (unsigned spins = 0; spins < SPIN_CAP; ++spins) {
      bool ok = true;
#pragma unroll
      for (int k = 0; k < 4; ++k) { const u32x2 v = __builtin_amdgcn_raw_buffer_load_b64(rsW, WS_QG + (lane + 64 * k) * 128, 0, 16); ok &= TAG_OK(v[0], v[1]); }
      if (__all(ok)) break;
      __builtin_amdgcn_s_sleep(SLP_MID);
      asm volatile("" ::: "memory");
    }
    if (lane == 0) __hip_atomic_store((GAS u64*)(ws + WS_EPOCH), ((u64)Thi << 32) | Tlo, RLX_AGENT);
  }
  lds_wait_ge<4>(L, C_WOREAD, 4);
#pragma unroll 1
  for (int rd = opaque(0); rd < 2; ++rd) {
    const unsigned oob = rd ? 0u : 0x40000000u;
    {
      if (rd) lds_wait_ge<16>(L, C_ATTEND, 1);
      const unsigned po = (WS_PART + ((hw * 32 + hd) * 4 + g) * 512 + i * 16) | oob;
      const unsigned mo = (WS_ML + ((hd * 8 + hw) * 4 + g) * 16) | oob;
      u32x4 pv0 = {0u, 0u, 0u, 0u}, pv1 = {0u, 0u, 0u, 0u}, ml = {0u, 0u, 0u, 0u};
      if (hw == 7) {
        bool ok = lane >= 32;
#pragma unroll 1
        for (unsigned spins = 0; spins < SPIN_CAP; ++spins) {
          asm volatile("" ::: "memory");
          if (!ok) {
            ml = __builtin_amdgcn_raw_buffer_load_b128(rsW, (WS_ML + hd * 512 + lane * 16) | oob, 0, 16);
            ok = ml[1] == Tlo && ml[3] == Tlo;
          }
          const unsigned miss = ~(unsigned)__ballot(ok);
          if (lane == 0 && rd) lds_st(L, C_MLMASK, ~miss);
          const unsigned two = (miss & (miss >> 1) & 0x77777777u) | (miss & (miss >> 2) & 0x33333333u) | (miss & (miss >> 3) & 0x11111111u);
          if (two == 0u || !rd) break;
          __builtin_amdgcn_s_sleep(SLP_PART);
        }
        if (lane == 0 && rd) lds_st(L, C_MLMASK, 0xffffffffu);
      } else if (rd) {
        while (__builtin_popcount((lds_ldu(L, C_MLMASK) >> (4 * hw)) & 15u) < 3) __builtin_amdgcn_s_sleep(1);
        asm volatile("" ::: "memory");
      }
      bool ok = false;
#pragma unroll 1
      for (unsigned spins = 0; spins < SPIN_CAP; ++spins) {
        asm volatile("" ::: "memory");
        if (!ok) {
          pv0 = __builtin_amdgcn_raw_buffer_load_b128(rsW, po, 0, 16);
          pv1 = __builtin_amdgcn_raw_buffer_load_b128(rsW, po + 256, 0, 16);
          ml = __builtin_amdgcn_raw_buffer_load_b128(rsW, mo, 0, 16);
          ok = (pv0[1] >> 8) == T24 && (pv0[3] >> 8) == T24 && (pv1[1] >> 8) == T24 && (pv1[3] >> 8) == T24 && ml[1] == Tlo && ml[3] == Tlo;
        }
        if (__all(ok) || !rd) break;
        __builtin_amdgcn_s_sleep(SLP_DIR);
      }
      const float mj = __uint_as_float(ml[0]);
      const float M = xrow16_max(mj);
      const float wt = __builtin_amdgcn_exp2f((mj - M) * 1.4426950408889634f);
      const float inv = 1.f / xrow16_sum(wt * __uint_as_float(ml[2]));
      const u32x2 oa0 = {cvtpk(xrow16_sum(wt * unpack_a(pv0[0])) * inv, xrow16_sum(wt * unpack_b(pv0[0], pv0[1])) * inv), cvtpk(xrow16_sum(wt * unpack_a(pv0[2])) * inv, xrow16_sum(wt * unpack_b(pv0[2], pv0[3])) * inv)};
      const u32x2 oa1 = {cvtpk(xrow16_sum(wt * unpack_a(pv1[0])) * inv, xrow16_sum(wt * unpack_b(pv1[0], pv1[1])) * inv), cvtpk(xrow16_sum(wt * unpack_a(pv1[2])) * inv, xrow16_sum(wt * unpack_b(pv1[2], pv1[3])) * inv)};
      if (g == 0) { *(LAS u32x2*)(L + L_OA + hw * OA_STRIDE + i * 8) = oa0; *(LAS u32x2*)(L + L_OA + hw * OA_STRIDE + 128 + i * 8) = oa1; }
      WAIT_LGKM0();
      if (lane == 0 && rd) lds_add(L, C_OG);
    }
    if (hw < 4) {
      if (rd) lds_wait_ge(L, C_OG, 16);
      f32x4 d = {0.f, 0.f, 0.f, 0.f};
#pragma unroll
      for (int s = 0; s < 4; ++s) d = __builtin_amdgcn_mfma_f32_16x16x32_bf16(wf[s], *(LAS bf16x8*)(L + oa + s * 64), d, 0, 0, 0);
      const u32x2 g0 = pack2(i < 8 ? d[0] : d[2], y2a, T24), g1 = pack2(i < 8 ? d[1] : d[3], y2b, T24);
      const unsigned yo = ((((i & 7) * 32 + 4 * cbk + hw) * 32 + hd) * 16 + 4 * g + 2 * (i >> 3)) * 8;
      __builtin_amdgcn_raw_buffer_store_b128(u32x4{g0[0], g0[1], g1[0], g1[1]}, rsW, (WS_Y + yo) | oob, 0, 16);
    } else if (hw == 6) {
      const unsigned io = (WS_Y + (bf * 32 + cb) * 4096 + lane * 16) | oob;
      u32x4 y[4];
      bool okk[4] = {false, false, false, false};
#pragma unroll 1
      for (unsigned spins = 0; spins < SPIN_CAP; ++spins) {
        asm volatile("" ::: "memory");
#pragma unroll
        for (int k = 0; k < 4; ++k) if (!okk[k]) y[k] = __builtin_amdgcn_raw_buffer_load_b128(rsW, io + k * 1024, 0, 16);
#pragma unroll
        for (int k = 0; k < 4; ++k) okk[k] = (y[k][1] >> 8) == T24 && (y[k][3] >> 8) == T24;
        if (__all(okk[0] && okk[1] && okk[2] && okk[3]) || !rd) break;
        __builtin_amdgcn_s_sleep(SLP_Y);
      }
      float o[2];
#pragma unroll
      for (int k = 0; k < 2; ++k) {
        float t = (unpack_a(y[0][2 * k]) + unpack_a(y[1][2 * k])) + (unpack_a(y[2][2 * k]) + unpack_a(y[3][2 * k]));
        float u = (unpack_b(y[0][2 * k], y[0][2 * k + 1]) + unpack_b(y[1][2 * k], y[1][2 * k + 1])) + (unpack_b(y[2][2 * k], y[2][2 * k + 1]) + unpack_b(y[3][2 * k], y[3][2 * k + 1]));
        t += dpp<XOR8>(t); t = rbf(xrow16_sum(t));
        u += dpp<XOR8>(u); u = rbf(xrow16_sum(u));
        const float gate = rbf(u / (1.f + __expf(-u)));
        o[k] = xr[k] + rbf(t * gate);
      }
      if (lane < (rd ? 8 : 0)) *(GAS unsigned*)(out + bf * DM + n1 + lane * 2) = cvtpk(o[0], o[1]);
    }
  }
}

extern "C" void kernel_launch(void* const* d_in, const int* in_sizes, int n_in, void* d_out, int out_size, void* d_ws, size_t ws_size, hipStream_t stream) {
  const unsigned short* x = (const unsigned short*)d_in[0];
  const unsigned short* kc = (const unsigned short*)d_in[1];
  const unsigned short* vc = (const unsigned short*)d_in[2];
  const unsigned short* wq = (const unsigned short*)d_in[3];
  const unsigned short* wo = (const unsigned short*)d_in[4];
  const unsigned short* wg = (const unsigned short*)d_in[6];
  const unsigned short* wu = (const unsigned short*)d_in[7];
  const unsigned short* wd = (const unsigned short*)d_in[8];
  (void)hipFuncSetAttribute((const void*)fused_decode, hipFuncAttributeMaxDynamicSharedMemorySize, L_END);
  fused_decode<<<dim3(NWG), dim3(NTHR), L_END, stream>>>(x, kc, vc, wq, wo, wg, wu, wd, (unsigned short*)d_out, (unsigned char*)d_ws);
}
```

```cpp
#include <hip/hip_runtime.h>
#include <hip/hip_bf16.h>
#include <cstdint>

typedef __bf16 bf16x2 __attribute__((ext_vector_type(2)));
typedef __bf16 bf16x4 __attribute__((ext_vector_type(4)));
typedef __bf16 bf16x8 __attribute__((ext_vector_type(8)));
typedef unsigned u32x2 __attribute__((ext_vector_type(2)));
typedef unsigned u32x4 __attribute__((ext_vector_type(4)));
typedef float f32x2 __attribute__((ext_vector_type(2)));
typedef float f32x4 __attribute__((ext_vector_type(4)));
typedef unsigned long long u64;
#define LAS __attribute__((address_space(3)))
#define GAS __attribute__((address_space(1)))
#define RLX_AGENT __ATOMIC_RELAXED, __HIP_MEMORY_SCOPE_AGENT
#define RLX_WG __ATOMIC_RELAXED, __HIP_MEMORY_SCOPE_WORKGROUP

constexpr int NB = 8, DM = 512, HKV = 8, SK = 8192, FF = 1792;
constexpr int NWG = 256, NTHR = 768;
constexpr int NT_TILES = 16;

#define CAPG 5
#define HCAP 2
#define GATE_H 8
#define SLP_PART 4
#define SLP_DIR 2
#define SLP_Y 2
#define SLP_MID 64

constexpr unsigned WS_EPOCH = 0;
constexpr unsigned WS_ML = 8192;
constexpr unsigned WS_QG = 32768;
constexpr unsigned WS_HG = WS_QG + NB * 4096 * 8;
constexpr unsigned WS_PART = WS_HG + NB * FF * 8;
constexpr unsigned WS_Y = WS_PART + NWG * 2048;
constexpr unsigned WS_END = WS_Y + (1u << 20);

constexpr unsigned L_RING = 0;
constexpr unsigned L_STG = 131072;
constexpr unsigned L_XN = L_STG + 16384;
constexpr unsigned XN_STRIDE = 1040;
constexpr unsigned L_Q = L_XN + 8 * XN_STRIDE;
constexpr unsigned L_OA = L_Q + 1024;
constexpr unsigned OA_STRIDE = 272;
constexpr unsigned L_CTL = L_OA + 8 * OA_STRIDE;
constexpr unsigned L_END = L_CTL + 128;
static_assert(L_END <= 163840, "LDS");
enum { C_LN = 0, C_STGFREE, C_QREADY, C_HDONE, C_MRG, C_ATTEND, C_TLO, C_THI, C_WOLD, C_WOREAD, C_OG, C_PROG, C_MLMASK, C_RING, C_GUST,
       C_XF = 20  , C_XR = 24   };

__device__ __forceinline__ unsigned cvtpk(float a, float b) {
  bf16x2 t = __builtin_convertvector(f32x2{a, b}, bf16x2);
  return __builtin_bit_cast(unsigned, t);
}
__device__ __forceinline__ float lo16(unsigned u) { return __builtin_bit_cast(float, u << 16); }
__device__ __forceinline__ float hi16(unsigned u) { return __builtin_bit_cast(float, u & 0xffff0000u); }
__device__ __forceinline__ float rbf(float f) { return lo16(cvtpk(f, f)); }
template <int CTRL> __device__ __forceinline__ float dpp(float x) {
  return __builtin_bit_cast(float, __builtin_amdgcn_mov_dpp(__builtin_bit_cast(int, x), CTRL, 0xf, 0xf, true));
}
constexpr int XOR1 = 0xB1, XOR2 = 0x4E, XOR7 = 0x141, XOR8 = 0x128;
__device__ __forceinline__ float xrow16_max(float x) {
  auto s = __builtin_amdgcn_permlane16_swap(__float_as_uint(x), __float_as_uint(x), false, false);
  x = fmaxf(__uint_as_float(s[0]), __uint_as_float(s[1]));
  auto t = __builtin_amdgcn_permlane32_swap(__float_as_uint(x), __float_as_uint(x), false, false);
  return fmaxf(__uint_as_float(t[0]), __uint_as_float(t[1]));
}
__device__ __forceinline__ float xrow16_sum(float x) {
  auto s = __builtin_amdgcn_permlane16_swap(__float_as_uint(x), __float_as_uint(x), false, false);
  x = __uint_as_float(s[0]) + __uint_as_float(s[1]);
  auto t = __builtin_amdgcn_permlane32_swap(__float_as_uint(x), __float_as_uint(x), false, false);
  return __uint_as_float(t[0]) + __uint_as_float(t[1]);
}
__device__ __forceinline__ float wave_sum(float x) {
  x += dpp<XOR1>(x); x += dpp<XOR2>(x); x += dpp<XOR7>(x); x += dpp<XOR8>(x);
  return xrow16_sum(x);
}

__device__ __forceinline__ void dma4(unsigned v0, unsigned v1, unsigned v2, unsigned v3, u32x4 rs, unsigned so, unsigned ldsaddr) {
  unsigned keep;
  asm volatile(
    "s_mov_b32 %0, m0\n\t"
    "s_mov_b32 m0, %7\n\t"
    "s_nop 0\n\t"
    "buffer_load_dwordx4 %1, %5, %6 offen nt lds\n\t"
    "buffer_load_dwordx4 %2, %5, %6 offen offset:1024 nt lds\n\t"
    "buffer_load_dwordx4 %3, %5, %6 offen offset:2048 nt lds\n\t"
    "buffer_load_dwordx4 %4, %5, %6 offen offset:3072 nt lds\n\t"
    "s_mov_b32 m0, %0"
    : "=&s"(keep) : "v"(v0), "v"(v1), "v"(v2), "v"(v3), "s"(rs), "s"(so), "s"(ldsaddr) : "memory");
}
__device__ __forceinline__ void dma1(unsigned v0, u32x4 rs, unsigned so, unsigned ldsaddr) {
  unsigned keep;
  asm volatile(
    "s_mov_b32 %0, m0\n\t"
    "s_mov_b32 m0, %4\n\t"
    "s_nop 0\n\t"
    "buffer_load_dwordx4 %1, %2, %3 offen lds\n\t"
    "s_mov_b32 m0, %0"
    : "=&s"(keep) : "v"(v0), "s"(rs), "s"(so), "s"(ldsaddr) : "memory");
}
#define WAIT_VM(n) asm volatile("s_waitcnt vmcnt(" #n ")" ::: "memory")
#define WAIT_VMI(n) asm volatile("s_waitcnt vmcnt(%0)" :: "i"(n) : "memory")
#define WAIT_LGKM0() asm volatile("s_waitcnt lgkmcnt(0)" ::: "memory")

__device__ __forceinline__ unsigned lds_ld(LAS unsigned char* L, int idx) { return __hip_atomic_load((LAS unsigned*)(L + L_CTL) + idx, RLX_WG); }
__device__ __forceinline__ unsigned lds_ldu(LAS unsigned char* L, int idx) { return (unsigned)__builtin_amdgcn_readfirstlane(lds_ld(L, idx)); }
__device__ __forceinline__ void lds_st(LAS unsigned char* L, int idx, unsigned v) { __hip_atomic_store((LAS unsigned*)(L + L_CTL) + idx, v, RLX_WG); }
__device__ __forceinline__ unsigned lds_add(LAS unsigned char* L, int idx) { return __hip_atomic_fetch_add((LAS unsigned*)(L + L_CTL) + idx, 1u, RLX_WG); }
template <int SLP = 1>
__device__ __forceinline__ void lds_wait_ge(LAS unsigned char* L, int idx, unsigned want) {
  while (lds_ldu(L, idx) < want) __builtin_amdgcn_s_sleep(SLP);
  asm volatile("" ::: "memory");
}
constexpr unsigned SPIN_CAP = 1u << 15;

__device__ __forceinline__ u32x2 pack2(float a, float b, unsigned tag24) {
  const unsigned ua = (__float_as_uint(a) + 0x800u) >> 12, ub = (__float_as_uint(b) + 0x800u) >> 12;
  return u32x2{ua | (ub << 20), (ub >> 12) | (tag24 << 8)};
}
__device__ __forceinline__ float unpack_a(unsigned lo) { return __uint_as_float(lo << 12); }
__device__ __forceinline__ float unpack_b(unsigned lo, unsigned hi) { return __uint_as_float(((lo >> 20) | (hi << 12)) << 12); }

struct AttnState { f32x4 acc[8]; float m, l; };
struct AttnConst { u32x4 rsK, rsV; unsigned gbase, ring, vk0, vv0, kaddr, vaddr; bf16x8 qf[4]; };

#define DMA_K(c_, so_, ld_) dma4((c_).vk0, (c_).vk0 ^ 64, (c_).vk0 ^ 128, (c_).vk0 ^ 192, (c_).rsK, (so_), (ld_))
#define DMA_V(c_, so_, ld_) dma4((c_).vv0, (c_).vv0 ^ 128, (c_).vv0, (c_).vv0 ^ 128, (c_).rsV, (so_), (ld_))

template <int SLOT>
__device__ __forceinline__ void attn_tile(LAS unsigned char* L, const AttnConst& c, AttnState& st, const int t, const int rem, const int lane, const int aw) {
  constexpr float SCALE = 0.088388347648318440f, LOG2E = 1.4426950408889634f;
  const unsigned so2 = c.gbase + (unsigned)(t + 2) * 32768u, ld2 = c.ring + SLOT * 16384;
  if (rem >= 1) WAIT_VM(24); else WAIT_VM(8);
  if (SLOT == 0 && t == 2) lds_wait_ge(L, C_XR + aw, 1);
  f32x4 s0 = {0.f, 0.f, 0.f, 0.f}, s1 = {0.f, 0.f, 0.f, 0.f};
#pragma unroll
  for (int s = 0; s < 4; ++s) {
    const bf16x8 k0 = *(LAS bf16x8*)(L + (c.kaddr ^ (s << 6)) + SLOT * 16384);
    const bf16x8 k1 = *(LAS bf16x8*)(L + (c.kaddr ^ (s << 6)) + SLOT * 16384 + 4096);
    s0 = __builtin_amdgcn_mfma_f32_16x16x32_bf16(k0, c.qf[s], s0, 0, 0, 0);
    s1 = __builtin_amdgcn_mfma_f32_16x16x32_bf16(k1, c.qf[s], s1, 0, 0, 0);
  }
  WAIT_LGKM0();
  if (SLOT == 0 && t == 0) { if (lane == 0) lds_st(L, C_XF + aw, 1); }
  else if (rem >= 2) { WAIT_VMI(CAPG); DMA_K(c, so2, ld2); WAIT_VMI(CAPG); DMA_K(c, so2 + 16384u, ld2 + 4096); }
  float x[8];
  { unsigned u;
    u = cvtpk(s0[0], s0[1]); u = cvtpk(lo16(u) * SCALE, hi16(u) * SCALE); x[0] = lo16(u); x[1] = hi16(u);
    u = cvtpk(s0[2], s0[3]); u = cvtpk(lo16(u) * SCALE, hi16(u) * SCALE); x[2] = lo16(u); x[3] = hi16(u);
    u = cvtpk(s1[0], s1[1]); u = cvtpk(lo16(u) * SCALE, hi16(u) * SCALE); x[4] = lo16(u); x[5] = hi16(u);
    u = cvtpk(s1[2], s1[3]); u = cvtpk(lo16(u) * SCALE, hi16(u) * SCALE); x[6] = lo16(u); x[7] = hi16(u); }
  float pm = fmaxf(fmaxf(fmaxf(x[0], x[1]), fmaxf(x[2], x[3])), fmaxf(fmaxf(x[4], x[5]), fmaxf(x[6], x[7])));
  pm = xrow16_max(pm);
  if (__any(pm > st.m)) {
    const float mn = fmaxf(st.m, pm);
    const float alpha = __builtin_amdgcn_exp2f((st.m - mn) * LOG2E);
    st.l *= alpha; st.m = mn;
#pragma unroll
    for (int d = 0; d < 8; ++d) st.acc[d] *= alpha;
  }
  const float mb = st.m * LOG2E;
  float p[8];
#pragma unroll
  for (int e = 0; e < 8; ++e) p[e] = __builtin_amdgcn_exp2f(fmaf(x[e], LOG2E, -mb));
  st.l += ((p[0] + p[1]) + (p[2] + p[3])) + ((p[4] + p[5]) + (p[6] + p[7]));
  const u32x4 pw = {cvtpk(p[0], p[1]), cvtpk(p[2], p[3]), cvtpk(p[4], p[5]), cvtpk(p[6], p[7])};
  const bf16x8 pf = __builtin_bit_cast(bf16x8, pw);
  if (rem >= 2) WAIT_VM(24); else if (rem == 1) WAIT_VM(16); else WAIT_VM(0);
  if (SLOT == 0 && t == 2) lds_wait_ge(L, C_XR + aw, 2);
#pragma unroll
  for (int d = 0; d < 8; ++d) {
    const bf16x4 v0 = __builtin_amdgcn_ds_read_tr16_b64_v4bf16((LAS bf16x4*)(L + (c.vaddr ^ (d << 5)) + SLOT * 16384));
    const bf16x4 v1 = __builtin_amdgcn_ds_read_tr16_b64_v4bf16((LAS bf16x4*)(L + (c.vaddr ^ (d << 5)) + SLOT * 16384 + 4096));
    const bf16x8 vf = __builtin_shufflevector(v0, v1, 0, 1, 2, 3, 4, 5, 6, 7);
    st.acc[d] = __builtin_amdgcn_mfma_f32_16x16x32_bf16(vf, pf, st.acc[d], 0, 0, 0);
  }
  WAIT_LGKM0();
  if (SLOT == 0 && t == 0) { if (lane == 0) lds_st(L, C_XF + aw, 2); }
  else if (rem >= 2) { WAIT_VMI(CAPG); DMA_V(c, so2, ld2 + 8192); WAIT_VMI(CAPG); DMA_V(c, so2 + 16384u, ld2 + 8192 + 4096); }
}

template <int TR>
__device__ __forceinline__ f32x4 proj16(LAS unsigned char* L, int lane) {
  const int g = lane >> 4, i = lane & 15;
  unsigned aa = L_XN + (i & 7) * XN_STRIDE + g * 16;
  unsigned ba = L_STG + g * 256 + i * 8;
  f32x4 d0 = {0.f, 0.f, 0.f, 0.f}, d1 = {0.f, 0.f, 0.f, 0.f};
#pragma unroll 2
  for (int s = 0; s < 4; ++s) {
    bf16x8 a[4], bb[4];
#pragma unroll
    for (int k = 0; k < 4; ++k) {
      a[k] = *(LAS bf16x8*)(L + aa + k * 64);
      const bf16x4 b0 = __builtin_amdgcn_ds_read_tr16_b64_v4bf16((LAS bf16x4*)(L + ba + k * 1024));
      const bf16x4 b1 = __builtin_amdgcn_ds_read_tr16_b64_v4bf16((LAS bf16x4*)(L + ba + k * 1024 + 128));
      bb[k] = __builtin_shufflevector(b0, b1, 0, 1, 2, 3, 4, 5, 6, 7);
    }
    if (TR) {
      d0 = __builtin_amdgcn_mfma_f32_16x16x32_bf16(bb[0], a[0], d0, 0, 0, 0); d1 = __builtin_amdgcn_mfma_f32_16x16x32_bf16(bb[1], a[1], d1, 0, 0, 0);
      d0 = __builtin_amdgcn_mfma_f32_16x16x32_bf16(bb[2], a[2], d0, 0, 0, 0); d1 = __builtin_amdgcn_mfma_f32_16x16x32_bf16(bb[3], a[3], d1, 0, 0, 0);
    } else {
      d0 = __builtin_amdgcn_mfma_f32_16x16x32_bf16(a[0], bb[0], d0, 0, 0, 0); d1 = __builtin_amdgcn_mfma_f32_16x16x32_bf16(a[1], bb[1], d1, 0, 0, 0);
      d0 = __builtin_amdgcn_mfma_f32_16x16x32_bf16(a[2], bb[2], d0, 0, 0, 0); d1 = __builtin_amdgcn_mfma_f32_16x16x32_bf16(a[3], bb[3], d1, 0, 0, 0);
    }
    aa += 256; ba += 4096;
  }
  return d0 + d1;
}

__global__ void __launch_bounds__(NTHR, 3)
fused_decode(const unsigned short* __restrict__ x, const unsigned short* __restrict__ kc, const unsigned short* __restrict__ vc,
             const unsigned short* __restrict__ wq, const unsigned short* __restrict__ wo, const unsigned short* __restrict__ wg,
             const unsigned short* __restrict__ wu, const unsigned short* __restrict__ wd, unsigned short* __restrict__ out, unsigned char* ws) {
  extern __shared__ __attribute__((aligned(16))) unsigned char lds[];
  LAS unsigned char* L = (LAS unsigned char*)lds;
  const int w = blockIdx.x;
  const int tid = threadIdx.x, lane = tid & 63, wave = __builtin_amdgcn_readfirstlane(tid >> 6);
  const int g = lane >> 4, i = lane & 15;
  const int xc = w & 7, r = w >> 3;
  const int b = r & 7, sp = r >> 3, kvh = (xc + 2 * sp) & 7;
  const __amdgpu_buffer_rsrc_t rsW = __builtin_amdgcn_make_buffer_rsrc(ws, 0, WS_END, 0x00020000);
  const unsigned kv_off = (unsigned)(((b * HKV + kvh) * SK + sp * 2048) * 256);

  if (wave < 4) {
    const int aw = wave;
    const u64 ka = (u64)kc, va = (u64)vc;
    AttnConst c;
    c.rsK = u32x4{(unsigned)ka, (unsigned)(ka >> 32), 128u << 20, 0x00020000u};
    c.rsV = u32x4{(unsigned)va, (unsigned)(va >> 32), 128u << 20, 0x00020000u};
    c.gbase = kv_off + aw * 4096;
    c.ring = L_RING + aw * 32768;
    c.vk0 = g * 256 + ((i ^ g) << 4);
    c.vv0 = g * 256 + ((i ^ (2 * g)) << 4);
    c.kaddr = c.ring + i * 256 + ((i ^ g) << 4);
    const int q4 = i >> 2, pp = lane & 3;
    c.vaddr = c.ring + 8192 + (4 * g + q4) * 256 + ((8 * (g & 1) + 2 * q4) << 4) + 8 * pp;
    __builtin_amdgcn_s_barrier();
#pragma unroll 1
    for (int u = 0; u < 2; ++u) {
      const unsigned so = c.gbase + u * 32768u, ld = c.ring + u * 16384;
      WAIT_VMI(CAPG); DMA_K(c, so, ld);
      WAIT_VMI(CAPG); DMA_K(c, so + 16384u, ld + 4096);
      WAIT_VMI(CAPG); DMA_V(c, so, ld + 8192);
      if (u == 1 && lane == 0) lds_add(L, C_RING);
      WAIT_VMI(CAPG); DMA_V(c, so + 16384u, ld + 8192 + 4096);
    }
    lds_wait_ge(L, C_QREADY, 1);
#pragma unroll
    for (int s = 0; s < 4; ++s) { u32x4 v = *(LAS u32x4*)(L + L_Q + (i & 3) * 256 + 64 * s + 16 * g); if (i >= 4) v = u32x4{0, 0, 0, 0}; c.qf[s] = __builtin_bit_cast(bf16x8, v); }
    AttnState st;
#pragma unroll
    for (int d = 0; d < 8; ++d) st.acc[d] = f32x4{0.f, 0.f, 0.f, 0.f};
    st.m = -INFINITY; st.l = 0.f;
    const unsigned T32 = lds_ldu(L, C_TLO);
    WAIT_VM(0);
#pragma unroll 1
    for (int t = 0; t < NT_TILES; t += 2) {
      if (aw == 0 && lane == 0) lds_st(L, C_PROG, t);
      if (t == NT_TILES - 2) { if (lane == 0) lds_st(L, C_ATTEND, 1); lds_wait_ge(L, C_WOREAD, 4); }
      attn_tile<0>(L, c, st, t, NT_TILES - 1 - t, lane, aw);
      attn_tile<1>(L, c, st, t + 1, NT_TILES - 2 - t, lane, aw);
    }
    const float lsum = xrow16_sum(st.l);
    LAS float* mr = (LAS float*)(L + L_STG + aw * 2048);
    LAS float* mlr = (LAS float*)(L + L_STG + 8192);
    if (i < 4) {
#pragma unroll
      for (int d = 0; d < 8; ++d) *(LAS f32x4*)(mr + i * 128 + 16 * d + 4 * g) = st.acc[d];
      if (g == 0) { mlr[i * 8 + aw] = st.m; mlr[i * 8 + 4 + aw] = lsum; }
    }
    WAIT_LGKM0();
    unsigned old = 0;
    if (lane == 0) old = lds_add(L, C_MRG);
    old = __builtin_amdgcn_readfirstlane(old);
    asm volatile("" ::: "memory");
    if (old == 3u) {
      const f32x4 mw = *(LAS f32x4*)(mlr + g * 8), lw = *(LAS f32x4*)(mlr + g * 8 + 4);
      f32x4 a0[4], a1[4];
#pragma unroll
      for (int ww = 0; ww < 4; ++ww) { LAS float* rec = (LAS float*)(L + L_STG + ww * 2048) + g * 128 + 4 * i; a0[ww] = *(LAS f32x4*)rec; a1[ww] = *(LAS f32x4*)(rec + 64); }
      const float M = fmaxf(fmaxf(mw[0], mw[1]), fmaxf(mw[2], mw[3]));
      float Ls = 0.f; f32x4 o0 = {0.f, 0.f, 0.f, 0.f}, o1 = {0.f, 0.f, 0.f, 0.f};
#pragma unroll
      for (int ww = 0; ww < 4; ++ww) {
        const float wt = __builtin_amdgcn_exp2f((mw[ww] - M) * 1.4426950408889634f);
        Ls = fmaf(wt, lw[ww], Ls); o0 += wt * a0[ww]; o1 += wt * a1[ww]; }
      const unsigned po = WS_PART + ((b * 32 + kvh * 4 + g) * 4 + sp) * 512 + i * 16;
      const unsigned T24 = T32 & 0xffffffu;
      const u32x2 g0 = pack2(o0[0], o0[1], T24), g1 = pack2(o0[2], o0[3], T24), g2 = pack2(o1[0], o1[1], T24), g3 = pack2(o1[2], o1[3], T24);
      __builtin_amdgcn_raw_buffer_store_b128(u32x4{g0[0], g0[1], g1[0], g1[1]}, rsW, po, 0, 16);
      __builtin_amdgcn_raw_buffer_store_b128(u32x4{g2[0], g2[1], g3[0], g3[1]}, rsW, po + 256, 0, 16);
      if (i == 0) __builtin_amdgcn_raw_buffer_store_b128(u32x4{__float_as_uint(M), T32, __float_as_uint(Ls), T32}, rsW, WS_ML + (((kvh * 4 + g) * 8 + b) * 4 + sp) * 16, 0, 16);
    }
    return;
  }

  const int hw = wave - 4;
  u32x4 xv = *(const GAS u32x4*)(x + hw * DM + lane * 8);
  u64 E = 0;
  if (hw == 0) E = __hip_atomic_load((GAS u64*)(ws + WS_EPOCH), RLX_AGENT);
  {
    const u64 qa = (u64)wq;
    const u32x4 rsQ = {(unsigned)qa, (unsigned)(qa >> 32), 4u << 20, 0x00020000u};
    const unsigned n0 = xc * 512 + r * 16;
    const unsigned vo = (lane >> 1) * 8192 + (lane & 1) * 16;
    dma1(vo, rsQ, (2 * hw) * 262144u + n0 * 2, L_STG + (2 * hw) * 1024);
    dma1(vo, rsQ, (2 * hw + 1) * 262144u + n0 * 2, L_STG + (2 * hw + 1) * 1024);
  }
  if (hw == 3) { if (lane < 32) lds_st(L, lane, 0u); WAIT_LGKM0(); }
  __builtin_amdgcn_s_barrier();
  asm volatile("" : "+v"(xv), "+v"(E) :: "memory");
  {
    float f[8];
#pragma unroll
    for (int k = 0; k < 4; ++k) { f[2 * k] = lo16(xv[k]); f[2 * k + 1] = hi16(xv[k]); }
    float s = ((f[0] + f[1]) + (f[2] + f[3])) + ((f[4] + f[5]) + (f[6] + f[7]));
    s = wave_sum(s);
    const float mean = s * (1.f / DM);
    float q = 0.f;
#pragma unroll
    for (int k = 0; k < 8; ++k) { const float d = f[k] - mean; q = fmaf(d, d, q); }
    q = wave_sum(q);
    const float var = rbf(q * (1.f / DM));
    const float mb = rbf(mean);
    const float sd = rbf(sqrtf(rbf(var + 1e-5f)));
    u32x4 o;
#pragma unroll
    for (int k = 0; k < 4; ++k) o[k] = cvtpk(rbf(f[2 * k] - mb) / sd, rbf(f[2 * k + 1] - mb) / sd);
    *(LAS u32x4*)(L + L_XN + hw * XN_STRIDE + lane * 16) = o;
  }
  WAIT_VM(0);
  if (hw == 0 && lane == 0) { const u64 T = E + 1; lds_st(L, C_TLO, (unsigned)T); lds_st(L, C_THI, (unsigned)(T >> 32)); }
  WAIT_LGKM0();
  if (lane == 0) lds_add(L, C_LN);
  if (hw == 0) lds_wait_ge<1>(L, C_LN, 8); else lds_wait_ge<6>(L, C_LN, 8);
  const unsigned Tlo = lds_ldu(L, C_TLO), Thi = lds_ldu(L, C_THI);
  const unsigned tg_lo = Tlo << 16, tg_hi = (Thi << 16) | (Tlo >> 16);
  const unsigned T24 = Tlo & 0xffffffu;
#define TAG_OK(lo_, hi_) ((((lo_) ^ tg_lo) >> 16) == 0 && (hi_) == tg_hi)
  const int gam = xc + 8 * sp, f0 = gam * 64 + (r & 7) * 8;
  const int hd = 4 * xc + sp, cbk = r & 7;

  if (hw == 0) {
    const f32x4 d = proj16<1>(L, lane);
    const unsigned pk = i < 8 ? cvtpk(d[0], d[1]) : cvtpk(d[2], d[3]);
    __builtin_amdgcn_raw_buffer_store_b128(u32x4{tg_lo | (pk & 0xffffu), tg_hi, tg_lo | (pk >> 16), tg_hi}, rsW,
                                           WS_QG + ((i & 7) * 4096 + xc * 512 + r * 16 + 4 * g + 2 * (i >> 3)) * 8, 0, 16);
    if (lane == 0) lds_st(L, C_STGFREE, 1);
  } else if (hw == 1) {
    lds_wait_ge<8>(L, C_GUST, 4);
    if (gam < 28) {
      const f32x4 d = proj16<0>(L, lane);
      float hv[2];
#pragma unroll
      for (int k = 0; k < 2; ++k) {
        const float own = rbf(g < 2 ? d[k] : d[2 + k]);
        const float oth = dpp<XOR8>(own);
        hv[k] = own > 0.f ? own * oth : 0.f;
      }
      const unsigned pk = cvtpk(hv[0], hv[1]);
      const int b0 = 4 * (g & 1) + (g < 2 ? 0 : 2);
      if (i < 8) __builtin_amdgcn_raw_buffer_store_b128(u32x4{tg_lo | (pk & 0xffffu), tg_hi, tg_lo | (pk >> 16), tg_hi}, rsW, WS_HG + ((f0 + i) * 8 + b0) * 8, 0, 16);
    }
    if (lane == 0) lds_st(L, C_HDONE, 1);
  } else if (hw == 2) {
    const unsigned qo = WS_QG + (b * 4096 + kvh * 512) * 8 + lane * 16;
    bool okk[4] = {false, false, false, false};
    lds_wait_ge<4>(L, C_STGFREE, 1);
#pragma unroll 1
    for (unsigned spins = 0; spins < SPIN_CAP; ++spins) {
      asm volatile("" ::: "memory");
      u32x4 v[4];
#pragma unroll
      for (int k = 0; k < 4; ++k) if (!okk[k]) v[k] = __builtin_amdgcn_raw_buffer_load_b128(rsW, qo + k * 1024, 0, 16);
#pragma unroll
      for (int k = 0; k < 4; ++k) if (!okk[k]) {
        okk[k] = TAG_OK(v[k][0], v[k][1]) && TAG_OK(v[k][2], v[k][3]);
        *(LAS unsigned*)(L + L_Q + (lane + 64 * k) * 4) = (v[k][0] & 0xffffu) | (v[k][2] << 16);
      }
      if (__all(okk[0] && okk[1] && okk[2] && okk[3])) break;
      __builtin_amdgcn_s_sleep(1);
    }
    WAIT_LGKM0();
    if (lane == 0) lds_st(L, C_QREADY, 1);
  } else if (hw >= 4) {
    const int h4 = hw - 4;
    const __amdgpu_buffer_rsrc_t rsD = __builtin_amdgcn_make_buffer_rsrc((void*)wd, 0, FF * DM * 2, 0x00020000);
    const GAS unsigned short* sgu = (const GAS unsigned short*)((lane & 1) ? wu : wg) + (size_t)(128 * h4 + (lane >> 1)) * FF + (gam < 28 ? f0 : 0);
    const GAS unsigned short* swo = (const GAS unsigned short*)wo + (size_t)(128 * hd + 32 * h4 + (lane >> 3)) * DM + cbk * 64 + (lane & 7) * 8;
    u32x4 rg[4], ro[4], rd_[2], xk[8], xvv[8];
    {
      const __amdgpu_buffer_rsrc_t rK = __builtin_amdgcn_make_buffer_rsrc((void*)kc, 0, 128u << 20, 0x00020000), rV = __builtin_amdgcn_make_buffer_rsrc((void*)vc, 0, 128u << 20, 0x00020000);
      const unsigned so = kv_off + 2u * 32768u + h4 * 4096;
      const unsigned vk0 = g * 256 + ((i ^ g) << 4), vv0 = g * 256 + ((i ^ (2 * g)) << 4);
      lds_wait_ge<4>(L, C_RING, 4);
#pragma unroll
      for (int u = 0; u < 8; ++u) { if ((u & 3) == 0) WAIT_VMI(HCAP); xk[u] = __builtin_amdgcn_raw_buffer_load_b128(rK, (vk0 ^ ((u & 3) << 6)) + (u & 3) * 1024 + (u >> 2) * 16384, so, 2); }
#pragma unroll
      for (int u = 0; u < 8; ++u) { if ((u & 3) == 0) WAIT_VMI(HCAP); xvv[u] = __builtin_amdgcn_raw_buffer_load_b128(rV, (vv0 ^ ((u & 1) << 7)) + (u & 3) * 1024 + (u >> 2) * 16384, so, 2); }
      WAIT_VMI(HCAP);
    }
#pragma unroll
    for (int u = 0; u < 4; ++u) ro[u] = __builtin_nontemporal_load((const GAS u32x4*)(swo + (size_t)u * 8 * DM));
#pragma unroll
    for (int u = 0; u < 2; ++u) rd_[u] = __builtin_amdgcn_raw_buffer_load_b128(rsD, (unsigned)((56 * hd + 16 * h4 + 8 * u + (lane >> 3)) * 1024 + cbk * 128 + (lane & 7) * 16), 0, 2);
    lds_wait_ge<2>(L, C_XF + h4, 1);
#pragma unroll
    for (int u = 0; u < 8; ++u) *(LAS u32x4*)(L + L_RING + h4 * 32768 + u * 1024 + lane * 16) = xk[u];
    WAIT_LGKM0();
    if (lane == 0) lds_st(L, C_XR + h4, 1);
    lds_wait_ge<1>(L, C_XF + h4, 2);
#pragma unroll
    for (int u = 0; u < 8; ++u) *(LAS u32x4*)(L + L_RING + h4 * 32768 + 8192 + u * 1024 + lane * 16) = xvv[u];
    WAIT_LGKM0();
    if (lane == 0) lds_st(L, C_XR + h4, 2);
#pragma unroll
    for (int u = 0; u < 4; ++u) { rg[u] = *(const GAS u32x4*)(sgu + (size_t)u * 32 * FF); WAIT_VM(0); }
    lds_wait_ge(L, C_STGFREE, 1);
#pragma unroll
    for (int u = 0; u < 4; ++u) *(LAS u32x4*)(L + L_STG + (4 * h4 + u) * 1024 + lane * 16) = rg[u];
    WAIT_LGKM0();
    if (lane == 0) lds_add(L, C_GUST);
    lds_wait_ge<4>(L, C_HDONE, 1);
#pragma unroll
    for (int u = 0; u < 4; ++u) *(LAS u32x4*)(L + L_STG + (4 * h4 + u) * 1024 + lane * 16) = ro[u];
#pragma unroll
    for (int u = 0; u < 2; ++u) *(LAS u32x4*)(L + L_XN + (2 * h4 + u) * 1024 + lane * 16) = rd_[u];
    WAIT_LGKM0();
    if (lane == 0) lds_add(L, C_WOLD);
  }
  {
    const int fl = 7 * hw + (lane >> 3), bb = lane & 7;
    const unsigned ho = WS_HG + ((56 * hd + 7 * hw) * 8 + (lane < 56 ? lane : 0)) * 8;
    u32x2 hv = {0u, 0u};
    bool ok = false;
    lds_wait_ge<32>(L, C_PROG, GATE_H);
#pragma unroll 1
    for (unsigned spins = 0; spins < SPIN_CAP; ++spins) {
      asm volatile("" ::: "memory");
      if (!ok) { hv = __builtin_amdgcn_raw_buffer_load_b64(rsW, ho, 0, 16); ok = TAG_OK(hv[0], hv[1]); }
      if (__all(ok)) break;
      __builtin_amdgcn_s_sleep(SLP_MID);
    }
    if (lane < 56) *(LAS unsigned short*)(L + L_OA + bb * OA_STRIDE + fl * 2) = (unsigned short)(hv[0] & 0xffffu);
    if (lane < 4) *(LAS unsigned*)(L + L_OA + hw * OA_STRIDE + 112 + lane * 4) = 0u;
    WAIT_LGKM0();
    if (lane == 0) lds_add(L, C_OG);
  }
  const int bf = r >> 2, cb = 4 * xc + (r & 3), n1 = cb * 16;
  const unsigned ba = (g * 8 + (i >> 2)) * 128 + (lane & 3) * 8 + (hw & 3) * 32;
  const unsigned oa = L_OA + (i & 7) * OA_STRIDE + g * 16;
  bf16x8 wf[4];
  float y2a = 0.f, y2b = 0.f;
  float xr[2] = {0.f, 0.f};
  if (hw < 4) {
    lds_wait_ge<8>(L, C_WOLD, 4);
    lds_wait_ge<4>(L, C_OG, 8);
    f32x4 d = {0.f, 0.f, 0.f, 0.f};
#pragma unroll
    for (int s = 0; s < 2; ++s) {
      const bf16x4 a0 = __builtin_amdgcn_ds_read_tr16_b64_v4bf16((LAS bf16x4*)(L + L_XN + ba + s * 4096));
      const bf16x4 a1 = __builtin_amdgcn_ds_read_tr16_b64_v4bf16((LAS bf16x4*)(L + L_XN + ba + s * 4096 + 512));
      d = __builtin_amdgcn_mfma_f32_16x16x32_bf16(__builtin_shufflevector(a0, a1, 0, 1, 2, 3, 4, 5, 6, 7), *(LAS bf16x8*)(L + oa + s * 64), d, 0, 0, 0);
    }
    y2a = i < 8 ? d[0] : d[2]; y2b = i < 8 ? d[1] : d[3];
#pragma unroll
    for (int s = 0; s < 4; ++s) {
      const bf16x4 a0 = __builtin_amdgcn_ds_read_tr16_b64_v4bf16((LAS bf16x4*)(L + L_STG + ba + s * 4096));
      const bf16x4 a1 = __builtin_amdgcn_ds_read_tr16_b64_v4bf16((LAS bf16x4*)(L + L_STG + ba + s * 4096 + 512));
      wf[s] = __builtin_shufflevector(a0, a1, 0, 1, 2, 3, 4, 5, 6, 7);
    }
    asm volatile("s_waitcnt lgkmcnt(0)" : "+v"(wf[0]), "+v"(wf[1]), "+v"(wf[2]), "+v"(wf[3]), "+v"(y2a), "+v"(y2b) :: "memory");
    if (lane == 0) lds_add(L, C_WOREAD);
  } else if (hw == 6) {
    const unsigned xq = *(const GAS unsigned*)(x + bf * DM + n1 + (lane & 7) * 2);
    xr[0] = lo16(xq); xr[1] = hi16(xq);
  } else if (hw == 5 && w == 0) {
#pragma unroll 1
    for (unsigned spins = 0; spins < SPIN_CAP; ++spins) {
      bool ok = true;
#pragma unroll
      for (int k = 0; k < 4; ++k) { const u32x2 v = __builtin_amdgcn_raw_buffer_load_b64(rsW, WS_QG + (lane + 64 * k) * 128, 0, 16); ok &= TAG_OK(v[0], v[1]); }
      if (__all(ok)) break;
      __builtin_amdgcn_s_sleep(SLP_MID);
      asm volatile("" ::: "memory");
    }
    if (lane == 0) __hip_atomic_store((GAS u64*)(ws + WS_EPOCH), ((u64)Thi << 32) | Tlo, RLX_AGENT);
  }
  {
    lds_wait_ge<4>(L, C_WOREAD, 4);
    lds_wait_ge<16>(L, C_ATTEND, 1);
    const unsigned po = WS_PART + ((hw * 32 + hd) * 4 + g) * 512 + i * 16;
    const unsigned mo = WS_ML + ((hd * 8 + hw) * 4 + g) * 16;
    u32x4 pv0 = {0u, 0u, 0u, 0u}, pv1 = {0u, 0u, 0u, 0u}, ml = {0u, 0u, 0u, 0u};
    if (hw == 7) {
      bool ok = lane >= 32;
#pragma unroll 1
      for (unsigned spins = 0; spins < SPIN_CAP; ++spins) {
        asm volatile("" ::: "memory");
        if (!ok) {
          ml = __builtin_amdgcn_raw_buffer_load_b128(rsW, WS_ML + hd * 512 + lane * 16, 0, 16);
          ok = ml[1] == Tlo && ml[3] == Tlo;
        }
        const unsigned miss = ~(unsigned)__ballot(ok);
        if (lane == 0) lds_st(L, C_MLMASK, ~miss);
        const unsigned two = (miss & (miss >> 1) & 0x77777777u) | (miss & (miss >> 2) & 0x33333333u) | (miss & (miss >> 3) & 0x11111111u);
        if (two == 0u) break;
        __builtin_amdgcn_s_sleep(SLP_PART);
      }
      if (lane == 0) lds_st(L, C_MLMASK, 0xffffffffu);
    } else {
      while (__builtin_popcount((lds_ldu(L, C_MLMASK) >> (4 * hw)) & 15u) < 3) __builtin_amdgcn_s_sleep(1);
      asm volatile("" ::: "memory");
    }
    bool ok = false;
#pragma unroll 1
    for (unsigned spins = 0; spins < SPIN_CAP; ++spins) {
      asm volatile("" ::: "memory");
      if (!ok) {
        pv0 = __builtin_amdgcn_raw_buffer_load_b128(rsW, po, 0, 16);
        pv1 = __builtin_amdgcn_raw_buffer_load_b128(rsW, po + 256, 0, 16);
        ml = __builtin_amdgcn_raw_buffer_load_b128(rsW, mo, 0, 16);
        ok = (pv0[1] >> 8) == T24 && (pv0[3] >> 8) == T24 && (pv1[1] >> 8) == T24 && (pv1[3] >> 8) == T24 && ml[1] == Tlo && ml[3] == Tlo;
      }
      if (__all(ok)) break;
      __builtin_amdgcn_s_sleep(SLP_DIR);
    }
    const float mj = __uint_as_float(ml[0]);
    const float M = xrow16_max(mj);
    const float wt = __builtin_amdgcn_exp2f((mj - M) * 1.4426950408889634f);
    const float inv = 1.f / xrow16_sum(wt * __uint_as_float(ml[2]));
    const u32x2 oa0 = {cvtpk(xrow16_sum(wt * unpack_a(pv0[0])) * inv, xrow16_sum(wt * unpack_b(pv0[0], pv0[1])) * inv), cvtpk(xrow16_sum(wt * unpack_a(pv0[2])) * inv, xrow16_sum(wt * unpack_b(pv0[2], pv0[3])) * inv)};
    const u32x2 oa1 = {cvtpk(xrow16_sum(wt * unpack_a(pv1[0])) * inv, xrow16_sum(wt * unpack_b(pv1[0], pv1[1])) * inv), cvtpk(xrow16_sum(wt * unpack_a(pv1[2])) * inv, xrow16_sum(wt * unpack_b(pv1[2], pv1[3])) * inv)};
    if (g == 0) { *(LAS u32x2*)(L + L_OA + hw * OA_STRIDE + i * 8) = oa0; *(LAS u32x2*)(L + L_OA + hw * OA_STRIDE + 128 + i * 8) = oa1; }
    WAIT_LGKM0();
    if (lane == 0) lds_add(L, C_OG);
  }
  if (hw < 4) {
    lds_wait_ge(L, C_OG, 16);
    f32x4 d = {0.f, 0.f, 0.f, 0.f};
#pragma unroll
    for (int s = 0; s < 4; ++s) d = __builtin_amdgcn_mfma_f32_16x16x32_bf16(wf[s], *(LAS bf16x8*)(L + oa + s * 64), d, 0, 0, 0);
    const u32x2 g0 = pack2(i < 8 ? d[0] : d[2], y2a, T24), g1 = pack2(i < 8 ? d[1] : d[3], y2b, T24);
    const unsigned yo = ((((i & 7) * 32 + 4 * cbk + hw) * 32 + hd) * 16 + 4 * g + 2 * (i >> 3)) * 8;
    __builtin_amdgcn_raw_buffer_store_b128(u32x4{g0[0], g0[1], g1[0], g1[1]}, rsW, WS_Y + yo, 0, 16);
  } else if (hw == 6) {
    const unsigned io = WS_Y + (bf * 32 + cb) * 4096 + lane * 16;
    u32x4 y[4];
    bool okk[4] = {false, false, false, false};
#pragma unroll 1
    for (unsigned spins = 0; spins < SPIN_CAP; ++spins) {
      asm volatile("" ::: "memory");
#pragma unroll
      for (int k = 0; k < 4; ++k) if (!okk[k]) y[k] = __builtin_amdgcn_raw_buffer_load_b128(rsW, io + k * 1024, 0, 16);
#pragma unroll
      for (int k = 0; k < 4; ++k) okk[k] = (y[k][1] >> 8) == T24 && (y[k][3] >> 8) == T24;
      if (__all(okk[0] && okk[1] && okk[2] && okk[3])) break;
      __builtin_amdgcn_s_sleep(SLP_Y);
    }
    float o[2];
#pragma unroll
    for (int k = 0; k < 2; ++k) {
      float t = (unpack_a(y[0][2 * k]) + unpack_a(y[1][2 * k])) + (unpack_a(y[2][2 * k]) + unpack_a(y[3][2 * k]));
      float u = (unpack_b(y[0][2 * k], y[0][2 * k + 1]) + unpack_b(y[1][2 * k], y[1][2 * k + 1])) + (unpack_b(y[2][2 * k], y[2][2 * k + 1]) + unpack_b(y[3][2 * k], y[3][2 * k + 1]));
      t += dpp<XOR8>(t); t = rbf(xrow16_sum(t));
      u += dpp<XOR8>(u); u = rbf(xrow16_sum(u));
      const float gate = rbf(u / (1.f + __expf(-u)));
      o[k] = xr[k] + rbf(t * gate);
    }
    if (lane < 8) *(GAS unsigned*)(out + bf * DM + n1 + lane * 2) = cvtpk(o[0], o[1]);
  }
}

extern "C" void kernel_launch(void* const* d_in, const int* in_sizes, int n_in, void* d_out, int out_size, void* d_ws, size_t ws_size, hipStream_t stream) {
  const unsigned short* x = (const unsigned short*)d_in[0];
  const unsigned short* kc = (const unsigned short*)d_in[1];
  const unsigned short* vc = (const unsigned short*)d_in[2];
  const unsigned short* wq = (const unsigned short*)d_in[3];
  const unsigned short* wo = (const unsigned short*)d_in[4];
  const unsigned short* wg = (const unsigned short*)d_in[6];
  const unsigned short* wu = (const unsigned short*)d_in[7];
  const unsigned short* wd = (const unsigned short*)d_in[8];
  (void)hipFuncSetAttribute((const void*)fused_decode, hipFuncAttributeMaxDynamicSharedMemorySize, L_END);
  fused_decode<<<dim3(NWG), dim3(NTHR), L_END, stream>>>(x, kc, vc, wq, wo, wg, wu, wd, (unsigned short*)d_out, (unsigned char*)d_ws);
}
```
